```python
import torch

def _softmax(x, axis=-1):
    return torch.softmax(x, dim=axis)
def _gelu(x):
    return torch.nn.functional.gelu(x, approximate="tanh")
def _silu(x):
    return torch.nn.functional.silu(x)
def _sigmoid(x):
    return torch.sigmoid(x)
def _layernorm(x, axis=-1, eps=1e-5):
    m = x.mean(dim=axis, keepdim=True)
    v = x.var(dim=axis, keepdim=True, unbiased=False)
    return (x - m) / torch.sqrt(v + eps)
def _scatter_add(tgt, idx, src, axis=0):
    return tgt.clone().index_add_(axis, idx, src)
def _segment_sum(src, seg, n):
    out = torch.zeros((n,) + src.shape[1:], dtype=src.dtype)
    return out.index_add_(0, seg, src)
def _pad(x, pw):
    flat = tuple(v for pair in reversed(pw) for v in pair)
    return torch.nn.functional.pad(x, flat)

kernel_name = 'sequential_preLN__sliding_block__glu_gated_glu__prompt'

def setup_inputs():

    _w = (lambda a: (lambda t: t.to(torch.bfloat16) if t.is_floating_point() else t)(torch.as_tensor(a)))
    def _A_setup():
        B, S, D = 1, 2048, 576
        return {
            "x": _w((lambda *s: torch.randn(*s, dtype=torch.bfloat16) * 0.05)(B, S, D)),
            "wqkv": _w((lambda *s: torch.randn(*s, dtype=torch.bfloat16) * 0.05)(D, 3 * D)),
            "wo": _w((lambda *s: torch.randn(*s, dtype=torch.bfloat16) * 0.05)(D, D)),
        }
    _A_in = _A_setup()

    def _B_setup():
        B, S, D, F = 1, 2048, 576, 4608
        return {
            "x": _w((lambda *s: torch.randn(*s, dtype=torch.bfloat16) * 0.05)(B, S, D)),
            "w_gate": _w((lambda *s: torch.randn(*s, dtype=torch.bfloat16) * 0.05)(D, F)),
            "w_up": _w((lambda *s: torch.randn(*s, dtype=torch.bfloat16) * 0.05)(D, F)),
            "w_down": _w((lambda *s: torch.randn(*s, dtype=torch.bfloat16) * 0.05)(F, D)),
        }
    _B_in = _B_setup()

    return {
        "A_x": _A_in["x"],
        "A_wqkv": _A_in["wqkv"],
        "A_wo": _A_in["wo"],
        "B_x": _B_in["x"],
        "B_w_gate": _B_in["w_gate"],
        "B_w_up": _B_in["w_up"],
        "B_w_down": _B_in["w_down"],
    }

def reference(A_x, A_wqkv, A_wo, B_x, B_w_gate, B_w_up, B_w_down):
    def _A_ref(x, wqkv, wo):
        B, S, D = x.shape
        H = 32; Dh = D // H; W = 256
        qkv = (lambda x, *d: x.permute(*d))((x @ wqkv).reshape(B, S, 3, H, Dh), 2, 0, 3, 1, 4)
        q, k, v = qkv[0], qkv[1], qkv[2]
        scores = torch.einsum("bhqd,bhkd->bhqk", q, k) * (Dh ** -0.5)
        idx = torch.arange(S)
        mask = (idx[:, None] - idx[None, :] >= 0) & (idx[:, None] - idx[None, :] < W)
        scores = torch.where(mask[None, None], scores, -1e9)
        p = _softmax(scores, axis=-1)
        o = torch.einsum("bhqk,bhkd->bhqd", p, v)
        return (lambda x, *d: x.permute(*d))(o, 0, 2, 1, 3).reshape(B, S, D) @ wo

    def _B_ref(x, w_gate, w_up, w_down):
        g = torch.einsum("bsd,df->bsf", x, w_gate)
        u = torch.einsum("bsd,df->bsf", x, w_up)
        h = _sigmoid(g) * u
        return torch.einsum("bsf,fd->bsd", h, w_down)

    x = A_x
    h = x + _A_ref(_layernorm(x, axis=-1), A_wqkv, A_wo)
    h2 = h + _B_ref(_layernorm(h, axis=-1), B_w_gate, B_w_up, B_w_down)
    return h2
```

```cpp
#include <hip/hip_runtime.h>
#include <hip/hip_bf16.h>
#include <stdint.h>

typedef unsigned short u16;
typedef short bf16x8 __attribute__((ext_vector_type(8)));
typedef float f32x4 __attribute__((ext_vector_type(4)));

#define S_ 2048
#define D_ 576
#define H_ 32
#define DH_ 18
#define W_ 256
#define F_ 4608

__device__ __forceinline__ float bf2f(u16 x) { return __uint_as_float(((unsigned)x) << 16); }
__device__ __forceinline__ u16 f2bf(float f) { __hip_bfloat16 b = __float2bfloat16(f); return *(u16*)&b; }

__global__ void ln_kernel(const u16* __restrict__ x, u16* __restrict__ y) {
    int row = blockIdx.x * 4 + (threadIdx.x >> 6);
    int lane = threadIdx.x & 63;
    const u16* xr = x + (size_t)row * D_;
    float v[9];
    float s = 0.f;
    for (int i = 0; i < 9; ++i) { v[i] = bf2f(xr[lane + 64 * i]); s += v[i]; }
    for (int o = 32; o; o >>= 1) s += __shfl_xor(s, o);
    float m = s * (1.f / D_);
    float q = 0.f;
    for (int i = 0; i < 9; ++i) { float d = v[i] - m; q += d * d; }
    for (int o = 32; o; o >>= 1) q += __shfl_xor(q, o);
    float r = rsqrtf(q * (1.f / D_) + 1e-5f);
    for (int i = 0; i < 9; ++i) y[(size_t)row * D_ + lane + 64 * i] = f2bf((v[i] - m) * r);
}

template <int MODE>
__global__ __launch_bounds__(256) void gemm_kernel(const u16* __restrict__ A, const u16* __restrict__ B, const u16* __restrict__ B2,
                                                   const u16* __restrict__ res, u16* __restrict__ C, int M, int N, int K) {
    __shared__ u16 As[64][40];
    __shared__ u16 Bs[64][40];
    __shared__ u16 Bs2[64][40];
    int tid = threadIdx.x, lane = tid & 63, w = tid >> 6;
    int m0 = blockIdx.y * 64, n0 = blockIdx.x * 64;
    int wm = (w >> 1) * 32, wn = (w & 1) * 32;
    f32x4 acc[2][2] = {}, acc2[2][2] = {};
    for (int k0 = 0; k0 < K; k0 += 32) {
        {
            int r = tid >> 2, c = (tid & 3) * 8;
            *(bf16x8*)&As[r][c] = *(const bf16x8*)&A[(size_t)(m0 + r) * K + k0 + c];
            int kr = tid >> 3, nc = (tid & 7) * 8;
            bf16x8 b = *(const bf16x8*)&B[(size_t)(k0 + kr) * N + n0 + nc];
            for (int j = 0; j < 8; ++j) Bs[nc + j][kr] = b[j];
            if (MODE == 2) {
                bf16x8 b2 = *(const bf16x8*)&B2[(size_t)(k0 + kr) * N + n0 + nc];
                for (int j = 0; j < 8; ++j) Bs2[nc + j][kr] = b2[j];
            }
        }
        __syncthreads();
        bf16x8 a[2], b[2], b2[2];
        for (int i = 0; i < 2; ++i) {
            a[i] = *(const bf16x8*)&As[wm + i * 16 + (lane & 15)][(lane >> 4) * 8];
            b[i] = *(const bf16x8*)&Bs[wn + i * 16 + (lane & 15)][(lane >> 4) * 8];
            if (MODE == 2) b2[i] = *(const bf16x8*)&Bs2[wn + i * 16 + (lane & 15)][(lane >> 4) * 8];
        }
        for (int i = 0; i < 2; ++i)
            for (int j = 0; j < 2; ++j) {
                acc[i][j] = __builtin_amdgcn_mfma_f32_16x16x32_bf16(a[i], b[j], acc[i][j], 0, 0, 0);
                if (MODE == 2) acc2[i][j] = __builtin_amdgcn_mfma_f32_16x16x32_bf16(a[i], b2[j], acc2[i][j], 0, 0, 0);
            }
        __syncthreads();
    }
    for (int i = 0; i < 2; ++i)
        for (int j = 0; j < 2; ++j)
            for (int r = 0; r < 4; ++r) {
                int row = m0 + wm + i * 16 + (lane >> 4) * 4 + r;
                int col = n0 + wn + j * 16 + (lane & 15);
                float v = acc[i][j][r];
                if (MODE == 1) v = bf2f(f2bf(v)) + bf2f(res[(size_t)row * N + col]);
                if (MODE == 2) { float g = bf2f(f2bf(v)), u = bf2f(f2bf(acc2[i][j][r])); v = bf2f(f2bf(1.f / (1.f + __expf(-g)))) * u; }
                C[(size_t)row * N + col] = f2bf(v);
            }
}

__global__ void attn_kernel(const u16* __restrict__ qkv, u16* __restrict__ o) {
    int q = blockIdx.x * 64 + (threadIdx.x & 63);
    int h = blockIdx.y * 4 + (threadIdx.x >> 6);
    float qv[DH_];
    for (int d = 0; d < DH_; ++d) qv[d] = bf2f(qkv[(size_t)q * 3 * D_ + h * DH_ + d]);
    const float scale = 0.23570226039551584f;
    float m = -1e30f, l = 0.f, acc[DH_];
    for (int d = 0; d < DH_; ++d) acc[d] = 0.f;
    int lo = q - W_ + 1; if (lo < 0) lo = 0;
    for (int k = lo; k <= q; ++k) {
        const u16* kp = qkv + (size_t)k * 3 * D_ + D_ + h * DH_;
        const u16* vp = qkv + (size_t)k * 3 * D_ + 2 * D_ + h * DH_;
        float s = 0.f;
        for (int d = 0; d < DH_; ++d) s += qv[d] * bf2f(kp[d]);
        s *= scale;
        float mn = fmaxf(m, s);
        float al = __expf(m - mn), p = __expf(s - mn);
        l = l * al + p;
        for (int d = 0; d < DH_; ++d) acc[d] = acc[d] * al + p * bf2f(vp[d]);
        m = mn;
    }
    float il = 1.f / l;
    for (int d = 0; d < DH_; ++d) o[(size_t)q * D_ + h * DH_ + d] = f2bf(acc[d] * il);
}

extern "C" void kernel_launch(void* const* d_in, const int* in_sizes, int n_in,
                              void* d_out, int out_size, void* d_ws, size_t ws_size,
                              hipStream_t stream) {
    const u16* x = (const u16*)d_in[0];
    const u16* wqkv = (const u16*)d_in[1];
    const u16* wo = (const u16*)d_in[2];
    const u16* wg = (const u16*)d_in[4];
    const u16* wu = (const u16*)d_in[5];
    const u16* wd = (const u16*)d_in[6];
    u16* out = (u16*)d_out;
    char* ws = (char*)d_ws;
    u16* ln = (u16*)(ws);
    u16* qkv = (u16*)(ws + (4 << 20));
    u16* ao = (u16*)(ws + (16 << 20));
    u16* h = (u16*)(ws + (20 << 20));
    u16* hh = (u16*)(ws + (24 << 20));
    ln_kernel<<<S_ / 4, 256, 0, stream>>>(x, ln);
    gemm_kernel<0><<<dim3(3 * D_ / 64, S_ / 64), 256, 0, stream>>>(ln, wqkv, nullptr, nullptr, qkv, S_, 3 * D_, D_);
    attn_kernel<<<dim3(S_ / 64, H_ / 4), 256, 0, stream>>>(qkv, ao);
    gemm_kernel<1><<<dim3(D_ / 64, S_ / 64), 256, 0, stream>>>(ao, wo, nullptr, x, h, S_, D_, D_);
    ln_kernel<<<S_ / 4, 256, 0, stream>>>(h, ln);
    gemm_kernel<2><<<dim3(F_ / 64, S_ / 64), 256, 0, stream>>>(ln, wg, wu, nullptr, hh, S_, F_, D_);
    gemm_kernel<1><<<dim3(D_ / 64, S_ / 64), 256, 0, stream>>>(hh, wd, nullptr, h, out, S_, D_, F_);
}
```

```cpp
#include <hip/hip_runtime.h>
#include <stdint.h>

typedef unsigned short u16;
typedef unsigned u32;
typedef unsigned long long u64;
typedef float f32x2 __attribute__((ext_vector_type(2)));
typedef unsigned u32x2 __attribute__((ext_vector_type(2)));
typedef unsigned u32x4 __attribute__((ext_vector_type(4)));
typedef __bf16 bf16x2_t __attribute__((ext_vector_type(2)));
#define DI __device__ __forceinline__

constexpr int D = 576;
constexpr size_t MB = 1u << 20;
constexpr size_t KB = 1024;
constexpr size_t OFF_QH = 0, OFF_KH = 2 * MB, OFF_QL = 4 * MB, OFF_KL = 4 * MB + 256 * KB, OFF_VT = 4 * MB + 512 * KB,
                 OFF_O32 = 7 * MB, OFF_H32 = 9 * MB + 512 * KB, OFF_MEAN1 = 11 * MB + 768 * KB, OFF_MEAN2 = OFF_MEAN1 + 8192, OFF_SSQ = OFF_MEAN1 + 16384,
                 OFF_WB = OFF_MEAN1 + 65536, OFF_FLAG = OFF_MEAN1 + 192 * KB, OFF_WOT = 12 * MB, OFF_W1T = 12 * MB + 640 * KB, OFF_JUNK = 13 * MB + 800 * KB,
                 OFF_W2T = 14 * MB, OFF_HH = 19 * MB + 128 * KB, OFF_PART = 37 * MB + 256 * KB, OFF_PART8 = OFF_PART + 6 * MB;
constexpr float QSCALE = 0.23570226039551584f * 1.4426950408889634f;

DI u32 rfl(u32 x) { return __builtin_amdgcn_readfirstlane(x); }
DI u64 rfl64(u64 x) { return (u64)rfl((u32)x) | ((u64)rfl((u32)(x >> 32)) << 32); }
DI float bflo(u32 x) { return __uint_as_float(x << 16); }
DI float bfhi(u32 x) { return __uint_as_float(x & 0xffff0000u); }
DI u32 pk(float lo, float hi) { f32x2 v = {lo, hi}; bf16x2_t b = __builtin_convertvector(v, bf16x2_t); return __builtin_bit_cast(u32, b); }

#define LDSP(T, a) ((__attribute__((address_space(3))) T*)(size_t)(a))
struct Job { const char* src; u32 ld, sg, st, se, s2, da, db, d8, sa, rot, nat, to; };
DI Job k1_job(u32 n, const u16* wo, const u16* wg, const u16* wu, const u16* wd) {
    Job J; J.s2 = 2; J.to = 256;
    if (n < 576) {
        const u32 nu = n & 3, m = n >> 2, q = (m * 3641) >> 16, s = m - 18 * q, kh = q & 1, j4 = q >> 1, kap = (s * 11) >> 5, k = s - 3 * kap;
        const u32 ui = s >> 1, cp = 2 * kh + (s & 1), u = ui < 4 ? ui : ui - 1;
        const u32 rb = 1152 * j4 + (ui == 4 ? 1024 + 32 * cp : 256 * cp + 16 * u);
        J.src = (const char*)wd + rb * 1152 + 288 * nu; J.ld = 1152; J.sg = ui == 4 ? 16 : 128; J.st = 4; J.se = 8;
        const u32 base = (u32)OFF_W2T + (nu * 4 + j4) * 331776 + (2 * k + kh) * 1024;
        J.da = base + kap * 43008; J.db = base + 252 * 1024 + kap * 12288; J.sa = 6144; J.rot = j4 + 1; J.nat = 0; J.d8 = J.da + 36864;
    } else if (n < 648) {
        const u32 nu = n & 3, s = (n - 576) >> 2, par = s >= 9, e = s - 9 * par;
        J.src = (const char*)wo + (e < 8 ? 72 * e + 20 * par : 16 + 2 * par) * 1152 + 288 * nu; J.ld = 1152;
        J.sg = e < 8 ? 36 : 288; J.st = e < 8 ? 8 : 72; J.se = e < 8 ? 4 : 144; J.s2 = e < 8 ? 2 : 36;
        J.da = (u32)OFF_WOT + nu * 147456 + s * 8192; J.db = J.da + 6144; J.sa = 1024; J.rot = 0; J.nat = 1; J.d8 = (u32)OFF_WB + nu * 21504 + s * 1152;
    } else {
        const u32 m = n - 648, j4 = m & 3, gu = (m >> 2) & 1, s = m >> 3, kap = (s * 11) >> 5, k = s - 3 * kap;
        const u32 rb = s < 16 ? 144 * (s >> 2) + 32 * (s & 3) : 288 * (s - 16) + 128;
        J.src = (const char*)(gu ? wu : wg) + rb * 9216 + (1152 * j4 + 1024) * 2; J.ld = 9216; J.sg = s < 16 ? 16 : 144; J.st = 4; J.se = 8; J.to = 0;
        J.da = (u32)OFF_W1T + j4 * 8 * 36864 + ((kap * 2 + gu) * 3 + k) * 1024; J.sa = 36864; J.db = J.da + 6 * 36864; J.rot = 0; J.nat = 1; J.d8 = (u32)OFF_JUNK + m * 1024;
    }
    return J;
}
DI void job_load(const Job& J, u32 lds, u32 lane) {
    const auto rs = __builtin_amdgcn_make_buffer_rsrc((void*)J.src, 0, 560 * J.ld, 0x00020000);
    const u32 v0 = ((lane >> 5) * J.sg + ((lane >> 4) & 1) * J.st) * J.ld;
    const u32 vm = v0 + (lane & 15) * 16, vt = v0 + (((lane >> 3) & 1) * J.se + ((lane >> 2) & 1) * J.s2 + ((lane >> 1) & 1)) * J.ld + (lane & 1) * 16;
#pragma unroll
    for (u32 e = 0; e < 8; ++e) __builtin_amdgcn_raw_ptr_buffer_load_lds(rs, LDSP(void, lds + e * 1056), 16, vm, ((e >> 2) * J.se + ((e >> 1) & 1) * J.s2 + (e & 1)) * J.ld, 0, 0);
    __builtin_amdgcn_raw_ptr_buffer_load_lds(rs, LDSP(void, lds + 8448), 16, vt, J.to, 0, 0);
}
DI void job_store(const Job& J, __amdgpu_buffer_rsrc_t rw, u32 lds, u32 lane) {
    const u32 e = (lane >> 2) & 7, p = lane & 3, b0 = lds + (lane >> 5) * 512;
    const u32 am = b0 + e * 1056 + p * (J.nat == 1 ? 8 : 16), at = b0 + 8448 + e * 32 + p * 8, odd = J.nat == 1 ? 32 : 8;
    u32 ad[9];
#pragma unroll
    for (u32 c = 0; c < 9; ++c) ad[c] = c < 8 ? am + ((((c >> 1) + J.rot) & 3) << 6) + (c & 1) * odd : at;
    u32x2 r[18];
    asm volatile("ds_read_b64_tr_b16 %0, %18\n ds_read_b64_tr_b16 %1, %18 offset:256\n"
                 "ds_read_b64_tr_b16 %2, %19\n ds_read_b64_tr_b16 %3, %19 offset:256\n"
                 "ds_read_b64_tr_b16 %4, %20\n ds_read_b64_tr_b16 %5, %20 offset:256\n"
                 "ds_read_b64_tr_b16 %6, %21\n ds_read_b64_tr_b16 %7, %21 offset:256\n"
                 "ds_read_b64_tr_b16 %8, %22\n ds_read_b64_tr_b16 %9, %22 offset:256\n"
                 "ds_read_b64_tr_b16 %10, %23\n ds_read_b64_tr_b16 %11, %23 offset:256\n"
                 "ds_read_b64_tr_b16 %12, %24\n ds_read_b64_tr_b16 %13, %24 offset:256\n"
                 "ds_read_b64_tr_b16 %14, %25\n ds_read_b64_tr_b16 %15, %25 offset:256\n"
                 "ds_read_b64_tr_b16 %16, %26\n ds_read_b64_tr_b16 %17, %26 offset:256\n"
                 "s_waitcnt lgkmcnt(0)"
                 : "=&v"(r[0]), "=&v"(r[1]), "=&v"(r[2]), "=&v"(r[3]), "=&v"(r[4]), "=&v"(r[5]), "=&v"(r[6]), "=&v"(r[7]), "=&v"(r[8]), "=&v"(r[9]), "=&v"(r[10]), "=&v"(r[11]), "=&v"(r[12]), "=&v"(r[13]), "=&v"(r[14]), "=&v"(r[15]), "=&v"(r[16]), "=&v"(r[17])
                 : "v"(ad[0]), "v"(ad[1]), "v"(ad[2]), "v"(ad[3]), "v"(ad[4]), "v"(ad[5]), "v"(ad[6]), "v"(ad[7]), "v"(ad[8]) : "memory");
#pragma unroll
    for (u32 c = 0; c < 9; ++c) {
        const u32 d = c < 6 ? J.da + c * J.sa : c < 8 ? J.db + (c - 6) * J.sa : J.d8;
        __builtin_amdgcn_raw_buffer_store_b128((u32x4){r[2 * c][0], r[2 * c][1], r[2 * c + 1][0], r[2 * c + 1][1]}, rw, lane * 16, d, 16);
    }
}
DI void wbar_job(int Jw, u32 lds, char* ws, int lane) {
    const int k = 8 * Jw + (lane >> 3);
    float sum = 0.f;
#pragma unroll
    for (int i = 0; i < 9; ++i) { const u32x4 v = *LDSP(u32x4, lds + i * 1024 + lane * 16); for (int e = 0; e < 4; ++e) sum += bflo(v[e]) + bfhi(v[e]); }
    sum += __shfl_xor(sum, 1); sum += __shfl_xor(sum, 2); sum += __shfl_xor(sum, 4);
    sum *= (1.f / D);
    const u32 hi = pk(sum, 0.f) & 0xffff, lo = pk(sum - bflo(hi), 0.f) & 0xffff;
    const int head = k / 18, d0 = k % 18, d = (head & 1) ? (d0 + 16) % 18 : d0;
    const int hh = head >> 1;
    int st, g, j;
    if (d < 16) { st = hh >> 1; g = 2 * (hh & 1) + ((d >> 2) & 1); j = (d & 3) + 4 * (d >> 3); } else { st = 8; g = hh >> 2; j = 2 * (hh & 3) + (d - 16); }
    st += 9 * (head & 1);
    u16* wb = (u16*)(ws + OFF_WB + (lane & 3) * 21504 + st * 1152 + 1024) + g * 16 + j;
    if ((lane & 7) < 4) { wb[0] = (u16)hi; wb[8] = (u16)lo; }
}

__global__ __launch_bounds__(256, 1) void k1_kernel(const u16* __restrict__ x, const u16* __restrict__ wqkv, const u16* __restrict__ wo, const u16* __restrict__ wg,
                                                    const u16* __restrict__ wu, const u16* __restrict__ wd, char* ws) {
    asm volatile("k1_code_begin:\n s_getpc_b64 s[36:37]\n s_cmp_ge_u32 %1, 32\n s_cbranch_scc1 .Lc%=\n v_lshlrev_b32 v255, 7, %0\n s_and_b32 s38, %1, 8\n s_lshl_b32 s38, s38, 12\n v_add_u32 v255, s38, v255\n"
                 "v_min_u32 v255, ffn_code_begin-k1_code_begin-64, v255\n global_load_dword v255, v255, s[36:37]\n.Lc%=:"
                 :: "v"(threadIdx.x), "s"(blockIdx.x) : "s36", "s37", "s38", "scc", "v255", "memory");
    const u32 w = __builtin_amdgcn_readfirstlane(threadIdx.x >> 6);
    const u32 b = blockIdx.x;
    const u32 pfx = (1024 * (b & 1) + 256 * ((b >> 1) & 3) + (b & ~7u)) * 1152, pfw = (128 + 14 * (b >> 3) + (b & 1)) * 3456 + ((b >> 1) & 3) * 288;
    if (__builtin_expect(b < 192, 1)) {
        const u32 xcd = b & 7, idx = b >> 3, mt = 8 * (xcd & 1) + (idx & 7), which = idx >> 3, hg = xcd >> 1, nt = 4 * which + hg;
        const u32 row0 = 128 * mt + 32 * w;
        const u32 omain = which == 2 ? (u32)OFF_VT + (8 * hg * 128 + (row0 >> 4)) * 608 : (u32)OFF_QH + which * (u32)(OFF_KH - OFF_QH) + (8 * hg * 2048 + row0) * 32;
        const u32 oleft = (u32)OFF_QL + (which & 1) * (u32)(OFF_KL - OFF_QL) + (4 * hg * 2048 + row0) * 8;
        const u32 scale = which == 0 ? __builtin_bit_cast(u32, QSCALE) : 0x3f800000u;
        asm volatile(
    "qkv_code_begin:\n"
    "s_nop 4\n"
    "s_mov_b32 s40, %2\n"
    "s_and_b32 s41, %3, 0xffff\n"
    "s_mov_b32 s42, 2359296\n"
    "s_mov_b32 s43, 0x20000\n"
    "s_mov_b32 s44, %4\n"
    "s_and_b32 s45, %5, 0xffff\n"
    "s_mov_b32 s46, 1990656\n"
    "s_mov_b32 s47, 0x20000\n"
    "v_mbcnt_lo_u32_b32 v3, -1, 0\n"
    "v_mbcnt_hi_u32_b32 v3, -1, v3\n"
    "v_lshrrev_b32 v0, 3, v3\n"
    "v_and_b32 v1, 7, v3\n"
    "v_xor_b32 v1, v0, v1\n"
    "v_mul_u32_u24 v243, 1152, v0\n"
    "v_lshl_add_u32 v243, v1, 4, v243\n"
    "v_lshrrev_b32 v0, 5, v3\n"
    "v_bfe_u32 v1, v3, 4, 1\n"
    "v_lshl_add_u32 v0, v0, 2, v1\n"
    "v_mul_u32_u24 v246, 13824, v0\n"
    "v_and_b32 v1, 15, v3\n"
    "v_lshl_add_u32 v246, v1, 4, v246\n"
    "v_bfe_u32 v0, v3, 3, 1\n"
    "v_bfe_u32 v1, v3, 4, 1\n"
    "v_lshl_add_u32 v0, v0, 1, v1\n"
    "v_bfe_u32 v1, v3, 1, 2\n"
    "v_lshl_add_u32 v0, v0, 2, v1\n"
    "v_mul_u32_u24 v247, 3456, v0\n"
    "v_and_b32 v1, 1, v3\n"
    "v_lshl_add_u32 v247, v1, 4, v247\n"
    "s_mul_i32 s52, %6, 2112\n"
    "s_lshr_b32 s53, %6, 1\n"
    "s_mul_i32 s55, s53, 9472\n"
    "s_and_b32 s53, %6, 1\n"
    "s_lshl_b32 s53, s53, 9\n"
    "s_add_u32 s55, s55, s53\n"
    "s_lshl_b32 s56, %6, 12\n"
    "s_add_u32 m0, s52, 0\n"
    "s_add_u32 s48, %8, 0\n"
    "buffer_load_dwordx4 v246, s[44:47], s48 offen lds\n"
    "s_add_u32 m0, s56, 18944\n"
    "s_add_u32 s49, %7, 0\n"
    "buffer_load_dwordx4 v243, s[40:43], s49 offen lds\n"
    "s_add_u32 m0, s52, 1056\n"
    "s_add_u32 s50, %8, 3456\n"
    "buffer_load_dwordx4 v246, s[44:47], s50 offen lds\n"
    "s_add_u32 m0, s56, 19968\n"
    "s_add_u32 s51, %7, 9216\n"
    "buffer_load_dwordx4 v243, s[40:43], s51 offen lds\n"
    "s_add_u32 m0, s52, 9472\n"
    "s_add_u32 s48, %8, 110592\n"
    "buffer_load_dwordx4 v246, s[44:47], s48 offen lds\n"
    "s_add_u32 m0, s56, 20992\n"
    "s_add_u32 s49, %7, 18432\n"
    "buffer_load_dwordx4 v243, s[40:43], s49 offen lds\n"
    "s_add_u32 m0, s52, 10528\n"
    "s_add_u32 s50, %8, 114048\n"
    "buffer_load_dwordx4 v246, s[44:47], s50 offen lds\n"
    "s_add_u32 m0, s56, 22016\n"
    "s_add_u32 s51, %7, 27648\n"
    "buffer_load_dwordx4 v243, s[40:43], s51 offen lds\n"
    "s_add_u32 m0, s55, 8448\n"
    "s_add_u32 s48, %9, 0\n"
    "s_mov_b32 exec_hi, 0\n"
    "buffer_load_dwordx4 v247, s[44:47], s48 offen lds\n"
    "s_mov_b32 exec_hi, -1\n"
    "s_add_u32 m0, s52, 35328\n"
    "s_add_u32 s49, %8, 221184\n"
    "buffer_load_dwordx4 v246, s[44:47], s49 offen lds\n"
    "s_add_u32 m0, s56, 54272\n"
    "s_add_u32 s50, %7, 128\n"
    "buffer_load_dwordx4 v243, s[40:43], s50 offen lds\n"
    "s_add_u32 m0, s52, 36384\n"
    "s_add_u32 s51, %8, 224640\n"
    "buffer_load_dwordx4 v246, s[44:47], s51 offen lds\n"
    "s_add_u32 m0, s56, 55296\n"
    "s_add_u32 s48, %7, 9344\n"
    "buffer_load_dwordx4 v243, s[40:43], s48 offen lds\n"
    "s_add_u32 m0, s52, 44800\n"
    "s_add_u32 s49, %8, 331776\n"
    "buffer_load_dwordx4 v246, s[44:47], s49 offen lds\n"
    "s_add_u32 m0, s56, 56320\n"
    "s_add_u32 s50, %7, 18560\n"
    "buffer_load_dwordx4 v243, s[40:43], s50 offen lds\n"
    "s_add_u32 m0, s52, 45856\n"
    "s_add_u32 s51, %8, 335232\n"
    "buffer_load_dwordx4 v246, s[44:47], s51 offen lds\n"
    "s_add_u32 m0, s56, 57344\n"
    "s_add_u32 s48, %7, 27776\n"
    "buffer_load_dwordx4 v243, s[40:43], s48 offen lds\n"
    "s_add_u32 m0, s55, 43776\n"
    "s_add_u32 s49, %9, 221184\n"
    "s_mov_b32 exec_hi, 0\n"
    "buffer_load_dwordx4 v247, s[44:47], s49 offen lds\n"
    "s_mov_b32 exec_hi, -1\n"
    "s_lshr_b32 s53, %6, 1\n"
    "s_lshl_b32 s53, s53, 5\n"
    "v_and_b32 v2, 31, v3\n"
    "v_add_u32 v2, s53, v2\n"
    "v_lshrrev_b32 v0, 3, v2\n"
    "v_lshlrev_b32 v1, 7, v2\n"
    "v_lshl_add_u32 v0, v0, 7, v1\n"
    "v_min_u32 v2, 62, v2\n"
    "v_mul_u32_u24 v1, 57, v2\n"
    "v_lshrrev_b32 v1, 9, v1\n"
    "v_mul_u32_u24 v4, 43, v2\n"
    "v_lshrrev_b32 v4, 7, v4\n"
    "v_mul_u32_u24 v1, 3456, v1\n"
    "v_mul_u32_u24 v4, 768, v4\n"
    "v_lshl_add_u32 v1, v2, 7, v1\n"
    "v_add_u32 v1, v1, v4\n"
    "s_mov_b32 s53, %18\n"
    "s_mov_b32 s54, %21\n"
    "s_mov_b32 exec_hi, 0\n"
    "s_bitcmp0_b32 %6, 0\n"
    "s_cbranch_scc1 .Lex_%=\n"
    "buffer_load_dword v244, v1, s[44:47], s54 offen\n"
    "s_branch .Lep_%=\n"
    ".Lex_%=:\n"
    "buffer_load_dword v244, v0, s[40:43], s53 offen offset:128\n"
    ".Lep_%=:\n"
    "s_mov_b32 exec_hi, -1\n"
    "v_lshrrev_b32 v0, 3, v3\n"
    "v_lshlrev_b32 v0, 13, v0\n"
    "v_lshl_add_u32 v245, v3, 7, v0\n"
    "s_mov_b32 s36, %0\n"
    "s_and_b32 s37, %1, 0xffff\n"
    "s_mov_b32 s38, 134217728\n"
    "s_mov_b32 s39, 0x20000\n"
    "v_bfe_u32 v0, v3, 2, 3\n"
    "v_lshrrev_b32 v1, 5, v3\n"
    "v_lshlrev_b32 v1, 9, v1\n"
    "v_and_b32 v2, 3, v3\n"
    "v_mul_u32_u24 v4, 1056, v0\n"
    "v_add_u32 v4, v1, v4\n"
    "v_mul_u32_u24 v240, 72, v2\n"
    "v_add3_u32 v240, v240, v4, 32\n"
    "v_lshl_add_u32 v4, v2, 3, v4\n"
    "v_lshl_add_u32 v1, v0, 5, v1\n"
    "v_lshl_add_u32 v1, v2, 3, v1\n"
    "v_add_u32 v1, 8448, v1\n"
    "s_xor_b32 s53, %6, 0\n"
    "s_mul_i32 s54, s53, 72\n"
    "v_add_u32 v232, s54, v4\n"
    "v_add_u32 v233, 40, v232\n"
    "s_cmp_eq_u32 s53, 3\n"
    "s_cselect_b64 vcc, -1, 0\n"
    "v_cndmask_b32 v233, v233, v1, vcc\n"
    "s_xor_b32 s53, %6, 1\n"
    "s_mul_i32 s54, s53, 72\n"
    "v_add_u32 v234, s54, v4\n"
    "v_add_u32 v235, 40, v234\n"
    "s_cmp_eq_u32 s53, 3\n"
    "s_cselect_b64 vcc, -1, 0\n"
    "v_cndmask_b32 v235, v235, v1, vcc\n"
    "s_xor_b32 s53, %6, 2\n"
    "s_mul_i32 s54, s53, 72\n"
    "v_add_u32 v236, s54, v4\n"
    "v_add_u32 v237, 40, v236\n"
    "s_cmp_eq_u32 s53, 3\n"
    "s_cselect_b64 vcc, -1, 0\n"
    "v_cndmask_b32 v237, v237, v1, vcc\n"
    "s_xor_b32 s53, %6, 3\n"
    "s_mul_i32 s54, s53, 72\n"
    "v_add_u32 v238, s54, v4\n"
    "v_add_u32 v239, 40, v238\n"
    "s_cmp_eq_u32 s53, 3\n"
    "s_cselect_b64 vcc, -1, 0\n"
    "v_cndmask_b32 v239, v239, v1, vcc\n"
    "v_and_b32 v0, 15, v3\n"
    "v_and_b32 v1, 7, v3\n"
    "v_lshrrev_b32 v2, 4, v3\n"
    "v_xor_b32 v1, v1, v2\n"
    "v_lshlrev_b32 v1, 4, v1\n"
    "v_lshl_add_u32 v1, v0, 7, v1\n"
    "v_xor_b32 v2, 64, v1\n"
    "s_add_u32 s53, s56, 18944\n"
    "v_add_u32 v241, s53, v1\n"
    "v_add_u32 v242, s53, v2\n"
    "v_mov_b32 v92, 0x3f803f80\n"
    "v_mov_b32 v93, 0x3f803f80\n"
    "v_mov_b32 v94, 0x3f803f80\n"
    "v_mov_b32 v95, 0x3f803f80\n"
    "s_cmp_lg_u32 %10, 0\n"
    "s_cbranch_scc1 .Lv_%=\n"
    "v_and_b32 v5, 15, v3\n"
    "v_lshrrev_b32 v6, 4, v3\n"
    "s_lshl_b32 s53, %6, 7\n"
    "s_add_u32 s53, s53, 143360\n"
    "v_lshl_add_u32 v8, v6, 4, s53\n"
    "v_mov_b32 v76, v8\n"
    "v_xor_b32 v77, 128, v76\n"
    "v_xor_b32 v78, 256, v76\n"
    "v_xor_b32 v79, 384, v76\n"
    "s_mov_b32 s53, 143360\n"
    "v_lshl_add_u32 v9, v6, 4, s53\n"
    "v_lshl_add_u32 v10, v5, 2, s53\n"
    "s_lshl_b32 s54, %6, 8\n"
    "s_add_u32 s54, s54, 144384\n"
    "v_lshl_add_u32 v11, v5, 2, s54\n"
    "v_lshl_add_u32 v12, v6, 4, s54\n"
    "s_lshl_b32 s54, %6, 1\n"
    "s_xor_b32 s72, s54, 0\n"
    "s_mul_i32 s72, s72, 0x10000\n"
    "s_add_u32 s72, s72, %11\n"
    "s_xor_b32 s73, s54, 1\n"
    "s_mul_i32 s73, s73, 0x10000\n"
    "s_add_u32 s73, s73, %11\n"
    "s_xor_b32 s74, s54, 2\n"
    "s_mul_i32 s74, s74, 0x10000\n"
    "s_add_u32 s74, s74, %11\n"
    "s_xor_b32 s75, s54, 3\n"
    "s_mul_i32 s75, s75, 0x10000\n"
    "s_add_u32 s75, s75, %11\n"
    "s_xor_b32 s76, s54, 4\n"
    "s_mul_i32 s76, s76, 0x10000\n"
    "s_add_u32 s76, s76, %11\n"
    "s_xor_b32 s77, s54, 5\n"
    "s_mul_i32 s77, s77, 0x10000\n"
    "s_add_u32 s77, s77, %11\n"
    "s_xor_b32 s78, s54, 6\n"
    "s_mul_i32 s78, s78, 0x10000\n"
    "s_add_u32 s78, s78, %11\n"
    "s_xor_b32 s79, s54, 7\n"
    "s_mul_i32 s79, s79, 0x10000\n"
    "s_add_u32 s79, s79, %11\n"
    "v_lshlrev_b32 v13, 3, v6\n"
    "v_lshl_add_u32 v13, v5, 5, v13\n"
    "v_lshlrev_b32 v14, 14, v6\n"
    "v_lshl_add_u32 v14, v5, 3, v14\n"
    "v_lshlrev_b32 v15, 2, v5\n"
    "v_add_u32 v60, 70656, v232\n"
    "v_add_u32 v61, 70656, v233\n"
    "v_add_u32 v62, 70656, v234\n"
    "v_add_u32 v63, 70656, v235\n"
    "v_add_u32 v64, 70656, v236\n"
    "v_add_u32 v65, 70656, v237\n"
    "v_add_u32 v66, 70656, v238\n"
    "v_add_u32 v67, 70656, v239\n"
    "v_add_u32 v68, 70656, v240\n"
    "v_add_u32 v69, 70656, v241\n"
    "v_add_u32 v70, 70656, v242\n"
    "s_waitcnt vmcnt(10)\n"
    "s_barrier\n"
    "ds_read_b128 v[96:99], v241 offset:0\n"
    "ds_read_b128 v[100:103], v241 offset:2048\n"
    "ds_read_b64_tr_b16 v[112:113], v232 offset:0\n"
    "ds_read_b64_tr_b16 v[114:115], v232 offset:256\n"
    "ds_read_b64_tr_b16 v[116:117], v233 offset:0\n"
    "ds_read_b64_tr_b16 v[118:119], v233 offset:256\n"
    "ds_read_b64_tr_b16 v[120:121], v234 offset:0\n"
    "ds_read_b64_tr_b16 v[122:123], v234 offset:256\n"
    "ds_read_b64_tr_b16 v[124:125], v235 offset:0\n"
    "ds_read_b64_tr_b16 v[126:127], v235 offset:256\n"
    "s_waitcnt lgkmcnt(6)\n"
    "v_mfma_f32_16x16x32_bf16 v[144:147], v[112:115], v[96:99], 0\n"
    "s_add_u32 m0, s52, 70656\n"
    "ds_read_b64_tr_b16 v[128:129], v236 offset:0\n"
    "v_mfma_f32_16x16x32_bf16 v[148:151], v[112:115], v[100:103], 0\n"
    "s_add_u32 s50, %8, 442368\n"
    "ds_read_b64_tr_b16 v[130:131], v236 offset:256\n"
    "buffer_load_dwordx4 v246, s[44:47], s50 offen lds\n"
    "v_mfma_f32_16x16x32_bf16 v[80:83], v[112:115], v[92:95], 0\n"
    "s_add_u32 m0, s56, 89600\n"
    "ds_read_b64_tr_b16 v[132:133], v237 offset:0\n"
    "s_waitcnt lgkmcnt(7)\n"
    "v_mfma_f32_16x16x32_bf16 v[152:155], v[116:119], v[96:99], 0\n"
    "s_add_u32 s51, %7, 256\n"
    "ds_read_b64_tr_b16 v[134:135], v237 offset:256\n"
    "buffer_load_dwordx4 v243, s[40:43], s51 offen lds\n"
    "v_mfma_f32_16x16x32_bf16 v[156:159], v[116:119], v[100:103], 0\n"
    "s_add_u32 m0, s52, 71712\n"
    "ds_read_b64_tr_b16 v[112:113], v238 offset:0\n"
    "v_mfma_f32_16x16x32_bf16 v[84:87], v[116:119], v[92:95], 0\n"
    "s_add_u32 s48, %8, 445824\n"
    "ds_read_b64_tr_b16 v[114:115], v238 offset:256\n"
    "buffer_load_dwordx4 v246, s[44:47], s48 offen lds\n"
    "s_waitcnt lgkmcnt(8)\n"
    "v_mfma_f32_16x16x32_bf16 v[160:163], v[120:123], v[96:99], 0\n"
    "s_add_u32 m0, s56, 90624\n"
    "ds_read_b64_tr_b16 v[116:117], v239 offset:0\n"
    "v_mfma_f32_16x16x32_bf16 v[164:167], v[120:123], v[100:103], 0\n"
    "s_add_u32 s49, %7, 9472\n"
    "ds_read_b64_tr_b16 v[118:119], v239 offset:256\n"
    "buffer_load_dwordx4 v243, s[40:43], s49 offen lds\n"
    "v_mfma_f32_16x16x32_bf16 v[216:219], v[92:95], v[96:99], 0\n"
    "s_add_u32 m0, s52, 80128\n"
    "s_add_u32 s50, %8, 552960\n"
    "buffer_load_dwordx4 v246, s[44:47], s50 offen lds\n"
    "s_waitcnt lgkmcnt(8)\n"
    "v_mfma_f32_16x16x32_bf16 v[168:171], v[124:127], v[96:99], 0\n"
    "s_add_u32 m0, s56, 91648\n"
    "ds_read_b64_tr_b16 v[120:121], v240 offset:0\n"
    "v_mfma_f32_16x16x32_bf16 v[172:175], v[124:127], v[100:103], 0\n"
    "s_add_u32 s51, %7, 18688\n"
    "ds_read_b64_tr_b16 v[122:123], v240 offset:256\n"
    "buffer_load_dwordx4 v243, s[40:43], s51 offen lds\n"
    "v_mfma_f32_16x16x32_bf16 v[220:223], v[92:95], v[100:103], 0\n"
    "s_add_u32 m0, s52, 81184\n"
    "ds_read_b128 v[104:107], v242 offset:0\n"
    "s_waitcnt lgkmcnt(9)\n"
    "v_mfma_f32_16x16x32_bf16 v[176:179], v[128:131], v[96:99], 0\n"
    "s_add_u32 s48, %8, 556416\n"
    "ds_read_b128 v[108:111], v242 offset:2048\n"
    "buffer_load_dwordx4 v246, s[44:47], s48 offen lds\n"
    "v_mfma_f32_16x16x32_bf16 v[180:183], v[128:131], v[100:103], 0\n"
    "s_add_u32 m0, s56, 92672\n"
    "ds_read_b64_tr_b16 v[124:125], v232 offset:9472\n"
    "v_mfma_f32_16x16x32_bf16 v[224:227], v[96:99], v[96:99], 0\n"
    "s_add_u32 s49, %7, 27904\n"
    "ds_read_b64_tr_b16 v[126:127], v232 offset:9728\n"
    "buffer_load_dwordx4 v243, s[40:43], s49 offen lds\n"
    "s_waitcnt lgkmcnt(10)\n"
    "v_mfma_f32_16x16x32_bf16 v[184:187], v[132:135], v[96:99], 0\n"
    "ds_read_b64_tr_b16 v[128:129], v233 offset:9472\n"
    "s_add_u32 m0, s55, 79104\n"
    "s_add_u32 s50, %9, 442368\n"
    "s_mov_b32 exec_hi, 0\n"
    "buffer_load_dwordx4 v247, s[44:47], s50 offen lds\n"
    "s_mov_b32 exec_hi, -1\n"
    "v_mfma_f32_16x16x32_bf16 v[188:191], v[132:135], v[100:103], 0\n"
    "s_add_u32 m0, s52, 105984\n"
    "ds_read_b64_tr_b16 v[130:131], v233 offset:9728\n"
    "v_mfma_f32_16x16x32_bf16 v[228:231], v[100:103], v[100:103], 0\n"
    "s_add_u32 s51, %8, 663552\n"
    "buffer_load_dwordx4 v246, s[44:47], s51 offen lds\n"
    "s_waitcnt lgkmcnt(10)\n"
    "v_mfma_f32_16x16x32_bf16 v[192:195], v[112:115], v[96:99], 0\n"
    "s_add_u32 m0, s56, 124928\n"
    "ds_read_b64_tr_b16 v[132:133], v234 offset:9472\n"
    "v_mfma_f32_16x16x32_bf16 v[196:199], v[112:115], v[100:103], 0\n"
    "s_add_u32 s48, %7, 384\n"
    "ds_read_b64_tr_b16 v[134:135], v234 offset:9728\n"
    "s_waitcnt lgkmcnt(10)\n"
    "v_mfma_f32_16x16x32_bf16 v[200:203], v[116:119], v[96:99], 0\n"
    "ds_read_b64_tr_b16 v[112:113], v235 offset:9472\n"
    "v_mfma_f32_16x16x32_bf16 v[204:207], v[116:119], v[100:103], 0\n"
    "ds_read_b64_tr_b16 v[114:115], v235 offset:9728\n"
    "s_waitcnt lgkmcnt(10)\n"
    "v_mfma_f32_16x16x32_bf16 v[208:211], v[120:123], v[96:99], 0\n"
    "ds_read_b64_tr_b16 v[116:117], v236 offset:9472\n"
    "v_mfma_f32_16x16x32_bf16 v[212:215], v[120:123], v[100:103], 0\n"
    "ds_read_b64_tr_b16 v[118:119], v236 offset:9728\n"
    "v_mfma_f32_16x16x32_bf16 v[88:91], v[120:123], v[92:95], 0\n"
    "buffer_load_dwordx4 v243, s[40:43], s48 offen lds\n"
    "s_waitcnt vmcnt(12)\n"
    "s_barrier\n"
    "s_waitcnt lgkmcnt(8)\n"
    "v_mfma_f32_16x16x32_bf16 v[144:147], v[124:127], v[104:107], v[144:147]\n"
    "s_add_u32 m0, s52, 107040\n"
    "ds_read_b64_tr_b16 v[120:121], v237 offset:9472\n"
    "v_mfma_f32_16x16x32_bf16 v[148:151], v[124:127], v[108:111], v[148:151]\n"
    "s_add_u32 s49, %8, 667008\n"
    "ds_read_b64_tr_b16 v[122:123], v237 offset:9728\n"
    "v_mfma_f32_16x16x32_bf16 v[80:83], v[124:127], v[92:95], v[80:83]\n"
    "buffer_load_dwordx4 v246, s[44:47], s49 offen lds\n"
    "s_waitcnt lgkmcnt(8)\n"
    "v_mfma_f32_16x16x32_bf16 v[152:155], v[128:131], v[104:107], v[152:155]\n"
    "s_add_u32 m0, s56, 125952\n"
    "ds_read_b64_tr_b16 v[124:125], v238 offset:9472\n"
    "v_mfma_f32_16x16x32_bf16 v[156:159], v[128:131], v[108:111], v[156:159]\n"
    "s_add_u32 s50, %7, 9600\n"
    "ds_read_b64_tr_b16 v[126:127], v238 offset:9728\n"
    "v_mfma_f32_16x16x32_bf16 v[84:87], v[128:131], v[92:95], v[84:87]\n"
    "buffer_load_dwordx4 v243, s[40:43], s50 offen lds\n"
    "s_waitcnt lgkmcnt(8)\n"
    "v_mfma_f32_16x16x32_bf16 v[160:163], v[132:135], v[104:107], v[160:163]\n"
    "s_add_u32 m0, s52, 115456\n"
    "ds_read_b64_tr_b16 v[128:129], v239 offset:9472\n"
    "v_mfma_f32_16x16x32_bf16 v[164:167], v[132:135], v[108:111], v[164:167]\n"
    "s_add_u32 s51, %8, 774144\n"
    "ds_read_b64_tr_b16 v[130:131], v239 offset:9728\n"
    "v_mfma_f32_16x16x32_bf16 v[216:219], v[92:95], v[104:107], v[216:219]\n"
    "buffer_load_dwordx4 v246, s[44:47], s51 offen lds\n"
    "s_waitcnt lgkmcnt(8)\n"
    "v_mfma_f32_16x16x32_bf16 v[168:171], v[112:115], v[104:107], v[168:171]\n"
    "s_add_u32 m0, s56, 126976\n"
    "ds_read_b64_tr_b16 v[132:133], v240 offset:9472\n"
    "v_mfma_f32_16x16x32_bf16 v[172:175], v[112:115], v[108:111], v[172:175]\n"
    "s_add_u32 s48, %7, 18816\n"
    "ds_read_b64_tr_b16 v[134:135], v240 offset:9728\n"
    "v_mfma_f32_16x16x32_bf16 v[220:223], v[92:95], v[108:111], v[220:223]\n"
    "ds_read_b128 v[96:99], v241 offset:35328\n"
    "s_waitcnt lgkmcnt(9)\n"
    "v_mfma_f32_16x16x32_bf16 v[176:179], v[116:119], v[104:107], v[176:179]\n"
    "ds_read_b128 v[100:103], v241 offset:37376\n"
    "v_mfma_f32_16x16x32_bf16 v[180:183], v[116:119], v[108:111], v[180:183]\n"
    "ds_read_b64_tr_b16 v[112:113], v232 offset:35328\n"
    "v_mfma_f32_16x16x32_bf16 v[224:227], v[104:107], v[104:107], v[224:227]\n"
    "ds_read_b64_tr_b16 v[114:115], v232 offset:35584\n"
    "s_waitcnt lgkmcnt(10)\n"
    "v_mfma_f32_16x16x32_bf16 v[184:187], v[120:123], v[104:107], v[184:187]\n"
    "ds_read_b64_tr_b16 v[116:117], v233 offset:35328\n"
    "v_mfma_f32_16x16x32_bf16 v[188:191], v[120:123], v[108:111], v[188:191]\n"
    "ds_read_b64_tr_b16 v[118:119], v233 offset:35584\n"
    "v_mfma_f32_16x16x32_bf16 v[228:231], v[108:111], v[108:111], v[228:231]\n"
    "buffer_load_dwordx4 v243, s[40:43], s48 offen lds\n"
    "s_waitcnt lgkmcnt(10)\n"
    "v_mfma_f32_16x16x32_bf16 v[192:195], v[124:127], v[104:107], v[192:195]\n"
    "s_add_u32 m0, s52, 116512\n"
    "ds_read_b64_tr_b16 v[120:121], v234 offset:35328\n"
    "v_mfma_f32_16x16x32_bf16 v[196:199], v[124:127], v[108:111], v[196:199]\n"
    "s_add_u32 s49, %8, 777600\n"
    "ds_read_b64_tr_b16 v[122:123], v234 offset:35584\n"
    "s_waitcnt lgkmcnt(10)\n"
    "v_mfma_f32_16x16x32_bf16 v[200:203], v[128:131], v[104:107], v[200:203]\n"
    "ds_read_b64_tr_b16 v[124:125], v235 offset:35328\n"
    "v_mfma_f32_16x16x32_bf16 v[204:207], v[128:131], v[108:111], v[204:207]\n"
    "ds_read_b64_tr_b16 v[126:127], v235 offset:35584\n"
    "s_waitcnt lgkmcnt(10)\n"
    "v_mfma_f32_16x16x32_bf16 v[208:211], v[132:135], v[104:107], v[208:211]\n"
    "ds_read_b64_tr_b16 v[128:129], v236 offset:35328\n"
    "v_mfma_f32_16x16x32_bf16 v[212:215], v[132:135], v[108:111], v[212:215]\n"
    "ds_read_b64_tr_b16 v[130:131], v236 offset:35584\n"
    "v_mfma_f32_16x16x32_bf16 v[88:91], v[132:135], v[92:95], v[88:91]\n"
    "buffer_load_dwordx4 v246, s[44:47], s49 offen lds\n"
    "s_waitcnt lgkmcnt(8)\n"
    "v_mfma_f32_16x16x32_bf16 v[144:147], v[112:115], v[96:99], v[144:147]\n"
    "s_add_u32 m0, s56, 128000\n"
    "ds_read_b64_tr_b16 v[132:133], v237 offset:35328\n"
    "v_mfma_f32_16x16x32_bf16 v[148:151], v[112:115], v[100:103], v[148:151]\n"
    "s_add_u32 s50, %7, 28032\n"
    "ds_read_b64_tr_b16 v[134:135], v237 offset:35584\n"
    "v_mfma_f32_16x16x32_bf16 v[80:83], v[112:115], v[92:95], v[80:83]\n"
    "buffer_load_dwordx4 v243, s[40:43], s50 offen lds\n"
    "s_waitcnt lgkmcnt(8)\n"
    "v_mfma_f32_16x16x32_bf16 v[152:155], v[116:119], v[96:99], v[152:155]\n"
    "ds_read_b64_tr_b16 v[112:113], v238 offset:35328\n"
    "v_mfma_f32_16x16x32_bf16 v[156:159], v[116:119], v[100:103], v[156:159]\n"
    "ds_read_b64_tr_b16 v[114:115], v238 offset:35584\n"
    "v_mfma_f32_16x16x32_bf16 v[84:87], v[116:119], v[92:95], v[84:87]\n"
    "s_add_u32 m0, s55, 114432\n"
    "s_add_u32 s51, %9, 663552\n"
    "s_mov_b32 exec_hi, 0\n"
    "buffer_load_dwordx4 v247, s[44:47], s51 offen lds\n"
    "s_mov_b32 exec_hi, -1\n"
    "s_waitcnt lgkmcnt(8)\n"
    "v_mfma_f32_16x16x32_bf16 v[160:163], v[120:123], v[96:99], v[160:163]\n"
    "ds_read_b64_tr_b16 v[116:117], v239 offset:35328\n"
    "v_mfma_f32_16x16x32_bf16 v[164:167], v[120:123], v[100:103], v[164:167]\n"
    "ds_read_b64_tr_b16 v[118:119], v239 offset:35584\n"
    "v_mfma_f32_16x16x32_bf16 v[216:219], v[92:95], v[96:99], v[216:219]\n"
    "s_waitcnt lgkmcnt(8)\n"
    "v_mfma_f32_16x16x32_bf16 v[168:171], v[124:127], v[96:99], v[168:171]\n"
    "ds_read_b64_tr_b16 v[120:121], v240 offset:35328\n"
    "v_mfma_f32_16x16x32_bf16 v[172:175], v[124:127], v[100:103], v[172:175]\n"
    "ds_read_b64_tr_b16 v[122:123], v240 offset:35584\n"
    "v_mfma_f32_16x16x32_bf16 v[220:223], v[92:95], v[100:103], v[220:223]\n"
    "ds_read_b128 v[104:107], v242 offset:35328\n"
    "s_waitcnt lgkmcnt(9)\n"
    "v_mfma_f32_16x16x32_bf16 v[176:179], v[128:131], v[96:99], v[176:179]\n"
    "ds_read_b128 v[108:111], v242 offset:37376\n"
    "v_mfma_f32_16x16x32_bf16 v[180:183], v[128:131], v[100:103], v[180:183]\n"
    "ds_read_b64_tr_b16 v[124:125], v232 offset:44800\n"
    "v_mfma_f32_16x16x32_bf16 v[224:227], v[96:99], v[96:99], v[224:227]\n"
    "ds_read_b64_tr_b16 v[126:127], v232 offset:45056\n"
    "s_waitcnt lgkmcnt(10)\n"
    "v_mfma_f32_16x16x32_bf16 v[184:187], v[132:135], v[96:99], v[184:187]\n"
    "ds_read_b64_tr_b16 v[128:129], v233 offset:44800\n"
    "v_mfma_f32_16x16x32_bf16 v[188:191], v[132:135], v[100:103], v[188:191]\n"
    "ds_read_b64_tr_b16 v[130:131], v233 offset:45056\n"
    "v_mfma_f32_16x16x32_bf16 v[228:231], v[100:103], v[100:103], v[228:231]\n"
    "s_waitcnt lgkmcnt(10)\n"
    "v_mfma_f32_16x16x32_bf16 v[192:195], v[112:115], v[96:99], v[192:195]\n"
    "ds_read_b64_tr_b16 v[132:133], v234 offset:44800\n"
    "v_mfma_f32_16x16x32_bf16 v[196:199], v[112:115], v[100:103], v[196:199]\n"
    "ds_read_b64_tr_b16 v[134:135], v234 offset:45056\n"
    "s_waitcnt lgkmcnt(10)\n"
    "v_mfma_f32_16x16x32_bf16 v[200:203], v[116:119], v[96:99], v[200:203]\n"
    "ds_read_b64_tr_b16 v[112:113], v235 offset:44800\n"
    "v_mfma_f32_16x16x32_bf16 v[204:207], v[116:119], v[100:103], v[204:207]\n"
    "ds_read_b64_tr_b16 v[114:115], v235 offset:45056\n"
    "s_waitcnt lgkmcnt(10)\n"
    "v_mfma_f32_16x16x32_bf16 v[208:211], v[120:123], v[96:99], v[208:211]\n"
    "ds_read_b64_tr_b16 v[116:117], v236 offset:44800\n"
    "v_mfma_f32_16x16x32_bf16 v[212:215], v[120:123], v[100:103], v[212:215]\n"
    "ds_read_b64_tr_b16 v[118:119], v236 offset:45056\n"
    "v_mfma_f32_16x16x32_bf16 v[88:91], v[120:123], v[92:95], v[88:91]\n"
    "s_waitcnt vmcnt(9)\n"
    "s_barrier\n"
    "s_waitcnt lgkmcnt(8)\n"
    "v_mfma_f32_16x16x32_bf16 v[144:147], v[124:127], v[104:107], v[144:147]\n"
    "s_add_u32 m0, s52, 0\n"
    "ds_read_b64_tr_b16 v[120:121], v237 offset:44800\n"
    "v_mfma_f32_16x16x32_bf16 v[148:151], v[124:127], v[108:111], v[148:151]\n"
    "s_add_u32 s48, %8, 884736\n"
    "ds_read_b64_tr_b16 v[122:123], v237 offset:45056\n"
    "v_mfma_f32_16x16x32_bf16 v[80:83], v[124:127], v[92:95], v[80:83]\n"
    "buffer_load_dwordx4 v246, s[44:47], s48 offen lds\n"
    "s_waitcnt lgkmcnt(8)\n"
    "v_mfma_f32_16x16x32_bf16 v[152:155], v[128:131], v[104:107], v[152:155]\n"
    "s_add_u32 m0, s56, 18944\n"
    "ds_read_b64_tr_b16 v[124:125], v238 offset:44800\n"
    "v_mfma_f32_16x16x32_bf16 v[156:159], v[128:131], v[108:111], v[156:159]\n"
    "s_add_u32 s49, %7, 512\n"
    "ds_read_b64_tr_b16 v[126:127], v238 offset:45056\n"
    "v_mfma_f32_16x16x32_bf16 v[84:87], v[128:131], v[92:95], v[84:87]\n"
    "buffer_load_dwordx4 v243, s[40:43], s49 offen lds\n"
    "s_waitcnt lgkmcnt(8)\n"
    "v_mfma_f32_16x16x32_bf16 v[160:163], v[132:135], v[104:107], v[160:163]\n"
    "s_add_u32 m0, s52, 1056\n"
    "ds_read_b64_tr_b16 v[128:129], v239 offset:44800\n"
    "v_mfma_f32_16x16x32_bf16 v[164:167], v[132:135], v[108:111], v[164:167]\n"
    "s_add_u32 s50, %8, 888192\n"
    "ds_read_b64_tr_b16 v[130:131], v239 offset:45056\n"
    "v_mfma_f32_16x16x32_bf16 v[216:219], v[92:95], v[104:107], v[216:219]\n"
    "buffer_load_dwordx4 v246, s[44:47], s50 offen lds\n"
    "s_waitcnt lgkmcnt(8)\n"
    "v_mfma_f32_16x16x32_bf16 v[168:171], v[112:115], v[104:107], v[168:171]\n"
    "s_add_u32 m0, s56, 19968\n"
    "ds_read_b64_tr_b16 v[132:133], v240 offset:44800\n"
    "v_mfma_f32_16x16x32_bf16 v[172:175], v[112:115], v[108:111], v[172:175]\n"
    "s_add_u32 s51, %7, 9728\n"
    "ds_read_b64_tr_b16 v[134:135], v240 offset:45056\n"
    "v_mfma_f32_16x16x32_bf16 v[220:223], v[92:95], v[108:111], v[220:223]\n"
    "ds_read_b128 v[96:99], v69 offset:0\n"
    "s_waitcnt lgkmcnt(9)\n"
    "v_mfma_f32_16x16x32_bf16 v[176:179], v[116:119], v[104:107], v[176:179]\n"
    "ds_read_b128 v[100:103], v69 offset:2048\n"
    "v_mfma_f32_16x16x32_bf16 v[180:183], v[116:119], v[108:111], v[180:183]\n"
    "ds_read_b64_tr_b16 v[112:113], v60 offset:0\n"
    "v_mfma_f32_16x16x32_bf16 v[224:227], v[104:107], v[104:107], v[224:227]\n"
    "ds_read_b64_tr_b16 v[114:115], v60 offset:256\n"
    "s_waitcnt lgkmcnt(10)\n"
    "v_mfma_f32_16x16x32_bf16 v[184:187], v[120:123], v[104:107], v[184:187]\n"
    "ds_read_b64_tr_b16 v[116:117], v61 offset:0\n"
    "v_mfma_f32_16x16x32_bf16 v[188:191], v[120:123], v[108:111], v[188:191]\n"
    "ds_read_b64_tr_b16 v[118:119], v61 offset:256\n"
    "v_mfma_f32_16x16x32_bf16 v[228:231], v[108:111], v[108:111], v[228:231]\n"
    "buffer_load_dwordx4 v243, s[40:43], s51 offen lds\n"
    "s_waitcnt lgkmcnt(10)\n"
    "v_mfma_f32_16x16x32_bf16 v[192:195], v[124:127], v[104:107], v[192:195]\n"
    "s_add_u32 m0, s52, 9472\n"
    "ds_read_b64_tr_b16 v[120:121], v62 offset:0\n"
    "v_mfma_f32_16x16x32_bf16 v[196:199], v[124:127], v[108:111], v[196:199]\n"
    "s_add_u32 s48, %8, 995328\n"
    "ds_read_b64_tr_b16 v[122:123], v62 offset:256\n"
    "s_waitcnt lgkmcnt(10)\n"
    "v_mfma_f32_16x16x32_bf16 v[200:203], v[128:131], v[104:107], v[200:203]\n"
    "ds_read_b64_tr_b16 v[124:125], v63 offset:0\n"
    "v_mfma_f32_16x16x32_bf16 v[204:207], v[128:131], v[108:111], v[204:207]\n"
    "ds_read_b64_tr_b16 v[126:127], v63 offset:256\n"
    "s_waitcnt lgkmcnt(10)\n"
    "v_mfma_f32_16x16x32_bf16 v[208:211], v[132:135], v[104:107], v[208:211]\n"
    "ds_read_b64_tr_b16 v[128:129], v64 offset:0\n"
    "v_mfma_f32_16x16x32_bf16 v[212:215], v[132:135], v[108:111], v[212:215]\n"
    "ds_read_b64_tr_b16 v[130:131], v64 offset:256\n"
    "v_mfma_f32_16x16x32_bf16 v[88:91], v[132:135], v[92:95], v[88:91]\n"
    "buffer_load_dwordx4 v246, s[44:47], s48 offen lds\n"
    "s_waitcnt lgkmcnt(8)\n"
    "v_mfma_f32_16x16x32_bf16 v[144:147], v[112:115], v[96:99], v[144:147]\n"
    "s_add_u32 m0, s56, 20992\n"
    "ds_read_b64_tr_b16 v[132:133], v65 offset:0\n"
    "v_mfma_f32_16x16x32_bf16 v[148:151], v[112:115], v[100:103], v[148:151]\n"
    "s_add_u32 s49, %7, 18944\n"
    "ds_read_b64_tr_b16 v[134:135], v65 offset:256\n"
    "v_mfma_f32_16x16x32_bf16 v[80:83], v[112:115], v[92:95], v[80:83]\n"
    "buffer_load_dwordx4 v243, s[40:43], s49 offen lds\n"
    "s_waitcnt lgkmcnt(8)\n"
    "v_mfma_f32_16x16x32_bf16 v[152:155], v[116:119], v[96:99], v[152:155]\n"
    "s_add_u32 m0, s52, 10528\n"
    "ds_read_b64_tr_b16 v[112:113], v66 offset:0\n"
    "v_mfma_f32_16x16x32_bf16 v[156:159], v[116:119], v[100:103], v[156:159]\n"
    "s_add_u32 s50, %8, 998784\n"
    "ds_read_b64_tr_b16 v[114:115], v66 offset:256\n"
    "v_mfma_f32_16x16x32_bf16 v[84:87], v[116:119], v[92:95], v[84:87]\n"
    "buffer_load_dwordx4 v246, s[44:47], s50 offen lds\n"
    "s_waitcnt lgkmcnt(8)\n"
    "v_mfma_f32_16x16x32_bf16 v[160:163], v[120:123], v[96:99], v[160:163]\n"
    "s_add_u32 m0, s56, 22016\n"
    "ds_read_b64_tr_b16 v[116:117], v67 offset:0\n"
    "v_mfma_f32_16x16x32_bf16 v[164:167], v[120:123], v[100:103], v[164:167]\n"
    "s_add_u32 s51, %7, 28160\n"
    "ds_read_b64_tr_b16 v[118:119], v67 offset:256\n"
    "v_mfma_f32_16x16x32_bf16 v[216:219], v[92:95], v[96:99], v[216:219]\n"
    "buffer_load_dwordx4 v243, s[40:43], s51 offen lds\n"
    "s_waitcnt lgkmcnt(8)\n"
    "v_mfma_f32_16x16x32_bf16 v[168:171], v[124:127], v[96:99], v[168:171]\n"
    "ds_read_b64_tr_b16 v[120:121], v68 offset:0\n"
    "v_mfma_f32_16x16x32_bf16 v[172:175], v[124:127], v[100:103], v[172:175]\n"
    "ds_read_b64_tr_b16 v[122:123], v68 offset:256\n"
    "v_mfma_f32_16x16x32_bf16 v[220:223], v[92:95], v[100:103], v[220:223]\n"
    "ds_read_b128 v[104:107], v70 offset:0\n"
    "s_waitcnt lgkmcnt(9)\n"
    "v_mfma_f32_16x16x32_bf16 v[176:179], v[128:131], v[96:99], v[176:179]\n"
    "ds_read_b128 v[108:111], v70 offset:2048\n"
    "v_mfma_f32_16x16x32_bf16 v[180:183], v[128:131], v[100:103], v[180:183]\n"
    "ds_read_b64_tr_b16 v[124:125], v60 offset:9472\n"
    "v_mfma_f32_16x16x32_bf16 v[224:227], v[96:99], v[96:99], v[224:227]\n"
    "ds_read_b64_tr_b16 v[126:127], v60 offset:9728\n"
    "s_waitcnt lgkmcnt(10)\n"
    "v_mfma_f32_16x16x32_bf16 v[184:187], v[132:135], v[96:99], v[184:187]\n"
    "ds_read_b64_tr_b16 v[128:129], v61 offset:9472\n"
    "v_mfma_f32_16x16x32_bf16 v[188:191], v[132:135], v[100:103], v[188:191]\n"
    "ds_read_b64_tr_b16 v[130:131], v61 offset:9728\n"
    "v_mfma_f32_16x16x32_bf16 v[228:231], v[100:103], v[100:103], v[228:231]\n"
    "s_add_u32 m0, s55, 8448\n"
    "s_add_u32 s48, %9, 884736\n"
    "s_mov_b32 exec_hi, 0\n"
    "buffer_load_dwordx4 v247, s[44:47], s48 offen lds\n"
    "s_mov_b32 exec_hi, -1\n"
    "s_waitcnt lgkmcnt(10)\n"
    "v_mfma_f32_16x16x32_bf16 v[192:195], v[112:115], v[96:99], v[192:195]\n"
    "ds_read_b64_tr_b16 v[132:133], v62 offset:9472\n"
    "v_mfma_f32_16x16x32_bf16 v[196:199], v[112:115], v[100:103], v[196:199]\n"
    "ds_read_b64_tr_b16 v[134:135], v62 offset:9728\n"
    "s_waitcnt lgkmcnt(10)\n"
    "v_mfma_f32_16x16x32_bf16 v[200:203], v[116:119], v[96:99], v[200:203]\n"
    "ds_read_b64_tr_b16 v[112:113], v63 offset:9472\n"
    "v_mfma_f32_16x16x32_bf16 v[204:207], v[116:119], v[100:103], v[204:207]\n"
    "ds_read_b64_tr_b16 v[114:115], v63 offset:9728\n"
    "s_waitcnt lgkmcnt(10)\n"
    "v_mfma_f32_16x16x32_bf16 v[208:211], v[120:123], v[96:99], v[208:211]\n"
    "ds_read_b64_tr_b16 v[116:117], v64 offset:9472\n"
    "v_mfma_f32_16x16x32_bf16 v[212:215], v[120:123], v[100:103], v[212:215]\n"
    "ds_read_b64_tr_b16 v[118:119], v64 offset:9728\n"
    "v_mfma_f32_16x16x32_bf16 v[88:91], v[120:123], v[92:95], v[88:91]\n"
    "s_waitcnt vmcnt(9)\n"
    "s_barrier\n"
    "s_waitcnt lgkmcnt(8)\n"
    "v_mfma_f32_16x16x32_bf16 v[144:147], v[124:127], v[104:107], v[144:147]\n"
    "s_add_u32 m0, s52, 35328\n"
    "ds_read_b64_tr_b16 v[120:121], v65 offset:9472\n"
    "v_mfma_f32_16x16x32_bf16 v[148:151], v[124:127], v[108:111], v[148:151]\n"
    "s_add_u32 s49, %8, 1105920\n"
    "ds_read_b64_tr_b16 v[122:123], v65 offset:9728\n"
    "v_mfma_f32_16x16x32_bf16 v[80:83], v[124:127], v[92:95], v[80:83]\n"
    "buffer_load_dwordx4 v246, s[44:47], s49 offen lds\n"
    "s_waitcnt lgkmcnt(8)\n"
    "v_mfma_f32_16x16x32_bf16 v[152:155], v[128:131], v[104:107], v[152:155]\n"
    "s_add_u32 m0, s56, 54272\n"
    "ds_read_b64_tr_b16 v[124:125], v66 offset:9472\n"
    "v_mfma_f32_16x16x32_bf16 v[156:159], v[128:131], v[108:111], v[156:159]\n"
    "s_add_u32 s50, %7, 640\n"
    "ds_read_b64_tr_b16 v[126:127], v66 offset:9728\n"
    "v_mfma_f32_16x16x32_bf16 v[84:87], v[128:131], v[92:95], v[84:87]\n"
    "buffer_load_dwordx4 v243, s[40:43], s50 offen lds\n"
    "s_waitcnt lgkmcnt(8)\n"
    "v_mfma_f32_16x16x32_bf16 v[160:163], v[132:135], v[104:107], v[160:163]\n"
    "s_add_u32 m0, s52, 36384\n"
    "ds_read_b64_tr_b16 v[128:129], v67 offset:9472\n"
    "v_mfma_f32_16x16x32_bf16 v[164:167], v[132:135], v[108:111], v[164:167]\n"
    "s_add_u32 s51, %8, 1109376\n"
    "ds_read_b64_tr_b16 v[130:131], v67 offset:9728\n"
    "v_mfma_f32_16x16x32_bf16 v[216:219], v[92:95], v[104:107], v[216:219]\n"
    "buffer_load_dwordx4 v246, s[44:47], s51 offen lds\n"
    "s_waitcnt lgkmcnt(8)\n"
    "v_mfma_f32_16x16x32_bf16 v[168:171], v[112:115], v[104:107], v[168:171]\n"
    "s_add_u32 m0, s56, 55296\n"
    "ds_read_b64_tr_b16 v[132:133], v68 offset:9472\n"
    "v_mfma_f32_16x16x32_bf16 v[172:175], v[112:115], v[108:111], v[172:175]\n"
    "s_add_u32 s48, %7, 9856\n"
    "ds_read_b64_tr_b16 v[134:135], v68 offset:9728\n"
    "v_mfma_f32_16x16x32_bf16 v[220:223], v[92:95], v[108:111], v[220:223]\n"
    "ds_read_b128 v[96:99], v69 offset:35328\n"
    "s_waitcnt lgkmcnt(9)\n"
    "v_mfma_f32_16x16x32_bf16 v[176:179], v[116:119], v[104:107], v[176:179]\n"
    "ds_read_b128 v[100:103], v69 offset:37376\n"
    "v_mfma_f32_16x16x32_bf16 v[180:183], v[116:119], v[108:111], v[180:183]\n"
    "ds_read_b64_tr_b16 v[112:113], v60 offset:35328\n"
    "v_mfma_f32_16x16x32_bf16 v[224:227], v[104:107], v[104:107], v[224:227]\n"
    "ds_read_b64_tr_b16 v[114:115], v60 offset:35584\n"
    "s_waitcnt lgkmcnt(10)\n"
    "v_mfma_f32_16x16x32_bf16 v[184:187], v[120:123], v[104:107], v[184:187]\n"
    "ds_read_b64_tr_b16 v[116:117], v61 offset:35328\n"
    "v_mfma_f32_16x16x32_bf16 v[188:191], v[120:123], v[108:111], v[188:191]\n"
    "ds_read_b64_tr_b16 v[118:119], v61 offset:35584\n"
    "v_mfma_f32_16x16x32_bf16 v[228:231], v[108:111], v[108:111], v[228:231]\n"
    "buffer_load_dwordx4 v243, s[40:43], s48 offen lds\n"
    "s_waitcnt lgkmcnt(10)\n"
    "v_mfma_f32_16x16x32_bf16 v[192:195], v[124:127], v[104:107], v[192:195]\n"
    "s_add_u32 m0, s52, 44800\n"
    "ds_read_b64_tr_b16 v[120:121], v62 offset:35328\n"
    "v_mfma_f32_16x16x32_bf16 v[196:199], v[124:127], v[108:111], v[196:199]\n"
    "s_add_u32 s49, %8, 1216512\n"
    "ds_read_b64_tr_b16 v[122:123], v62 offset:35584\n"
    "s_waitcnt lgkmcnt(10)\n"
    "v_mfma_f32_16x16x32_bf16 v[200:203], v[128:131], v[104:107], v[200:203]\n"
    "ds_read_b64_tr_b16 v[124:125], v63 offset:35328\n"
    "v_mfma_f32_16x16x32_bf16 v[204:207], v[128:131], v[108:111], v[204:207]\n"
    "ds_read_b64_tr_b16 v[126:127], v63 offset:35584\n"
    "s_waitcnt lgkmcnt(10)\n"
    "v_mfma_f32_16x16x32_bf16 v[208:211], v[132:135], v[104:107], v[208:211]\n"
    "ds_read_b64_tr_b16 v[128:129], v64 offset:35328\n"
    "v_mfma_f32_16x16x32_bf16 v[212:215], v[132:135], v[108:111], v[212:215]\n"
    "ds_read_b64_tr_b16 v[130:131], v64 offset:35584\n"
    "v_mfma_f32_16x16x32_bf16 v[88:91], v[132:135], v[92:95], v[88:91]\n"
    "buffer_load_dwordx4 v246, s[44:47], s49 offen lds\n"
    "s_waitcnt lgkmcnt(8)\n"
    "v_mfma_f32_16x16x32_bf16 v[144:147], v[112:115], v[96:99], v[144:147]\n"
    "s_add_u32 m0, s56, 56320\n"
    "ds_read_b64_tr_b16 v[132:133], v65 offset:35328\n"
    "v_mfma_f32_16x16x32_bf16 v[148:151], v[112:115], v[100:103], v[148:151]\n"
    "s_add_u32 s50, %7, 19072\n"
    "ds_read_b64_tr_b16 v[134:135], v65 offset:35584\n"
    "v_mfma_f32_16x16x32_bf16 v[80:83], v[112:115], v[92:95], v[80:83]\n"
    "buffer_load_dwordx4 v243, s[40:43], s50 offen lds\n"
    "s_waitcnt lgkmcnt(8)\n"
    "v_mfma_f32_16x16x32_bf16 v[152:155], v[116:119], v[96:99], v[152:155]\n"
    "s_add_u32 m0, s52, 45856\n"
    "ds_read_b64_tr_b16 v[112:113], v66 offset:35328\n"
    "v_mfma_f32_16x16x32_bf16 v[156:159], v[116:119], v[100:103], v[156:159]\n"
    "s_add_u32 s51, %8, 1219968\n"
    "ds_read_b64_tr_b16 v[114:115], v66 offset:35584\n"
    "v_mfma_f32_16x16x32_bf16 v[84:87], v[116:119], v[92:95], v[84:87]\n"
    "buffer_load_dwordx4 v246, s[44:47], s51 offen lds\n"
    "s_waitcnt lgkmcnt(8)\n"
    "v_mfma_f32_16x16x32_bf16 v[160:163], v[120:123], v[96:99], v[160:163]\n"
    "s_add_u32 m0, s56, 57344\n"
    "ds_read_b64_tr_b16 v[116:117], v67 offset:35328\n"
    "v_mfma_f32_16x16x32_bf16 v[164:167], v[120:123], v[100:103], v[164:167]\n"
    "s_add_u32 s48, %7, 28288\n"
    "ds_read_b64_tr_b16 v[118:119], v67 offset:35584\n"
    "v_mfma_f32_16x16x32_bf16 v[216:219], v[92:95], v[96:99], v[216:219]\n"
    "buffer_load_dwordx4 v243, s[40:43], s48 offen lds\n"
    "s_waitcnt lgkmcnt(8)\n"
    "v_mfma_f32_16x16x32_bf16 v[168:171], v[124:127], v[96:99], v[168:171]\n"
    "ds_read_b64_tr_b16 v[120:121], v68 offset:35328\n"
    "v_mfma_f32_16x16x32_bf16 v[172:175], v[124:127], v[100:103], v[172:175]\n"
    "ds_read_b64_tr_b16 v[122:123], v68 offset:35584\n"
    "v_mfma_f32_16x16x32_bf16 v[220:223], v[92:95], v[100:103], v[220:223]\n"
    "ds_read_b128 v[104:107], v70 offset:35328\n"
    "s_waitcnt lgkmcnt(9)\n"
    "v_mfma_f32_16x16x32_bf16 v[176:179], v[128:131], v[96:99], v[176:179]\n"
    "ds_read_b128 v[108:111], v70 offset:37376\n"
    "v_mfma_f32_16x16x32_bf16 v[180:183], v[128:131], v[100:103], v[180:183]\n"
    "ds_read_b64_tr_b16 v[124:125], v60 offset:44800\n"
    "v_mfma_f32_16x16x32_bf16 v[224:227], v[96:99], v[96:99], v[224:227]\n"
    "ds_read_b64_tr_b16 v[126:127], v60 offset:45056\n"
    "s_waitcnt lgkmcnt(10)\n"
    "v_mfma_f32_16x16x32_bf16 v[184:187], v[132:135], v[96:99], v[184:187]\n"
    "ds_read_b64_tr_b16 v[128:129], v61 offset:44800\n"
    "v_mfma_f32_16x16x32_bf16 v[188:191], v[132:135], v[100:103], v[188:191]\n"
    "ds_read_b64_tr_b16 v[130:131], v61 offset:45056\n"
    "v_mfma_f32_16x16x32_bf16 v[228:231], v[100:103], v[100:103], v[228:231]\n"
    "s_add_u32 m0, s55, 43776\n"
    "s_add_u32 s49, %9, 1105920\n"
    "s_mov_b32 exec_hi, 0\n"
    "buffer_load_dwordx4 v247, s[44:47], s49 offen lds\n"
    "s_mov_b32 exec_hi, -1\n"
    "s_waitcnt lgkmcnt(10)\n"
    "v_mfma_f32_16x16x32_bf16 v[192:195], v[112:115], v[96:99], v[192:195]\n"
    "ds_read_b64_tr_b16 v[132:133], v62 offset:44800\n"
    "v_mfma_f32_16x16x32_bf16 v[196:199], v[112:115], v[100:103], v[196:199]\n"
    "ds_read_b64_tr_b16 v[134:135], v62 offset:45056\n"
    "s_waitcnt lgkmcnt(10)\n"
    "v_mfma_f32_16x16x32_bf16 v[200:203], v[116:119], v[96:99], v[200:203]\n"
    "ds_read_b64_tr_b16 v[112:113], v63 offset:44800\n"
    "v_mfma_f32_16x16x32_bf16 v[204:207], v[116:119], v[100:103], v[204:207]\n"
    "ds_read_b64_tr_b16 v[114:115], v63 offset:45056\n"
    "s_waitcnt lgkmcnt(10)\n"
    "v_mfma_f32_16x16x32_bf16 v[208:211], v[120:123], v[96:99], v[208:211]\n"
    "ds_read_b64_tr_b16 v[116:117], v64 offset:44800\n"
    "v_mfma_f32_16x16x32_bf16 v[212:215], v[120:123], v[100:103], v[212:215]\n"
    "ds_read_b64_tr_b16 v[118:119], v64 offset:45056\n"
    "v_mfma_f32_16x16x32_bf16 v[88:91], v[120:123], v[92:95], v[88:91]\n"
    "s_waitcnt vmcnt(9)\n"
    "s_barrier\n"
    "s_waitcnt lgkmcnt(8)\n"
    "v_mfma_f32_16x16x32_bf16 v[144:147], v[124:127], v[104:107], v[144:147]\n"
    "s_add_u32 m0, s52, 70656\n"
    "ds_read_b64_tr_b16 v[120:121], v65 offset:44800\n"
    "v_mfma_f32_16x16x32_bf16 v[148:151], v[124:127], v[108:111], v[148:151]\n"
    "s_add_u32 s50, %8, 1327104\n"
    "ds_read_b64_tr_b16 v[122:123], v65 offset:45056\n"
    "v_mfma_f32_16x16x32_bf16 v[80:83], v[124:127], v[92:95], v[80:83]\n"
    "buffer_load_dwordx4 v246, s[44:47], s50 offen lds\n"
    "s_waitcnt lgkmcnt(8)\n"
    "v_mfma_f32_16x16x32_bf16 v[152:155], v[128:131], v[104:107], v[152:155]\n"
    "s_add_u32 m0, s56, 89600\n"
    "ds_read_b64_tr_b16 v[124:125], v66 offset:44800\n"
    "v_mfma_f32_16x16x32_bf16 v[156:159], v[128:131], v[108:111], v[156:159]\n"
    "s_add_u32 s51, %7, 768\n"
    "ds_read_b64_tr_b16 v[126:127], v66 offset:45056\n"
    "v_mfma_f32_16x16x32_bf16 v[84:87], v[128:131], v[92:95], v[84:87]\n"
    "buffer_load_dwordx4 v243, s[40:43], s51 offen lds\n"
    "s_waitcnt lgkmcnt(8)\n"
    "v_mfma_f32_16x16x32_bf16 v[160:163], v[132:135], v[104:107], v[160:163]\n"
    "s_add_u32 m0, s52, 71712\n"
    "ds_read_b64_tr_b16 v[128:129], v67 offset:44800\n"
    "v_mfma_f32_16x16x32_bf16 v[164:167], v[132:135], v[108:111], v[164:167]\n"
    "s_add_u32 s48, %8, 1330560\n"
    "ds_read_b64_tr_b16 v[130:131], v67 offset:45056\n"
    "v_mfma_f32_16x16x32_bf16 v[216:219], v[92:95], v[104:107], v[216:219]\n"
    "buffer_load_dwordx4 v246, s[44:47], s48 offen lds\n"
    "s_waitcnt lgkmcnt(8)\n"
    "v_mfma_f32_16x16x32_bf16 v[168:171], v[112:115], v[104:107], v[168:171]\n"
    "s_add_u32 m0, s56, 90624\n"
    "ds_read_b64_tr_b16 v[132:133], v68 offset:44800\n"
    "v_mfma_f32_16x16x32_bf16 v[172:175], v[112:115], v[108:111], v[172:175]\n"
    "s_add_u32 s49, %7, 9984\n"
    "ds_read_b64_tr_b16 v[134:135], v68 offset:45056\n"
    "v_mfma_f32_16x16x32_bf16 v[220:223], v[92:95], v[108:111], v[220:223]\n"
    "ds_read_b128 v[96:99], v241 offset:0\n"
    "s_waitcnt lgkmcnt(9)\n"
    "v_mfma_f32_16x16x32_bf16 v[176:179], v[116:119], v[104:107], v[176:179]\n"
    "ds_read_b128 v[100:103], v241 offset:2048\n"
    "v_mfma_f32_16x16x32_bf16 v[180:183], v[116:119], v[108:111], v[180:183]\n"
    "ds_read_b64_tr_b16 v[112:113], v232 offset:0\n"
    "v_mfma_f32_16x16x32_bf16 v[224:227], v[104:107], v[104:107], v[224:227]\n"
    "ds_read_b64_tr_b16 v[114:115], v232 offset:256\n"
    "s_waitcnt lgkmcnt(10)\n"
    "v_mfma_f32_16x16x32_bf16 v[184:187], v[120:123], v[104:107], v[184:187]\n"
    "ds_read_b64_tr_b16 v[116:117], v233 offset:0\n"
    "v_mfma_f32_16x16x32_bf16 v[188:191], v[120:123], v[108:111], v[188:191]\n"
    "ds_read_b64_tr_b16 v[118:119], v233 offset:256\n"
    "v_mfma_f32_16x16x32_bf16 v[228:231], v[108:111], v[108:111], v[228:231]\n"
    "buffer_load_dwordx4 v243, s[40:43], s49 offen lds\n"
    "s_waitcnt lgkmcnt(10)\n"
    "v_mfma_f32_16x16x32_bf16 v[192:195], v[124:127], v[104:107], v[192:195]\n"
    "s_add_u32 m0, s52, 80128\n"
    "ds_read_b64_tr_b16 v[120:121], v234 offset:0\n"
    "v_mfma_f32_16x16x32_bf16 v[196:199], v[124:127], v[108:111], v[196:199]\n"
    "s_add_u32 s50, %8, 1437696\n"
    "ds_read_b64_tr_b16 v[122:123], v234 offset:256\n"
    "s_waitcnt lgkmcnt(10)\n"
    "v_mfma_f32_16x16x32_bf16 v[200:203], v[128:131], v[104:107], v[200:203]\n"
    "ds_read_b64_tr_b16 v[124:125], v235 offset:0\n"
    "v_mfma_f32_16x16x32_bf16 v[204:207], v[128:131], v[108:111], v[204:207]\n"
    "ds_read_b64_tr_b16 v[126:127], v235 offset:256\n"
    "s_waitcnt lgkmcnt(10)\n"
    "v_mfma_f32_16x16x32_bf16 v[208:211], v[132:135], v[104:107], v[208:211]\n"
    "ds_read_b64_tr_b16 v[128:129], v236 offset:0\n"
    "v_mfma_f32_16x16x32_bf16 v[212:215], v[132:135], v[108:111], v[212:215]\n"
    "ds_read_b64_tr_b16 v[130:131], v236 offset:256\n"
    "v_mfma_f32_16x16x32_bf16 v[88:91], v[132:135], v[92:95], v[88:91]\n"
    "buffer_load_dwordx4 v246, s[44:47], s50 offen lds\n"
    "s_waitcnt lgkmcnt(8)\n"
    "v_mfma_f32_16x16x32_bf16 v[144:147], v[112:115], v[96:99], v[144:147]\n"
    "s_add_u32 m0, s56, 91648\n"
    "ds_read_b64_tr_b16 v[132:133], v237 offset:0\n"
    "v_mfma_f32_16x16x32_bf16 v[148:151], v[112:115], v[100:103], v[148:151]\n"
    "s_add_u32 s51, %7, 19200\n"
    "ds_read_b64_tr_b16 v[134:135], v237 offset:256\n"
    "v_mfma_f32_16x16x32_bf16 v[80:83], v[112:115], v[92:95], v[80:83]\n"
    "buffer_load_dwordx4 v243, s[40:43], s51 offen lds\n"
    "s_waitcnt lgkmcnt(8)\n"
    "v_mfma_f32_16x16x32_bf16 v[152:155], v[116:119], v[96:99], v[152:155]\n"
    "s_add_u32 m0, s52, 81184\n"
    "ds_read_b64_tr_b16 v[112:113], v238 offset:0\n"
    "v_mfma_f32_16x16x32_bf16 v[156:159], v[116:119], v[100:103], v[156:159]\n"
    "s_add_u32 s48, %8, 1441152\n"
    "ds_read_b64_tr_b16 v[114:115], v238 offset:256\n"
    "v_mfma_f32_16x16x32_bf16 v[84:87], v[116:119], v[92:95], v[84:87]\n"
    "buffer_load_dwordx4 v246, s[44:47], s48 offen lds\n"
    "s_waitcnt lgkmcnt(8)\n"
    "v_mfma_f32_16x16x32_bf16 v[160:163], v[120:123], v[96:99], v[160:163]\n"
    "s_add_u32 m0, s56, 92672\n"
    "ds_read_b64_tr_b16 v[116:117], v239 offset:0\n"
    "v_mfma_f32_16x16x32_bf16 v[164:167], v[120:123], v[100:103], v[164:167]\n"
    "s_add_u32 s49, %7, 28416\n"
    "ds_read_b64_tr_b16 v[118:119], v239 offset:256\n"
    "v_mfma_f32_16x16x32_bf16 v[216:219], v[92:95], v[96:99], v[216:219]\n"
    "buffer_load_dwordx4 v243, s[40:43], s49 offen lds\n"
    "s_waitcnt lgkmcnt(8)\n"
    "v_mfma_f32_16x16x32_bf16 v[168:171], v[124:127], v[96:99], v[168:171]\n"
    "ds_read_b64_tr_b16 v[120:121], v240 offset:0\n"
    "v_mfma_f32_16x16x32_bf16 v[172:175], v[124:127], v[100:103], v[172:175]\n"
    "ds_read_b64_tr_b16 v[122:123], v240 offset:256\n"
    "v_mfma_f32_16x16x32_bf16 v[220:223], v[92:95], v[100:103], v[220:223]\n"
    "ds_read_b128 v[104:107], v242 offset:0\n"
    "s_waitcnt lgkmcnt(9)\n"
    "v_mfma_f32_16x16x32_bf16 v[176:179], v[128:131], v[96:99], v[176:179]\n"
    "ds_read_b128 v[108:111], v242 offset:2048\n"
    "v_mfma_f32_16x16x32_bf16 v[180:183], v[128:131], v[100:103], v[180:183]\n"
    "ds_read_b64_tr_b16 v[124:125], v232 offset:9472\n"
    "v_mfma_f32_16x16x32_bf16 v[224:227], v[96:99], v[96:99], v[224:227]\n"
    "ds_read_b64_tr_b16 v[126:127], v232 offset:9728\n"
    "s_waitcnt lgkmcnt(10)\n"
    "v_mfma_f32_16x16x32_bf16 v[184:187], v[132:135], v[96:99], v[184:187]\n"
    "ds_read_b64_tr_b16 v[128:129], v233 offset:9472\n"
    "v_mfma_f32_16x16x32_bf16 v[188:191], v[132:135], v[100:103], v[188:191]\n"
    "ds_read_b64_tr_b16 v[130:131], v233 offset:9728\n"
    "v_mfma_f32_16x16x32_bf16 v[228:231], v[100:103], v[100:103], v[228:231]\n"
    "s_add_u32 m0, s55, 79104\n"
    "s_add_u32 s50, %9, 1327104\n"
    "s_mov_b32 exec_hi, 0\n"
    "buffer_load_dwordx4 v247, s[44:47], s50 offen lds\n"
    "s_mov_b32 exec_hi, -1\n"
    "s_waitcnt lgkmcnt(10)\n"
    "v_mfma_f32_16x16x32_bf16 v[192:195], v[112:115], v[96:99], v[192:195]\n"
    "ds_read_b64_tr_b16 v[132:133], v234 offset:9472\n"
    "v_mfma_f32_16x16x32_bf16 v[196:199], v[112:115], v[100:103], v[196:199]\n"
    "ds_read_b64_tr_b16 v[134:135], v234 offset:9728\n"
    "s_waitcnt lgkmcnt(10)\n"
    "v_mfma_f32_16x16x32_bf16 v[200:203], v[116:119], v[96:99], v[200:203]\n"
    "ds_read_b64_tr_b16 v[112:113], v235 offset:9472\n"
    "v_mfma_f32_16x16x32_bf16 v[204:207], v[116:119], v[100:103], v[204:207]\n"
    "ds_read_b64_tr_b16 v[114:115], v235 offset:9728\n"
    "s_waitcnt lgkmcnt(10)\n"
    "v_mfma_f32_16x16x32_bf16 v[208:211], v[120:123], v[96:99], v[208:211]\n"
    "ds_read_b64_tr_b16 v[116:117], v236 offset:9472\n"
    "v_mfma_f32_16x16x32_bf16 v[212:215], v[120:123], v[100:103], v[212:215]\n"
    "ds_read_b64_tr_b16 v[118:119], v236 offset:9728\n"
    "v_mfma_f32_16x16x32_bf16 v[88:91], v[120:123], v[92:95], v[88:91]\n"
    "s_waitcnt vmcnt(9)\n"
    "s_barrier\n"
    "s_waitcnt lgkmcnt(8)\n"
    "v_mfma_f32_16x16x32_bf16 v[144:147], v[124:127], v[104:107], v[144:147]\n"
    "s_add_u32 m0, s52, 105984\n"
    "ds_read_b64_tr_b16 v[120:121], v237 offset:9472\n"
    "v_mfma_f32_16x16x32_bf16 v[148:151], v[124:127], v[108:111], v[148:151]\n"
    "s_add_u32 s51, %8, 1548288\n"
    "ds_read_b64_tr_b16 v[122:123], v237 offset:9728\n"
    "v_mfma_f32_16x16x32_bf16 v[80:83], v[124:127], v[92:95], v[80:83]\n"
    "buffer_load_dwordx4 v246, s[44:47], s51 offen lds\n"
    "s_waitcnt lgkmcnt(8)\n"
    "v_mfma_f32_16x16x32_bf16 v[152:155], v[128:131], v[104:107], v[152:155]\n"
    "s_add_u32 m0, s56, 124928\n"
    "ds_read_b64_tr_b16 v[124:125], v238 offset:9472\n"
    "v_mfma_f32_16x16x32_bf16 v[156:159], v[128:131], v[108:111], v[156:159]\n"
    "s_add_u32 s48, %7, 896\n"
    "ds_read_b64_tr_b16 v[126:127], v238 offset:9728\n"
    "v_mfma_f32_16x16x32_bf16 v[84:87], v[128:131], v[92:95], v[84:87]\n"
    "buffer_load_dwordx4 v243, s[40:43], s48 offen lds\n"
    "s_waitcnt lgkmcnt(8)\n"
    "v_mfma_f32_16x16x32_bf16 v[160:163], v[132:135], v[104:107], v[160:163]\n"
    "s_add_u32 m0, s52, 107040\n"
    "ds_read_b64_tr_b16 v[128:129], v239 offset:9472\n"
    "v_mfma_f32_16x16x32_bf16 v[164:167], v[132:135], v[108:111], v[164:167]\n"
    "s_add_u32 s49, %8, 1551744\n"
    "ds_read_b64_tr_b16 v[130:131], v239 offset:9728\n"
    "v_mfma_f32_16x16x32_bf16 v[216:219], v[92:95], v[104:107], v[216:219]\n"
    "buffer_load_dwordx4 v246, s[44:47], s49 offen lds\n"
    "s_waitcnt lgkmcnt(8)\n"
    "v_mfma_f32_16x16x32_bf16 v[168:171], v[112:115], v[104:107], v[168:171]\n"
    "s_add_u32 m0, s56, 125952\n"
    "ds_read_b64_tr_b16 v[132:133], v240 offset:9472\n"
    "v_mfma_f32_16x16x32_bf16 v[172:175], v[112:115], v[108:111], v[172:175]\n"
    "s_add_u32 s50, %7, 10112\n"
    "ds_read_b64_tr_b16 v[134:135], v240 offset:9728\n"
    "v_mfma_f32_16x16x32_bf16 v[220:223], v[92:95], v[108:111], v[220:223]\n"
    "ds_read_b128 v[96:99], v241 offset:35328\n"
    "s_waitcnt lgkmcnt(9)\n"
    "v_mfma_f32_16x16x32_bf16 v[176:179], v[116:119], v[104:107], v[176:179]\n"
    "ds_read_b128 v[100:103], v241 offset:37376\n"
    "v_mfma_f32_16x16x32_bf16 v[180:183], v[116:119], v[108:111], v[180:183]\n"
    "ds_read_b64_tr_b16 v[112:113], v232 offset:35328\n"
    "v_mfma_f32_16x16x32_bf16 v[224:227], v[104:107], v[104:107], v[224:227]\n"
    "ds_read_b64_tr_b16 v[114:115], v232 offset:35584\n"
    "s_waitcnt lgkmcnt(10)\n"
    "v_mfma_f32_16x16x32_bf16 v[184:187], v[120:123], v[104:107], v[184:187]\n"
    "ds_read_b64_tr_b16 v[116:117], v233 offset:35328\n"
    "v_mfma_f32_16x16x32_bf16 v[188:191], v[120:123], v[108:111], v[188:191]\n"
    "ds_read_b64_tr_b16 v[118:119], v233 offset:35584\n"
    "v_mfma_f32_16x16x32_bf16 v[228:231], v[108:111], v[108:111], v[228:231]\n"
    "buffer_load_dwordx4 v243, s[40:43], s50 offen lds\n"
    "s_waitcnt lgkmcnt(10)\n"
    "v_mfma_f32_16x16x32_bf16 v[192:195], v[124:127], v[104:107], v[192:195]\n"
    "s_add_u32 m0, s52, 115456\n"
    "ds_read_b64_tr_b16 v[120:121], v234 offset:35328\n"
    "v_mfma_f32_16x16x32_bf16 v[196:199], v[124:127], v[108:111], v[196:199]\n"
    "s_add_u32 s51, %8, 1658880\n"
    "ds_read_b64_tr_b16 v[122:123], v234 offset:35584\n"
    "s_waitcnt lgkmcnt(10)\n"
    "v_mfma_f32_16x16x32_bf16 v[200:203], v[128:131], v[104:107], v[200:203]\n"
    "ds_read_b64_tr_b16 v[124:125], v235 offset:35328\n"
    "v_mfma_f32_16x16x32_bf16 v[204:207], v[128:131], v[108:111], v[204:207]\n"
    "ds_read_b64_tr_b16 v[126:127], v235 offset:35584\n"
    "s_waitcnt lgkmcnt(10)\n"
    "v_mfma_f32_16x16x32_bf16 v[208:211], v[132:135], v[104:107], v[208:211]\n"
    "ds_read_b64_tr_b16 v[128:129], v236 offset:35328\n"
    "v_mfma_f32_16x16x32_bf16 v[212:215], v[132:135], v[108:111], v[212:215]\n"
    "ds_read_b64_tr_b16 v[130:131], v236 offset:35584\n"
    "v_mfma_f32_16x16x32_bf16 v[88:91], v[132:135], v[92:95], v[88:91]\n"
    "buffer_load_dwordx4 v246, s[44:47], s51 offen lds\n"
    "s_waitcnt lgkmcnt(8)\n"
    "v_mfma_f32_16x16x32_bf16 v[144:147], v[112:115], v[96:99], v[144:147]\n"
    "s_add_u32 m0, s56, 126976\n"
    "ds_read_b64_tr_b16 v[132:133], v237 offset:35328\n"
    "v_mfma_f32_16x16x32_bf16 v[148:151], v[112:115], v[100:103], v[148:151]\n"
    "s_add_u32 s48, %7, 19328\n"
    "ds_read_b64_tr_b16 v[134:135], v237 offset:35584\n"
    "v_mfma_f32_16x16x32_bf16 v[80:83], v[112:115], v[92:95], v[80:83]\n"
    "buffer_load_dwordx4 v243, s[40:43], s48 offen lds\n"
    "s_waitcnt lgkmcnt(8)\n"
    "v_mfma_f32_16x16x32_bf16 v[152:155], v[116:119], v[96:99], v[152:155]\n"
    "s_add_u32 m0, s52, 116512\n"
    "ds_read_b64_tr_b16 v[112:113], v238 offset:35328\n"
    "v_mfma_f32_16x16x32_bf16 v[156:159], v[116:119], v[100:103], v[156:159]\n"
    "s_add_u32 s49, %8, 1662336\n"
    "ds_read_b64_tr_b16 v[114:115], v238 offset:35584\n"
    "v_mfma_f32_16x16x32_bf16 v[84:87], v[116:119], v[92:95], v[84:87]\n"
    "buffer_load_dwordx4 v246, s[44:47], s49 offen lds\n"
    "s_waitcnt lgkmcnt(8)\n"
    "v_mfma_f32_16x16x32_bf16 v[160:163], v[120:123], v[96:99], v[160:163]\n"
    "s_add_u32 m0, s56, 128000\n"
    "ds_read_b64_tr_b16 v[116:117], v239 offset:35328\n"
    "v_mfma_f32_16x16x32_bf16 v[164:167], v[120:123], v[100:103], v[164:167]\n"
    "s_add_u32 s50, %7, 28544\n"
    "ds_read_b64_tr_b16 v[118:119], v239 offset:35584\n"
    "v_mfma_f32_16x16x32_bf16 v[216:219], v[92:95], v[96:99], v[216:219]\n"
    "buffer_load_dwordx4 v243, s[40:43], s50 offen lds\n"
    "s_waitcnt lgkmcnt(8)\n"
    "v_mfma_f32_16x16x32_bf16 v[168:171], v[124:127], v[96:99], v[168:171]\n"
    "ds_read_b64_tr_b16 v[120:121], v240 offset:35328\n"
    "v_mfma_f32_16x16x32_bf16 v[172:175], v[124:127], v[100:103], v[172:175]\n"
    "ds_read_b64_tr_b16 v[122:123], v240 offset:35584\n"
    "v_mfma_f32_16x16x32_bf16 v[220:223], v[92:95], v[100:103], v[220:223]\n"
    "ds_read_b128 v[104:107], v242 offset:35328\n"
    "s_waitcnt lgkmcnt(9)\n"
    "v_mfma_f32_16x16x32_bf16 v[176:179], v[128:131], v[96:99], v[176:179]\n"
    "ds_read_b128 v[108:111], v242 offset:37376\n"
    "v_mfma_f32_16x16x32_bf16 v[180:183], v[128:131], v[100:103], v[180:183]\n"
    "ds_read_b64_tr_b16 v[124:125], v232 offset:44800\n"
    "v_mfma_f32_16x16x32_bf16 v[224:227], v[96:99], v[96:99], v[224:227]\n"
    "ds_read_b64_tr_b16 v[126:127], v232 offset:45056\n"
    "s_waitcnt lgkmcnt(10)\n"
    "v_mfma_f32_16x16x32_bf16 v[184:187], v[132:135], v[96:99], v[184:187]\n"
    "ds_read_b64_tr_b16 v[128:129], v233 offset:44800\n"
    "v_mfma_f32_16x16x32_bf16 v[188:191], v[132:135], v[100:103], v[188:191]\n"
    "ds_read_b64_tr_b16 v[130:131], v233 offset:45056\n"
    "v_mfma_f32_16x16x32_bf16 v[228:231], v[100:103], v[100:103], v[228:231]\n"
    "s_add_u32 m0, s55, 114432\n"
    "s_add_u32 s51, %9, 1548288\n"
    "s_mov_b32 exec_hi, 0\n"
    "buffer_load_dwordx4 v247, s[44:47], s51 offen lds\n"
    "s_mov_b32 exec_hi, -1\n"
    "s_waitcnt lgkmcnt(10)\n"
    "v_mfma_f32_16x16x32_bf16 v[192:195], v[112:115], v[96:99], v[192:195]\n"
    "ds_read_b64_tr_b16 v[132:133], v234 offset:44800\n"
    "v_mfma_f32_16x16x32_bf16 v[196:199], v[112:115], v[100:103], v[196:199]\n"
    "ds_read_b64_tr_b16 v[134:135], v234 offset:45056\n"
    "s_waitcnt lgkmcnt(10)\n"
    "v_mfma_f32_16x16x32_bf16 v[200:203], v[116:119], v[96:99], v[200:203]\n"
    "ds_read_b64_tr_b16 v[112:113], v235 offset:44800\n"
    "v_mfma_f32_16x16x32_bf16 v[204:207], v[116:119], v[100:103], v[204:207]\n"
    "ds_read_b64_tr_b16 v[114:115], v235 offset:45056\n"
    "s_waitcnt lgkmcnt(10)\n"
    "v_mfma_f32_16x16x32_bf16 v[208:211], v[120:123], v[96:99], v[208:211]\n"
    "ds_read_b64_tr_b16 v[116:117], v236 offset:44800\n"
    "v_mfma_f32_16x16x32_bf16 v[212:215], v[120:123], v[100:103], v[212:215]\n"
    "ds_read_b64_tr_b16 v[118:119], v236 offset:45056\n"
    "v_mfma_f32_16x16x32_bf16 v[88:91], v[120:123], v[92:95], v[88:91]\n"
    "s_waitcnt vmcnt(9)\n"
    "s_barrier\n"
    "s_waitcnt lgkmcnt(8)\n"
    "v_mfma_f32_16x16x32_bf16 v[144:147], v[124:127], v[104:107], v[144:147]\n"
    "s_add_u32 m0, s52, 0\n"
    "ds_read_b64_tr_b16 v[120:121], v237 offset:44800\n"
    "v_mfma_f32_16x16x32_bf16 v[148:151], v[124:127], v[108:111], v[148:151]\n"
    "s_add_u32 s48, %8, 1769472\n"
    "ds_read_b64_tr_b16 v[122:123], v237 offset:45056\n"
    "v_mfma_f32_16x16x32_bf16 v[80:83], v[124:127], v[92:95], v[80:83]\n"
    "buffer_load_dwordx4 v246, s[44:47], s48 offen lds\n"
    "s_waitcnt lgkmcnt(8)\n"
    "v_mfma_f32_16x16x32_bf16 v[152:155], v[128:131], v[104:107], v[152:155]\n"
    "s_add_u32 m0, s56, 18944\n"
    "ds_read_b64_tr_b16 v[124:125], v238 offset:44800\n"
    "v_mfma_f32_16x16x32_bf16 v[156:159], v[128:131], v[108:111], v[156:159]\n"
    "s_add_u32 s49, %7, 1024\n"
    "ds_read_b64_tr_b16 v[126:127], v238 offset:45056\n"
    "v_mfma_f32_16x16x32_bf16 v[84:87], v[128:131], v[92:95], v[84:87]\n"
    "buffer_load_dwordx4 v243, s[40:43], s49 offen lds\n"
    "s_waitcnt lgkmcnt(8)\n"
    "v_mfma_f32_16x16x32_bf16 v[160:163], v[132:135], v[104:107], v[160:163]\n"
    "s_add_u32 m0, s52, 1056\n"
    "ds_read_b64_tr_b16 v[128:129], v239 offset:44800\n"
    "v_mfma_f32_16x16x32_bf16 v[164:167], v[132:135], v[108:111], v[164:167]\n"
    "s_add_u32 s50, %8, 1772928\n"
    "ds_read_b64_tr_b16 v[130:131], v239 offset:45056\n"
    "v_mfma_f32_16x16x32_bf16 v[216:219], v[92:95], v[104:107], v[216:219]\n"
    "buffer_load_dwordx4 v246, s[44:47], s50 offen lds\n"
    "s_waitcnt lgkmcnt(8)\n"
    "v_mfma_f32_16x16x32_bf16 v[168:171], v[112:115], v[104:107], v[168:171]\n"
    "s_add_u32 m0, s56, 19968\n"
    "ds_read_b64_tr_b16 v[132:133], v240 offset:44800\n"
    "v_mfma_f32_16x16x32_bf16 v[172:175], v[112:115], v[108:111], v[172:175]\n"
    "s_add_u32 s51, %7, 10240\n"
    "ds_read_b64_tr_b16 v[134:135], v240 offset:45056\n"
    "v_mfma_f32_16x16x32_bf16 v[220:223], v[92:95], v[108:111], v[220:223]\n"
    "ds_read_b128 v[96:99], v69 offset:0\n"
    "s_waitcnt lgkmcnt(9)\n"
    "v_mfma_f32_16x16x32_bf16 v[176:179], v[116:119], v[104:107], v[176:179]\n"
    "ds_read_b128 v[100:103], v69 offset:2048\n"
    "v_mfma_f32_16x16x32_bf16 v[180:183], v[116:119], v[108:111], v[180:183]\n"
    "ds_read_b64_tr_b16 v[112:113], v60 offset:0\n"
    "v_mfma_f32_16x16x32_bf16 v[224:227], v[104:107], v[104:107], v[224:227]\n"
    "ds_read_b64_tr_b16 v[114:115], v60 offset:256\n"
    "s_waitcnt lgkmcnt(10)\n"
    "v_mfma_f32_16x16x32_bf16 v[184:187], v[120:123], v[104:107], v[184:187]\n"
    "ds_read_b64_tr_b16 v[116:117], v61 offset:0\n"
    "v_mfma_f32_16x16x32_bf16 v[188:191], v[120:123], v[108:111], v[188:191]\n"
    "ds_read_b64_tr_b16 v[118:119], v61 offset:256\n"
    "v_mfma_f32_16x16x32_bf16 v[228:231], v[108:111], v[108:111], v[228:231]\n"
    "buffer_load_dwordx4 v243, s[40:43], s51 offen lds\n"
    "s_waitcnt lgkmcnt(10)\n"
    "v_mfma_f32_16x16x32_bf16 v[192:195], v[124:127], v[104:107], v[192:195]\n"
    "s_add_u32 m0, s52, 9472\n"
    "ds_read_b64_tr_b16 v[120:121], v62 offset:0\n"
    "v_mfma_f32_16x16x32_bf16 v[196:199], v[124:127], v[108:111], v[196:199]\n"
    "s_add_u32 s48, %8, 1880064\n"
    "ds_read_b64_tr_b16 v[122:123], v62 offset:256\n"
    "s_waitcnt lgkmcnt(10)\n"
    "v_mfma_f32_16x16x32_bf16 v[200:203], v[128:131], v[104:107], v[200:203]\n"
    "ds_read_b64_tr_b16 v[124:125], v63 offset:0\n"
    "v_mfma_f32_16x16x32_bf16 v[204:207], v[128:131], v[108:111], v[204:207]\n"
    "ds_read_b64_tr_b16 v[126:127], v63 offset:256\n"
    "s_waitcnt lgkmcnt(10)\n"
    "v_mfma_f32_16x16x32_bf16 v[208:211], v[132:135], v[104:107], v[208:211]\n"
    "ds_read_b64_tr_b16 v[128:129], v64 offset:0\n"
    "v_mfma_f32_16x16x32_bf16 v[212:215], v[132:135], v[108:111], v[212:215]\n"
    "ds_read_b64_tr_b16 v[130:131], v64 offset:256\n"
    "v_mfma_f32_16x16x32_bf16 v[88:91], v[132:135], v[92:95], v[88:91]\n"
    "buffer_load_dwordx4 v246, s[44:47], s48 offen lds\n"
    "s_waitcnt lgkmcnt(8)\n"
    "v_mfma_f32_16x16x32_bf16 v[144:147], v[112:115], v[96:99], v[144:147]\n"
    "s_add_u32 m0, s56, 20992\n"
    "ds_read_b64_tr_b16 v[132:133], v65 offset:0\n"
    "v_mfma_f32_16x16x32_bf16 v[148:151], v[112:115], v[100:103], v[148:151]\n"
    "s_add_u32 s49, %7, 19456\n"
    "ds_read_b64_tr_b16 v[134:135], v65 offset:256\n"
    "v_mfma_f32_16x16x32_bf16 v[80:83], v[112:115], v[92:95], v[80:83]\n"
    "buffer_load_dwordx4 v243, s[40:43], s49 offen lds\n"
    "s_waitcnt lgkmcnt(8)\n"
    "v_mfma_f32_16x16x32_bf16 v[152:155], v[116:119], v[96:99], v[152:155]\n"
    "s_add_u32 m0, s52, 10528\n"
    "ds_read_b64_tr_b16 v[112:113], v66 offset:0\n"
    "v_mfma_f32_16x16x32_bf16 v[156:159], v[116:119], v[100:103], v[156:159]\n"
    "s_add_u32 s50, %8, 1883520\n"
    "ds_read_b64_tr_b16 v[114:115], v66 offset:256\n"
    "v_mfma_f32_16x16x32_bf16 v[84:87], v[116:119], v[92:95], v[84:87]\n"
    "buffer_load_dwordx4 v246, s[44:47], s50 offen lds\n"
    "s_waitcnt lgkmcnt(8)\n"
    "v_mfma_f32_16x16x32_bf16 v[160:163], v[120:123], v[96:99], v[160:163]\n"
    "s_add_u32 m0, s56, 22016\n"
    "ds_read_b64_tr_b16 v[116:117], v67 offset:0\n"
    "v_mfma_f32_16x16x32_bf16 v[164:167], v[120:123], v[100:103], v[164:167]\n"
    "s_add_u32 s51, %7, 28672\n"
    "ds_read_b64_tr_b16 v[118:119], v67 offset:256\n"
    "v_mfma_f32_16x16x32_bf16 v[216:219], v[92:95], v[96:99], v[216:219]\n"
    "buffer_load_dwordx4 v243, s[40:43], s51 offen lds\n"
    "s_waitcnt lgkmcnt(8)\n"
    "v_mfma_f32_16x16x32_bf16 v[168:171], v[124:127], v[96:99], v[168:171]\n"
    "ds_read_b64_tr_b16 v[120:121], v68 offset:0\n"
    "v_mfma_f32_16x16x32_bf16 v[172:175], v[124:127], v[100:103], v[172:175]\n"
    "ds_read_b64_tr_b16 v[122:123], v68 offset:256\n"
    "v_mfma_f32_16x16x32_bf16 v[220:223], v[92:95], v[100:103], v[220:223]\n"
    "ds_read_b128 v[104:107], v70 offset:0\n"
    "s_waitcnt lgkmcnt(9)\n"
    "v_mfma_f32_16x16x32_bf16 v[176:179], v[128:131], v[96:99], v[176:179]\n"
    "ds_read_b128 v[108:111], v70 offset:2048\n"
    "v_mfma_f32_16x16x32_bf16 v[180:183], v[128:131], v[100:103], v[180:183]\n"
    "ds_read_b64_tr_b16 v[124:125], v60 offset:9472\n"
    "v_mfma_f32_16x16x32_bf16 v[224:227], v[96:99], v[96:99], v[224:227]\n"
    "ds_read_b64_tr_b16 v[126:127], v60 offset:9728\n"
    "s_waitcnt lgkmcnt(10)\n"
    "v_mfma_f32_16x16x32_bf16 v[184:187], v[132:135], v[96:99], v[184:187]\n"
    "ds_read_b64_tr_b16 v[128:129], v61 offset:9472\n"
    "v_mfma_f32_16x16x32_bf16 v[188:191], v[132:135], v[100:103], v[188:191]\n"
    "ds_read_b64_tr_b16 v[130:131], v61 offset:9728\n"
    "v_mfma_f32_16x16x32_bf16 v[228:231], v[100:103], v[100:103], v[228:231]\n"
    "s_add_u32 m0, s55, 8448\n"
    "s_add_u32 s48, %9, 1769472\n"
    "s_mov_b32 exec_hi, 0\n"
    "buffer_load_dwordx4 v247, s[44:47], s48 offen lds\n"
    "s_mov_b32 exec_hi, -1\n"
    "s_waitcnt lgkmcnt(10)\n"
    "v_mfma_f32_16x16x32_bf16 v[192:195], v[112:115], v[96:99], v[192:195]\n"
    "ds_read_b64_tr_b16 v[132:133], v62 offset:9472\n"
    "v_mfma_f32_16x16x32_bf16 v[196:199], v[112:115], v[100:103], v[196:199]\n"
    "ds_read_b64_tr_b16 v[134:135], v62 offset:9728\n"
    "s_waitcnt lgkmcnt(10)\n"
    "v_mfma_f32_16x16x32_bf16 v[200:203], v[116:119], v[96:99], v[200:203]\n"
    "ds_read_b64_tr_b16 v[112:113], v63 offset:9472\n"
    "v_mfma_f32_16x16x32_bf16 v[204:207], v[116:119], v[100:103], v[204:207]\n"
    "ds_read_b64_tr_b16 v[114:115], v63 offset:9728\n"
    "s_waitcnt lgkmcnt(10)\n"
    "v_mfma_f32_16x16x32_bf16 v[208:211], v[120:123], v[96:99], v[208:211]\n"
    "ds_read_b64_tr_b16 v[116:117], v64 offset:9472\n"
    "v_mfma_f32_16x16x32_bf16 v[212:215], v[120:123], v[100:103], v[212:215]\n"
    "ds_read_b64_tr_b16 v[118:119], v64 offset:9728\n"
    "v_mfma_f32_16x16x32_bf16 v[88:91], v[120:123], v[92:95], v[88:91]\n"
    "s_waitcnt vmcnt(9)\n"
    "s_barrier\n"
    "s_waitcnt lgkmcnt(8)\n"
    "v_mfma_f32_16x16x32_bf16 v[144:147], v[124:127], v[104:107], v[144:147]\n"
    "ds_read_b64_tr_b16 v[120:121], v65 offset:9472\n"
    "v_mfma_f32_16x16x32_bf16 v[148:151], v[124:127], v[108:111], v[148:151]\n"
    "ds_read_b64_tr_b16 v[122:123], v65 offset:9728\n"
    "v_mfma_f32_16x16x32_bf16 v[80:83], v[124:127], v[92:95], v[80:83]\n"
    "s_waitcnt lgkmcnt(8)\n"
    "v_mfma_f32_16x16x32_bf16 v[152:155], v[128:131], v[104:107], v[152:155]\n"
    "ds_read_b64_tr_b16 v[124:125], v66 offset:9472\n"
    "v_mfma_f32_16x16x32_bf16 v[156:159], v[128:131], v[108:111], v[156:159]\n"
    "ds_read_b64_tr_b16 v[126:127], v66 offset:9728\n"
    "v_mfma_f32_16x16x32_bf16 v[84:87], v[128:131], v[92:95], v[84:87]\n"
    "s_waitcnt lgkmcnt(8)\n"
    "v_mfma_f32_16x16x32_bf16 v[160:163], v[132:135], v[104:107], v[160:163]\n"
    "ds_read_b64_tr_b16 v[128:129], v67 offset:9472\n"
    "v_mfma_f32_16x16x32_bf16 v[164:167], v[132:135], v[108:111], v[164:167]\n"
    "ds_read_b64_tr_b16 v[130:131], v67 offset:9728\n"
    "v_mfma_f32_16x16x32_bf16 v[216:219], v[92:95], v[104:107], v[216:219]\n"
    "s_waitcnt lgkmcnt(8)\n"
    "v_mfma_f32_16x16x32_bf16 v[168:171], v[112:115], v[104:107], v[168:171]\n"
    "ds_read_b64_tr_b16 v[132:133], v68 offset:9472\n"
    "v_mfma_f32_16x16x32_bf16 v[172:175], v[112:115], v[108:111], v[172:175]\n"
    "ds_read_b64_tr_b16 v[134:135], v68 offset:9728\n"
    "v_mfma_f32_16x16x32_bf16 v[220:223], v[92:95], v[108:111], v[220:223]\n"
    "ds_read_b128 v[96:99], v69 offset:35328\n"
    "s_waitcnt lgkmcnt(9)\n"
    "v_mfma_f32_16x16x32_bf16 v[176:179], v[116:119], v[104:107], v[176:179]\n"
    "ds_read_b128 v[100:103], v69 offset:37376\n"
    "v_mfma_f32_16x16x32_bf16 v[180:183], v[116:119], v[108:111], v[180:183]\n"
    "ds_read_b64_tr_b16 v[112:113], v60 offset:35328\n"
    "v_mfma_f32_16x16x32_bf16 v[224:227], v[104:107], v[104:107], v[224:227]\n"
    "ds_read_b64_tr_b16 v[114:115], v60 offset:35584\n"
    "s_waitcnt lgkmcnt(10)\n"
    "v_mfma_f32_16x16x32_bf16 v[184:187], v[120:123], v[104:107], v[184:187]\n"
    "ds_read_b64_tr_b16 v[116:117], v61 offset:35328\n"
    "v_mfma_f32_16x16x32_bf16 v[188:191], v[120:123], v[108:111], v[188:191]\n"
    "ds_read_b64_tr_b16 v[118:119], v61 offset:35584\n"
    "v_mfma_f32_16x16x32_bf16 v[228:231], v[108:111], v[108:111], v[228:231]\n"
    "s_waitcnt lgkmcnt(10)\n"
    "v_mfma_f32_16x16x32_bf16 v[192:195], v[124:127], v[104:107], v[192:195]\n"
    "ds_read_b64_tr_b16 v[120:121], v62 offset:35328\n"
    "v_mfma_f32_16x16x32_bf16 v[196:199], v[124:127], v[108:111], v[196:199]\n"
    "ds_read_b64_tr_b16 v[122:123], v62 offset:35584\n"
    "s_waitcnt lgkmcnt(10)\n"
    "v_mfma_f32_16x16x32_bf16 v[200:203], v[128:131], v[104:107], v[200:203]\n"
    "ds_read_b64_tr_b16 v[124:125], v63 offset:35328\n"
    "v_mfma_f32_16x16x32_bf16 v[204:207], v[128:131], v[108:111], v[204:207]\n"
    "ds_read_b64_tr_b16 v[126:127], v63 offset:35584\n"
    "s_waitcnt lgkmcnt(10)\n"
    "v_mfma_f32_16x16x32_bf16 v[208:211], v[132:135], v[104:107], v[208:211]\n"
    "ds_read_b64_tr_b16 v[128:129], v64 offset:35328\n"
    "v_mfma_f32_16x16x32_bf16 v[212:215], v[132:135], v[108:111], v[212:215]\n"
    "ds_read_b64_tr_b16 v[130:131], v64 offset:35584\n"
    "v_mfma_f32_16x16x32_bf16 v[88:91], v[132:135], v[92:95], v[88:91]\n"
    "s_waitcnt lgkmcnt(8)\n"
    "v_mfma_f32_16x16x32_bf16 v[144:147], v[112:115], v[96:99], v[144:147]\n"
    "ds_read_b64_tr_b16 v[132:133], v65 offset:35328\n"
    "v_mfma_f32_16x16x32_bf16 v[148:151], v[112:115], v[100:103], v[148:151]\n"
    "ds_read_b64_tr_b16 v[134:135], v65 offset:35584\n"
    "v_mfma_f32_16x16x32_bf16 v[80:83], v[112:115], v[92:95], v[80:83]\n"
    "s_waitcnt lgkmcnt(8)\n"
    "v_mfma_f32_16x16x32_bf16 v[152:155], v[116:119], v[96:99], v[152:155]\n"
    "ds_read_b64_tr_b16 v[112:113], v66 offset:35328\n"
    "v_mfma_f32_16x16x32_bf16 v[156:159], v[116:119], v[100:103], v[156:159]\n"
    "ds_read_b64_tr_b16 v[114:115], v66 offset:35584\n"
    "v_mfma_f32_16x16x32_bf16 v[84:87], v[116:119], v[92:95], v[84:87]\n"
    "s_waitcnt lgkmcnt(8)\n"
    "v_mfma_f32_16x16x32_bf16 v[160:163], v[120:123], v[96:99], v[160:163]\n"
    "ds_read_b64_tr_b16 v[116:117], v67 offset:35328\n"
    "v_mfma_f32_16x16x32_bf16 v[164:167], v[120:123], v[100:103], v[164:167]\n"
    "ds_read_b64_tr_b16 v[118:119], v67 offset:35584\n"
    "v_mfma_f32_16x16x32_bf16 v[216:219], v[92:95], v[96:99], v[216:219]\n"
    "s_waitcnt lgkmcnt(8)\n"
    "v_mfma_f32_16x16x32_bf16 v[168:171], v[124:127], v[96:99], v[168:171]\n"
    "ds_read_b64_tr_b16 v[120:121], v68 offset:35328\n"
    "v_mfma_f32_16x16x32_bf16 v[172:175], v[124:127], v[100:103], v[172:175]\n"
    "ds_read_b64_tr_b16 v[122:123], v68 offset:35584\n"
    "v_mfma_f32_16x16x32_bf16 v[220:223], v[92:95], v[100:103], v[220:223]\n"
    "ds_read_b128 v[104:107], v70 offset:35328\n"
    "s_waitcnt lgkmcnt(9)\n"
    "v_mfma_f32_16x16x32_bf16 v[176:179], v[128:131], v[96:99], v[176:179]\n"
    "ds_read_b128 v[108:111], v70 offset:37376\n"
    "v_mfma_f32_16x16x32_bf16 v[180:183], v[128:131], v[100:103], v[180:183]\n"
    "ds_read_b64_tr_b16 v[124:125], v60 offset:44800\n"
    "v_mfma_f32_16x16x32_bf16 v[224:227], v[96:99], v[96:99], v[224:227]\n"
    "ds_read_b64_tr_b16 v[126:127], v60 offset:45056\n"
    "s_waitcnt lgkmcnt(10)\n"
    "v_mfma_f32_16x16x32_bf16 v[184:187], v[132:135], v[96:99], v[184:187]\n"
    "ds_read_b64_tr_b16 v[128:129], v61 offset:44800\n"
    "v_mfma_f32_16x16x32_bf16 v[188:191], v[132:135], v[100:103], v[188:191]\n"
    "ds_read_b64_tr_b16 v[130:131], v61 offset:45056\n"
    "v_mfma_f32_16x16x32_bf16 v[228:231], v[100:103], v[100:103], v[228:231]\n"
    "s_waitcnt lgkmcnt(10)\n"
    "v_mfma_f32_16x16x32_bf16 v[192:195], v[112:115], v[96:99], v[192:195]\n"
    "ds_read_b64_tr_b16 v[132:133], v62 offset:44800\n"
    "v_mfma_f32_16x16x32_bf16 v[196:199], v[112:115], v[100:103], v[196:199]\n"
    "ds_read_b64_tr_b16 v[134:135], v62 offset:45056\n"
    "s_waitcnt lgkmcnt(10)\n"
    "v_mfma_f32_16x16x32_bf16 v[200:203], v[116:119], v[96:99], v[200:203]\n"
    "ds_read_b64_tr_b16 v[112:113], v63 offset:44800\n"
    "v_mfma_f32_16x16x32_bf16 v[204:207], v[116:119], v[100:103], v[204:207]\n"
    "ds_read_b64_tr_b16 v[114:115], v63 offset:45056\n"
    "s_waitcnt lgkmcnt(10)\n"
    "v_mfma_f32_16x16x32_bf16 v[208:211], v[120:123], v[96:99], v[208:211]\n"
    "ds_read_b64_tr_b16 v[116:117], v64 offset:44800\n"
    "v_mfma_f32_16x16x32_bf16 v[212:215], v[120:123], v[100:103], v[212:215]\n"
    "ds_read_b64_tr_b16 v[118:119], v64 offset:45056\n"
    "v_mfma_f32_16x16x32_bf16 v[88:91], v[120:123], v[92:95], v[88:91]\n"
    "s_waitcnt vmcnt(0)\n"
    "s_barrier\n"
    "s_cmp_ge_u32 %6, 4\n"
    "s_cbranch_scc1 .Llp0_%=\n"
    "global_load_dword v244, v245, %19\n"
    "s_nop 2\n"
    ".Llp0_%=:\n"
    "s_waitcnt lgkmcnt(8)\n"
    "v_mfma_f32_16x16x32_bf16 v[144:147], v[124:127], v[104:107], v[144:147]\n"
    "ds_read_b64_tr_b16 v[120:121], v65 offset:44800\n"
    "v_mfma_f32_16x16x32_bf16 v[148:151], v[124:127], v[108:111], v[148:151]\n"
    "ds_read_b64_tr_b16 v[122:123], v65 offset:45056\n"
    "v_mfma_f32_16x16x32_bf16 v[80:83], v[124:127], v[92:95], v[80:83]\n"
    "s_waitcnt lgkmcnt(8)\n"
    "v_mfma_f32_16x16x32_bf16 v[152:155], v[128:131], v[104:107], v[152:155]\n"
    "ds_read_b64_tr_b16 v[124:125], v66 offset:44800\n"
    "v_mfma_f32_16x16x32_bf16 v[156:159], v[128:131], v[108:111], v[156:159]\n"
    "ds_read_b64_tr_b16 v[126:127], v66 offset:45056\n"
    "v_mfma_f32_16x16x32_bf16 v[84:87], v[128:131], v[92:95], v[84:87]\n"
    "s_waitcnt lgkmcnt(8)\n"
    "v_mfma_f32_16x16x32_bf16 v[160:163], v[132:135], v[104:107], v[160:163]\n"
    "ds_read_b64_tr_b16 v[128:129], v67 offset:44800\n"
    "v_mfma_f32_16x16x32_bf16 v[164:167], v[132:135], v[108:111], v[164:167]\n"
    "ds_read_b64_tr_b16 v[130:131], v67 offset:45056\n"
    "v_mfma_f32_16x16x32_bf16 v[216:219], v[92:95], v[104:107], v[216:219]\n"
    "s_waitcnt lgkmcnt(8)\n"
    "v_mfma_f32_16x16x32_bf16 v[168:171], v[112:115], v[104:107], v[168:171]\n"
    "ds_read_b64_tr_b16 v[132:133], v68 offset:44800\n"
    "v_mfma_f32_16x16x32_bf16 v[172:175], v[112:115], v[108:111], v[172:175]\n"
    "ds_read_b64_tr_b16 v[134:135], v68 offset:45056\n"
    "v_mfma_f32_16x16x32_bf16 v[220:223], v[92:95], v[108:111], v[220:223]\n"
    "ds_read_b128 v[96:99], v241 offset:0\n"
    "s_waitcnt lgkmcnt(9)\n"
    "v_mfma_f32_16x16x32_bf16 v[176:179], v[116:119], v[104:107], v[176:179]\n"
    "ds_read_b128 v[100:103], v241 offset:2048\n"
    "v_mfma_f32_16x16x32_bf16 v[180:183], v[116:119], v[108:111], v[180:183]\n"
    "ds_read_b64_tr_b16 v[112:113], v232 offset:0\n"
    "v_mfma_f32_16x16x32_bf16 v[224:227], v[104:107], v[104:107], v[224:227]\n"
    "ds_read_b64_tr_b16 v[114:115], v232 offset:256\n"
    "s_waitcnt lgkmcnt(10)\n"
    "v_mfma_f32_16x16x32_bf16 v[184:187], v[120:123], v[104:107], v[184:187]\n"
    "ds_read_b64_tr_b16 v[116:117], v233 offset:0\n"
    "v_mfma_f32_16x16x32_bf16 v[188:191], v[120:123], v[108:111], v[188:191]\n"
    "ds_read_b64_tr_b16 v[118:119], v233 offset:256\n"
    "v_mfma_f32_16x16x32_bf16 v[228:231], v[108:111], v[108:111], v[228:231]\n"
    "s_waitcnt lgkmcnt(10)\n"
    "v_mfma_f32_16x16x32_bf16 v[192:195], v[124:127], v[104:107], v[192:195]\n"
    "ds_read_b64_tr_b16 v[120:121], v234 offset:0\n"
    "v_mfma_f32_16x16x32_bf16 v[196:199], v[124:127], v[108:111], v[196:199]\n"
    "ds_read_b64_tr_b16 v[122:123], v234 offset:256\n"
    "s_waitcnt lgkmcnt(10)\n"
    "v_mfma_f32_16x16x32_bf16 v[200:203], v[128:131], v[104:107], v[200:203]\n"
    "ds_read_b64_tr_b16 v[124:125], v235 offset:0\n"
    "v_mfma_f32_16x16x32_bf16 v[204:207], v[128:131], v[108:111], v[204:207]\n"
    "ds_read_b64_tr_b16 v[126:127], v235 offset:256\n"
    "s_waitcnt lgkmcnt(10)\n"
    "v_mfma_f32_16x16x32_bf16 v[208:211], v[132:135], v[104:107], v[208:211]\n"
    "ds_read_b64_tr_b16 v[128:129], v236 offset:0\n"
    "v_mfma_f32_16x16x32_bf16 v[212:215], v[132:135], v[108:111], v[212:215]\n"
    "ds_read_b64_tr_b16 v[130:131], v236 offset:256\n"
    "v_mfma_f32_16x16x32_bf16 v[88:91], v[132:135], v[92:95], v[88:91]\n"
    "s_waitcnt lgkmcnt(8)\n"
    "v_mfma_f32_16x16x32_bf16 v[144:147], v[112:115], v[96:99], v[144:147]\n"
    "ds_read_b64_tr_b16 v[132:133], v237 offset:0\n"
    "v_mfma_f32_16x16x32_bf16 v[148:151], v[112:115], v[100:103], v[148:151]\n"
    "ds_read_b64_tr_b16 v[134:135], v237 offset:256\n"
    "v_mfma_f32_16x16x32_bf16 v[80:83], v[112:115], v[92:95], v[80:83]\n"
    "s_waitcnt lgkmcnt(8)\n"
    "v_mfma_f32_16x16x32_bf16 v[152:155], v[116:119], v[96:99], v[152:155]\n"
    "ds_read_b64_tr_b16 v[112:113], v238 offset:0\n"
    "v_mfma_f32_16x16x32_bf16 v[156:159], v[116:119], v[100:103], v[156:159]\n"
    "ds_read_b64_tr_b16 v[114:115], v238 offset:256\n"
    "v_mfma_f32_16x16x32_bf16 v[84:87], v[116:119], v[92:95], v[84:87]\n"
    "s_waitcnt lgkmcnt(8)\n"
    "v_mfma_f32_16x16x32_bf16 v[160:163], v[120:123], v[96:99], v[160:163]\n"
    "ds_read_b64_tr_b16 v[116:117], v239 offset:0\n"
    "v_mfma_f32_16x16x32_bf16 v[164:167], v[120:123], v[100:103], v[164:167]\n"
    "ds_read_b64_tr_b16 v[118:119], v239 offset:256\n"
    "v_mfma_f32_16x16x32_bf16 v[216:219], v[92:95], v[96:99], v[216:219]\n"
    "s_waitcnt lgkmcnt(8)\n"
    "v_mfma_f32_16x16x32_bf16 v[168:171], v[124:127], v[96:99], v[168:171]\n"
    "ds_read_b64_tr_b16 v[120:121], v240 offset:0\n"
    "v_mfma_f32_16x16x32_bf16 v[172:175], v[124:127], v[100:103], v[172:175]\n"
    "ds_read_b64_tr_b16 v[122:123], v240 offset:256\n"
    "v_mfma_f32_16x16x32_bf16 v[220:223], v[92:95], v[100:103], v[220:223]\n"
    "ds_read_b128 v[104:107], v242 offset:0\n"
    "s_waitcnt lgkmcnt(9)\n"
    "v_mfma_f32_16x16x32_bf16 v[176:179], v[128:131], v[96:99], v[176:179]\n"
    "ds_read_b128 v[108:111], v242 offset:2048\n"
    "v_mfma_f32_16x16x32_bf16 v[180:183], v[128:131], v[100:103], v[180:183]\n"
    "ds_read_b64_tr_b16 v[124:125], v232 offset:9472\n"
    "v_mfma_f32_16x16x32_bf16 v[224:227], v[96:99], v[96:99], v[224:227]\n"
    "ds_read_b64_tr_b16 v[126:127], v232 offset:9728\n"
    "s_waitcnt lgkmcnt(10)\n"
    "v_mfma_f32_16x16x32_bf16 v[184:187], v[132:135], v[96:99], v[184:187]\n"
    "ds_read_b64_tr_b16 v[128:129], v233 offset:9472\n"
    "v_mfma_f32_16x16x32_bf16 v[188:191], v[132:135], v[100:103], v[188:191]\n"
    "ds_read_b64_tr_b16 v[130:131], v233 offset:9728\n"
    "v_mfma_f32_16x16x32_bf16 v[228:231], v[100:103], v[100:103], v[228:231]\n"
    "s_waitcnt lgkmcnt(10)\n"
    "v_mfma_f32_16x16x32_bf16 v[192:195], v[112:115], v[96:99], v[192:195]\n"
    "ds_read_b64_tr_b16 v[132:133], v234 offset:9472\n"
    "v_mfma_f32_16x16x32_bf16 v[196:199], v[112:115], v[100:103], v[196:199]\n"
    "ds_read_b64_tr_b16 v[134:135], v234 offset:9728\n"
    "s_waitcnt lgkmcnt(10)\n"
    "v_mfma_f32_16x16x32_bf16 v[200:203], v[116:119], v[96:99], v[200:203]\n"
    "ds_read_b64_tr_b16 v[112:113], v235 offset:9472\n"
    "v_mfma_f32_16x16x32_bf16 v[204:207], v[116:119], v[100:103], v[204:207]\n"
    "ds_read_b64_tr_b16 v[114:115], v235 offset:9728\n"
    "s_waitcnt lgkmcnt(10)\n"
    "v_mfma_f32_16x16x32_bf16 v[208:211], v[120:123], v[96:99], v[208:211]\n"
    "ds_read_b64_tr_b16 v[116:117], v236 offset:9472\n"
    "v_mfma_f32_16x16x32_bf16 v[212:215], v[120:123], v[100:103], v[212:215]\n"
    "ds_read_b64_tr_b16 v[118:119], v236 offset:9728\n"
    "v_mfma_f32_16x16x32_bf16 v[88:91], v[120:123], v[92:95], v[88:91]\n"
    "s_waitcnt lgkmcnt(8)\n"
    "v_mfma_f32_16x16x32_bf16 v[144:147], v[124:127], v[104:107], v[144:147]\n"
    "ds_read_b64_tr_b16 v[120:121], v237 offset:9472\n"
    "v_mfma_f32_16x16x32_bf16 v[148:151], v[124:127], v[108:111], v[148:151]\n"
    "ds_read_b64_tr_b16 v[122:123], v237 offset:9728\n"
    "v_mfma_f32_16x16x32_bf16 v[80:83], v[124:127], v[92:95], v[80:83]\n"
    "s_waitcnt lgkmcnt(8)\n"
    "v_mfma_f32_16x16x32_bf16 v[152:155], v[128:131], v[104:107], v[152:155]\n"
    "ds_read_b64_tr_b16 v[124:125], v238 offset:9472\n"
    "v_mfma_f32_16x16x32_bf16 v[156:159], v[128:131], v[108:111], v[156:159]\n"
    "ds_read_b64_tr_b16 v[126:127], v238 offset:9728\n"
    "v_mfma_f32_16x16x32_bf16 v[84:87], v[128:131], v[92:95], v[84:87]\n"
    "s_waitcnt lgkmcnt(8)\n"
    "v_mfma_f32_16x16x32_bf16 v[160:163], v[132:135], v[104:107], v[160:163]\n"
    "ds_read_b64_tr_b16 v[128:129], v239 offset:9472\n"
    "v_mfma_f32_16x16x32_bf16 v[164:167], v[132:135], v[108:111], v[164:167]\n"
    "ds_read_b64_tr_b16 v[130:131], v239 offset:9728\n"
    "v_mfma_f32_16x16x32_bf16 v[216:219], v[92:95], v[104:107], v[216:219]\n"
    "s_waitcnt lgkmcnt(8)\n"
    "v_mfma_f32_16x16x32_bf16 v[168:171], v[112:115], v[104:107], v[168:171]\n"
    "ds_read_b64_tr_b16 v[132:133], v240 offset:9472\n"
    "v_mfma_f32_16x16x32_bf16 v[172:175], v[112:115], v[108:111], v[172:175]\n"
    "ds_read_b64_tr_b16 v[134:135], v240 offset:9728\n"
    "v_mfma_f32_16x16x32_bf16 v[220:223], v[92:95], v[108:111], v[220:223]\n"
    "s_waitcnt lgkmcnt(8)\n"
    "v_mfma_f32_16x16x32_bf16 v[176:179], v[116:119], v[104:107], v[176:179]\n"
    "v_mfma_f32_16x16x32_bf16 v[180:183], v[116:119], v[108:111], v[180:183]\n"
    "v_mfma_f32_16x16x32_bf16 v[224:227], v[104:107], v[104:107], v[224:227]\n"
    "s_waitcnt lgkmcnt(6)\n"
    "v_mfma_f32_16x16x32_bf16 v[184:187], v[120:123], v[104:107], v[184:187]\n"
    "v_mfma_f32_16x16x32_bf16 v[188:191], v[120:123], v[108:111], v[188:191]\n"
    "v_mfma_f32_16x16x32_bf16 v[228:231], v[108:111], v[108:111], v[228:231]\n"
    "s_waitcnt lgkmcnt(4)\n"
    "v_mfma_f32_16x16x32_bf16 v[192:195], v[124:127], v[104:107], v[192:195]\n"
    "v_mfma_f32_16x16x32_bf16 v[196:199], v[124:127], v[108:111], v[196:199]\n"
    "s_waitcnt lgkmcnt(2)\n"
    "v_mfma_f32_16x16x32_bf16 v[200:203], v[128:131], v[104:107], v[200:203]\n"
    "v_mfma_f32_16x16x32_bf16 v[204:207], v[128:131], v[108:111], v[204:207]\n"
    "s_waitcnt lgkmcnt(0)\n"
    "v_mfma_f32_16x16x32_bf16 v[208:211], v[132:135], v[104:107], v[208:211]\n"
    "v_mfma_f32_16x16x32_bf16 v[212:215], v[132:135], v[108:111], v[212:215]\n"
    "v_mfma_f32_16x16x32_bf16 v[88:91], v[132:135], v[92:95], v[88:91]\n"
    "s_mov_b32 exec_lo, 0x10001\n"
    "s_mov_b32 exec_hi, 0x10001\n"
    "ds_write_b128 v8, v[80:83]\n"
    "ds_write_b128 v8, v[84:87] offset:64\n"
    "s_mov_b64 exec, -1\n"
    "s_mov_b32 vcc_lo, 0x22222222\n"
    "s_mov_b32 vcc_hi, 0x22222222\n"
    "v_cndmask_b32 v224, v224, v225, vcc\n"
    "v_cndmask_b32 v228, v228, v229, vcc\n"
    "s_mov_b32 vcc_lo, 0x44444444\n"
    "s_mov_b32 vcc_hi, 0x44444444\n"
    "v_cndmask_b32 v224, v224, v226, vcc\n"
    "v_cndmask_b32 v228, v228, v230, vcc\n"
    "s_mov_b32 vcc_lo, 0x88888888\n"
    "s_mov_b32 vcc_hi, 0x88888888\n"
    "v_cndmask_b32 v224, v224, v227, vcc\n"
    "v_cndmask_b32 v228, v228, v231, vcc\n"
    "s_waitcnt lgkmcnt(0)\n"
    "s_barrier\n"
    "ds_read_b128 v[24:27], v77 offset:0\n"
    "ds_read_b128 v[28:31], v77 offset:64\n"
    "ds_read_b128 v[32:35], v78 offset:0\n"
    "ds_read_b128 v[36:39], v78 offset:64\n"
    "ds_read_b128 v[40:43], v79 offset:0\n"
    "ds_read_b128 v[44:47], v79 offset:64\n"
    "v_mul_f32 v248, 0xbae38e39, v216\n"
    "v_mul_f32 v252, 0x3ae38e39, v224\n"
    "v_mul_f32 v250, 0xbae38e39, v220\n"
    "v_mul_f32 v140, 0x3ae38e39, v228\n"
    "v_fma_f32 v252, -v248, v248, v252\n"
    "v_fma_f32 v140, -v250, v250, v140\n"
    "v_max_f32 v252, 0, v252\n"
    "v_max_f32 v140, 0, v140\n"
    "v_cvt_pk_bf16_f32 v252, v252, v252\n"
    "v_cvt_pk_bf16_f32 v140, v140, v140\n"
    "v_lshlrev_b32 v252, 16, v252\n"
    "v_lshlrev_b32 v140, 16, v140\n"
    "v_add_f32 v252, 0x3727c5ac, v252\n"
    "v_add_f32 v140, 0x3727c5ac, v140\n"
    "v_cvt_pk_bf16_f32 v252, v252, v252\n"
    "v_cvt_pk_bf16_f32 v140, v140, v140\n"
    "v_lshlrev_b32 v252, 16, v252\n"
    "v_lshlrev_b32 v140, 16, v140\n"
    "v_sqrt_f32 v252, v252\n"
    "v_sqrt_f32 v140, v140\n"
    "v_cvt_pk_bf16_f32 v252, v252, v252\n"
    "v_cvt_pk_bf16_f32 v140, v140, v140\n"
    "v_lshlrev_b32 v252, 16, v252\n"
    "v_lshlrev_b32 v140, 16, v140\n"
    "v_rcp_f32 v252, v252\n"
    "v_rcp_f32 v140, v140\n"
    "v_mul_f32 v252, %14, v252\n"
    "v_mul_f32 v140, %14, v140\n"
    "s_mov_b32 exec_lo, 0x00f0000f\n"
    "s_mov_b32 exec_hi, 0xf0000f00\n"
    "ds_write_b32 v11, v252 offset:64\n"
    "ds_write_b32 v11, v140 offset:192\n"
    "s_mov_b64 exec, -1\n"
    "ds_read_b32 v252, v11 offset:64\n"
    "ds_read_b32 v140, v11 offset:192\n"
    "v_pk_fma_f32 v[144:145], v[248:249], v[80:81], v[144:145] op_sel_hi:[0,1,1]\n"
    "v_pk_fma_f32 v[146:147], v[248:249], v[82:83], v[146:147] op_sel_hi:[0,1,1]\n"
    "v_pk_fma_f32 v[148:149], v[250:251], v[80:81], v[148:149] op_sel_hi:[0,1,1]\n"
    "v_pk_fma_f32 v[150:151], v[250:251], v[82:83], v[150:151] op_sel_hi:[0,1,1]\n"
    "v_pk_fma_f32 v[152:153], v[248:249], v[84:85], v[152:153] op_sel_hi:[0,1,1]\n"
    "v_pk_fma_f32 v[154:155], v[248:249], v[86:87], v[154:155] op_sel_hi:[0,1,1]\n"
    "v_pk_fma_f32 v[156:157], v[250:251], v[84:85], v[156:157] op_sel_hi:[0,1,1]\n"
    "v_pk_fma_f32 v[158:159], v[250:251], v[86:87], v[158:159] op_sel_hi:[0,1,1]\n"
    "v_pk_fma_f32 v[208:209], v[248:249], v[88:89], v[208:209] op_sel_hi:[0,1,1]\n"
    "v_pk_fma_f32 v[210:211], v[248:249], v[90:91], v[210:211] op_sel_hi:[0,1,1]\n"
    "v_pk_fma_f32 v[212:213], v[250:251], v[88:89], v[212:213] op_sel_hi:[0,1,1]\n"
    "v_pk_fma_f32 v[214:215], v[250:251], v[90:91], v[214:215] op_sel_hi:[0,1,1]\n"
    "s_waitcnt lgkmcnt(9)\n"
    "v_pk_fma_f32 v[160:161], v[248:249], v[24:25], v[160:161] op_sel_hi:[0,1,1]\n"
    "v_pk_fma_f32 v[162:163], v[248:249], v[26:27], v[162:163] op_sel_hi:[0,1,1]\n"
    "v_pk_fma_f32 v[164:165], v[250:251], v[24:25], v[164:165] op_sel_hi:[0,1,1]\n"
    "v_pk_fma_f32 v[166:167], v[250:251], v[26:27], v[166:167] op_sel_hi:[0,1,1]\n"
    "s_waitcnt lgkmcnt(8)\n"
    "v_pk_fma_f32 v[168:169], v[248:249], v[28:29], v[168:169] op_sel_hi:[0,1,1]\n"
    "v_pk_fma_f32 v[170:171], v[248:249], v[30:31], v[170:171] op_sel_hi:[0,1,1]\n"
    "v_pk_fma_f32 v[172:173], v[250:251], v[28:29], v[172:173] op_sel_hi:[0,1,1]\n"
    "v_pk_fma_f32 v[174:175], v[250:251], v[30:31], v[174:175] op_sel_hi:[0,1,1]\n"
    "s_waitcnt lgkmcnt(7)\n"
    "v_pk_fma_f32 v[176:177], v[248:249], v[32:33], v[176:177] op_sel_hi:[0,1,1]\n"
    "v_pk_fma_f32 v[178:179], v[248:249], v[34:35], v[178:179] op_sel_hi:[0,1,1]\n"
    "v_pk_fma_f32 v[180:181], v[250:251], v[32:33], v[180:181] op_sel_hi:[0,1,1]\n"
    "v_pk_fma_f32 v[182:183], v[250:251], v[34:35], v[182:183] op_sel_hi:[0,1,1]\n"
    "s_waitcnt lgkmcnt(6)\n"
    "v_pk_fma_f32 v[184:185], v[248:249], v[36:37], v[184:185] op_sel_hi:[0,1,1]\n"
    "v_pk_fma_f32 v[186:187], v[248:249], v[38:39], v[186:187] op_sel_hi:[0,1,1]\n"
    "v_pk_fma_f32 v[188:189], v[250:251], v[36:37], v[188:189] op_sel_hi:[0,1,1]\n"
    "v_pk_fma_f32 v[190:191], v[250:251], v[38:39], v[190:191] op_sel_hi:[0,1,1]\n"
    "s_waitcnt lgkmcnt(5)\n"
    "v_pk_fma_f32 v[192:193], v[248:249], v[40:41], v[192:193] op_sel_hi:[0,1,1]\n"
    "v_pk_fma_f32 v[194:195], v[248:249], v[42:43], v[194:195] op_sel_hi:[0,1,1]\n"
    "v_pk_fma_f32 v[196:197], v[250:251], v[40:41], v[196:197] op_sel_hi:[0,1,1]\n"
    "v_pk_fma_f32 v[198:199], v[250:251], v[42:43], v[198:199] op_sel_hi:[0,1,1]\n"
    "s_waitcnt lgkmcnt(4)\n"
    "v_pk_fma_f32 v[200:201], v[248:249], v[44:45], v[200:201] op_sel_hi:[0,1,1]\n"
    "v_pk_fma_f32 v[202:203], v[248:249], v[46:47], v[202:203] op_sel_hi:[0,1,1]\n"
    "v_pk_fma_f32 v[204:205], v[250:251], v[44:45], v[204:205] op_sel_hi:[0,1,1]\n"
    "v_pk_fma_f32 v[206:207], v[250:251], v[46:47], v[206:207] op_sel_hi:[0,1,1]\n"
    "s_waitcnt lgkmcnt(0)\n"
    "v_pk_mul_f32 v[144:145], v[252:253], v[144:145] op_sel_hi:[0,1]\n"
    "v_pk_mul_f32 v[146:147], v[252:253], v[146:147] op_sel_hi:[0,1]\n"
    "v_pk_mul_f32 v[148:149], v[140:141], v[148:149] op_sel_hi:[0,1]\n"
    "v_pk_mul_f32 v[150:151], v[140:141], v[150:151] op_sel_hi:[0,1]\n"
    "v_cvt_pk_bf16_f32 v144, v144, v145\n"
    "v_cvt_pk_bf16_f32 v145, v146, v147\n"
    "buffer_store_dwordx2 v[144:145], v13, s[36:39], s72 offen offset:0\n"
    "v_cvt_pk_bf16_f32 v148, v148, v149\n"
    "v_cvt_pk_bf16_f32 v149, v150, v151\n"
    "buffer_store_dwordx2 v[148:149], v13, s[36:39], s72 offen offset:512\n"
    "v_pk_mul_f32 v[152:153], v[252:253], v[152:153] op_sel_hi:[0,1]\n"
    "v_pk_mul_f32 v[154:155], v[252:253], v[154:155] op_sel_hi:[0,1]\n"
    "v_pk_mul_f32 v[156:157], v[140:141], v[156:157] op_sel_hi:[0,1]\n"
    "v_pk_mul_f32 v[158:159], v[140:141], v[158:159] op_sel_hi:[0,1]\n"
    "v_cvt_pk_bf16_f32 v152, v152, v153\n"
    "v_cvt_pk_bf16_f32 v153, v154, v155\n"
    "buffer_store_dwordx2 v[152:153], v13, s[36:39], s73 offen offset:0\n"
    "v_cvt_pk_bf16_f32 v156, v156, v157\n"
    "v_cvt_pk_bf16_f32 v157, v158, v159\n"
    "buffer_store_dwordx2 v[156:157], v13, s[36:39], s73 offen offset:512\n"
    "v_pk_mul_f32 v[208:209], v[252:253], v[208:209] op_sel_hi:[0,1]\n"
    "v_pk_mul_f32 v[210:211], v[252:253], v[210:211] op_sel_hi:[0,1]\n"
    "v_pk_mul_f32 v[212:213], v[140:141], v[212:213] op_sel_hi:[0,1]\n"
    "v_pk_mul_f32 v[214:215], v[140:141], v[214:215] op_sel_hi:[0,1]\n"
    "v_cvt_pk_bf16_f32 v208, v208, v209\n"
    "v_cvt_pk_bf16_f32 v209, v210, v211\n"
    "buffer_store_dwordx2 v[208:209], v14, s[36:39], %12 offen offset:0\n"
    "v_cvt_pk_bf16_f32 v212, v212, v213\n"
    "v_cvt_pk_bf16_f32 v213, v214, v215\n"
    "buffer_store_dwordx2 v[212:213], v14, s[36:39], %12 offen offset:128\n"
    "v_pk_mul_f32 v[160:161], v[252:253], v[160:161] op_sel_hi:[0,1]\n"
    "v_pk_mul_f32 v[162:163], v[252:253], v[162:163] op_sel_hi:[0,1]\n"
    "v_pk_mul_f32 v[164:165], v[140:141], v[164:165] op_sel_hi:[0,1]\n"
    "v_pk_mul_f32 v[166:167], v[140:141], v[166:167] op_sel_hi:[0,1]\n"
    "v_cvt_pk_bf16_f32 v160, v160, v161\n"
    "v_cvt_pk_bf16_f32 v161, v162, v163\n"
    "buffer_store_dwordx2 v[160:161], v13, s[36:39], s74 offen offset:0\n"
    "v_cvt_pk_bf16_f32 v164, v164, v165\n"
    "v_cvt_pk_bf16_f32 v165, v166, v167\n"
    "buffer_store_dwordx2 v[164:165], v13, s[36:39], s74 offen offset:512\n"
    "v_pk_mul_f32 v[168:169], v[252:253], v[168:169] op_sel_hi:[0,1]\n"
    "v_pk_mul_f32 v[170:171], v[252:253], v[170:171] op_sel_hi:[0,1]\n"
    "v_pk_mul_f32 v[172:173], v[140:141], v[172:173] op_sel_hi:[0,1]\n"
    "v_pk_mul_f32 v[174:175], v[140:141], v[174:175] op_sel_hi:[0,1]\n"
    "v_cvt_pk_bf16_f32 v168, v168, v169\n"
    "v_cvt_pk_bf16_f32 v169, v170, v171\n"
    "buffer_store_dwordx2 v[168:169], v13, s[36:39], s75 offen offset:0\n"
    "v_cvt_pk_bf16_f32 v172, v172, v173\n"
    "v_cvt_pk_bf16_f32 v173, v174, v175\n"
    "buffer_store_dwordx2 v[172:173], v13, s[36:39], s75 offen offset:512\n"
    "v_pk_mul_f32 v[176:177], v[252:253], v[176:177] op_sel_hi:[0,1]\n"
    "v_pk_mul_f32 v[178:179], v[252:253], v[178:179] op_sel_hi:[0,1]\n"
    "v_pk_mul_f32 v[180:181], v[140:141], v[180:181] op_sel_hi:[0,1]\n"
    "v_pk_mul_f32 v[182:183], v[140:141], v[182:183] op_sel_hi:[0,1]\n"
    "v_cvt_pk_bf16_f32 v176, v176, v177\n"
    "v_cvt_pk_bf16_f32 v177, v178, v179\n"
    "buffer_store_dwordx2 v[176:177], v13, s[36:39], s76 offen offset:0\n"
    "v_cvt_pk_bf16_f32 v180, v180, v181\n"
    "v_cvt_pk_bf16_f32 v181, v182, v183\n"
    "buffer_store_dwordx2 v[180:181], v13, s[36:39], s76 offen offset:512\n"
    "v_pk_mul_f32 v[184:185], v[252:253], v[184:185] op_sel_hi:[0,1]\n"
    "v_pk_mul_f32 v[186:187], v[252:253], v[186:187] op_sel_hi:[0,1]\n"
    "v_pk_mul_f32 v[188:189], v[140:141], v[188:189] op_sel_hi:[0,1]\n"
    "v_pk_mul_f32 v[190:191], v[140:141], v[190:191] op_sel_hi:[0,1]\n"
    "v_cvt_pk_bf16_f32 v184, v184, v185\n"
    "v_cvt_pk_bf16_f32 v185, v186, v187\n"
    "buffer_store_dwordx2 v[184:185], v13, s[36:39], s77 offen offset:0\n"
    "v_cvt_pk_bf16_f32 v188, v188, v189\n"
    "v_cvt_pk_bf16_f32 v189, v190, v191\n"
    "buffer_store_dwordx2 v[188:189], v13, s[36:39], s77 offen offset:512\n"
    "v_pk_mul_f32 v[192:193], v[252:253], v[192:193] op_sel_hi:[0,1]\n"
    "v_pk_mul_f32 v[194:195], v[252:253], v[194:195] op_sel_hi:[0,1]\n"
    "v_pk_mul_f32 v[196:197], v[140:141], v[196:197] op_sel_hi:[0,1]\n"
    "v_pk_mul_f32 v[198:199], v[140:141], v[198:199] op_sel_hi:[0,1]\n"
    "v_cvt_pk_bf16_f32 v192, v192, v193\n"
    "v_cvt_pk_bf16_f32 v193, v194, v195\n"
    "buffer_store_dwordx2 v[192:193], v13, s[36:39], s78 offen offset:0\n"
    "v_cvt_pk_bf16_f32 v196, v196, v197\n"
    "v_cvt_pk_bf16_f32 v197, v198, v199\n"
    "buffer_store_dwordx2 v[196:197], v13, s[36:39], s78 offen offset:512\n"
    "v_pk_mul_f32 v[200:201], v[252:253], v[200:201] op_sel_hi:[0,1]\n"
    "v_pk_mul_f32 v[202:203], v[252:253], v[202:203] op_sel_hi:[0,1]\n"
    "v_pk_mul_f32 v[204:205], v[140:141], v[204:205] op_sel_hi:[0,1]\n"
    "v_pk_mul_f32 v[206:207], v[140:141], v[206:207] op_sel_hi:[0,1]\n"
    "v_cvt_pk_bf16_f32 v200, v200, v201\n"
    "v_cvt_pk_bf16_f32 v201, v202, v203\n"
    "buffer_store_dwordx2 v[200:201], v13, s[36:39], s79 offen offset:0\n"
    "v_cvt_pk_bf16_f32 v204, v204, v205\n"
    "v_cvt_pk_bf16_f32 v205, v206, v207\n"
    "buffer_store_dwordx2 v[204:205], v13, s[36:39], s79 offen offset:512\n"
    "s_cmp_lg_u32 %16, 0\n"
    "s_cbranch_scc1 .Lnm_%=\n"
    "v_sub_f32 v248, 0, v248\n"
    "buffer_store_dword v248, v15, s[36:39], %15 offen offset:0\n"
    "v_sub_f32 v250, 0, v250\n"
    "buffer_store_dword v250, v15, s[36:39], %15 offen offset:64\n"
    "s_nop 2\n"
    ".Lnm_%=:\n"
    "s_branch .Ldone_%=\n"
    ".Lv_%=:\n"
    "s_nop 1\n"
    "v_and_b32 v5, 15, v3\n"
    "v_lshrrev_b32 v6, 4, v3\n"
    "s_lshl_b32 s53, %6, 7\n"
    "s_add_u32 s53, s53, 143360\n"
    "v_lshl_add_u32 v8, v6, 4, s53\n"
    "v_lshl_add_u32 v76, v5, 2, s53\n"
    "v_xor_b32 v77, 128, v76\n"
    "v_xor_b32 v78, 256, v76\n"
    "v_xor_b32 v79, 384, v76\n"
    "s_mov_b32 s53, 143360\n"
    "v_lshl_add_u32 v9, v6, 4, s53\n"
    "v_lshl_add_u32 v10, v5, 2, s53\n"
    "s_lshl_b32 s54, %6, 8\n"
    "s_add_u32 s54, s54, 144384\n"
    "v_lshl_add_u32 v11, v5, 2, s54\n"
    "v_lshl_add_u32 v12, v6, 4, s54\n"
    "s_lshl_b32 s54, %6, 1\n"
    "s_xor_b32 s72, s54, 0\n"
    "s_mul_i32 s72, s72, %13\n"
    "s_add_u32 s72, s72, %11\n"
    "s_xor_b32 s73, s54, 1\n"
    "s_mul_i32 s73, s73, %13\n"
    "s_add_u32 s73, s73, %11\n"
    "s_xor_b32 s74, s54, 2\n"
    "s_mul_i32 s74, s74, %13\n"
    "s_add_u32 s74, s74, %11\n"
    "s_xor_b32 s75, s54, 3\n"
    "s_mul_i32 s75, s75, %13\n"
    "s_add_u32 s75, s75, %11\n"
    "s_xor_b32 s76, s54, 4\n"
    "s_mul_i32 s76, s76, %13\n"
    "s_add_u32 s76, s76, %11\n"
    "s_xor_b32 s77, s54, 5\n"
    "s_mul_i32 s77, s77, %13\n"
    "s_add_u32 s77, s77, %11\n"
    "s_xor_b32 s78, s54, 6\n"
    "s_mul_i32 s78, s78, %13\n"
    "s_add_u32 s78, s78, %11\n"
    "s_xor_b32 s79, s54, 7\n"
    "s_mul_i32 s79, s79, %13\n"
    "s_add_u32 s79, s79, %11\n"
    "v_and_b32 v7, 1, v6\n"
    "v_mul_u32_u24 v13, 304, v7\n"
    "v_lshrrev_b32 v7, 1, v6\n"
    "v_lshl_add_u32 v13, v7, 3, v13\n"
    "v_lshrrev_b32 v7, 1, v5\n"
    "v_mul_lo_u32 v15, %13, v7\n"
    "v_add_u32 v14, v15, v13\n"
    "v_and_b32 v7, 1, v5\n"
    "v_lshl_add_u32 v14, v7, 4, v14\n"
    "v_mul_u32_u24 v7, 304, v7\n"
    "v_add_u32 v15, v7, v15\n"
    "v_lshl_add_u32 v13, v5, 4, v13\n"
    "buffer_store_dwordx4 v[92:95], v15, s[36:39], %11 offen offset:288\n"
    "buffer_store_dwordx4 v[92:95], v15, s[36:39], %11 offen offset:896\n"
    "v_add_u32 v60, 70656, v232\n"
    "v_add_u32 v61, 70656, v233\n"
    "v_add_u32 v62, 70656, v234\n"
    "v_add_u32 v63, 70656, v235\n"
    "v_add_u32 v64, 70656, v236\n"
    "v_add_u32 v65, 70656, v237\n"
    "v_add_u32 v66, 70656, v238\n"
    "v_add_u32 v67, 70656, v239\n"
    "v_add_u32 v68, 70656, v240\n"
    "v_add_u32 v69, 70656, v241\n"
    "v_add_u32 v70, 70656, v242\n"
    "s_waitcnt vmcnt(12)\n"
    "s_barrier\n"
    "ds_read_b128 v[96:99], v241 offset:0\n"
    "ds_read_b128 v[100:103], v241 offset:2048\n"
    "ds_read_b64_tr_b16 v[112:113], v232 offset:0\n"
    "ds_read_b64_tr_b16 v[114:115], v232 offset:256\n"
    "ds_read_b64_tr_b16 v[116:117], v233 offset:0\n"
    "ds_read_b64_tr_b16 v[118:119], v233 offset:256\n"
    "ds_read_b64_tr_b16 v[120:121], v234 offset:0\n"
    "ds_read_b64_tr_b16 v[122:123], v234 offset:256\n"
    "ds_read_b64_tr_b16 v[124:125], v235 offset:0\n"
    "ds_read_b64_tr_b16 v[126:127], v235 offset:256\n"
    "s_waitcnt lgkmcnt(6)\n"
    "v_mfma_f32_16x16x32_bf16 v[144:147], v[96:99], v[112:115], 0\n"
    "s_add_u32 m0, s52, 70656\n"
    "ds_read_b64_tr_b16 v[128:129], v236 offset:0\n"
    "v_mfma_f32_16x16x32_bf16 v[148:151], v[100:103], v[112:115], 0\n"
    "s_add_u32 s50, %8, 442368\n"
    "ds_read_b64_tr_b16 v[130:131], v236 offset:256\n"
    "buffer_load_dwordx4 v246, s[44:47], s50 offen lds\n"
    "v_mfma_f32_16x16x32_bf16 v[80:83], v[112:115], v[92:95], 0\n"
    "s_add_u32 m0, s56, 89600\n"
    "ds_read_b64_tr_b16 v[132:133], v237 offset:0\n"
    "s_waitcnt lgkmcnt(7)\n"
    "v_mfma_f32_16x16x32_bf16 v[152:155], v[96:99], v[116:119], 0\n"
    "s_add_u32 s51, %7, 256\n"
    "ds_read_b64_tr_b16 v[134:135], v237 offset:256\n"
    "buffer_load_dwordx4 v243, s[40:43], s51 offen lds\n"
    "v_mfma_f32_16x16x32_bf16 v[156:159], v[100:103], v[116:119], 0\n"
    "s_add_u32 m0, s52, 71712\n"
    "ds_read_b64_tr_b16 v[112:113], v238 offset:0\n"
    "v_mfma_f32_16x16x32_bf16 v[84:87], v[116:119], v[92:95], 0\n"
    "s_add_u32 s48, %8, 445824\n"
    "ds_read_b64_tr_b16 v[114:115], v238 offset:256\n"
    "buffer_load_dwordx4 v246, s[44:47], s48 offen lds\n"
    "s_waitcnt lgkmcnt(8)\n"
    "v_mfma_f32_16x16x32_bf16 v[160:163], v[96:99], v[120:123], 0\n"
    "s_add_u32 m0, s56, 90624\n"
    "ds_read_b64_tr_b16 v[116:117], v239 offset:0\n"
    "v_mfma_f32_16x16x32_bf16 v[164:167], v[100:103], v[120:123], 0\n"
    "s_add_u32 s49, %7, 9472\n"
    "ds_read_b64_tr_b16 v[118:119], v239 offset:256\n"
    "buffer_load_dwordx4 v243, s[40:43], s49 offen lds\n"
    "v_mfma_f32_16x16x32_bf16 v[216:219], v[92:95], v[96:99], 0\n"
    "s_add_u32 m0, s52, 80128\n"
    "s_add_u32 s50, %8, 552960\n"
    "buffer_load_dwordx4 v246, s[44:47], s50 offen lds\n"
    "s_waitcnt lgkmcnt(8)\n"
    "v_mfma_f32_16x16x32_bf16 v[168:171], v[96:99], v[124:127], 0\n"
    "s_add_u32 m0, s56, 91648\n"
    "ds_read_b64_tr_b16 v[120:121], v240 offset:0\n"
    "v_mfma_f32_16x16x32_bf16 v[172:175], v[100:103], v[124:127], 0\n"
    "s_add_u32 s51, %7, 18688\n"
    "ds_read_b64_tr_b16 v[122:123], v240 offset:256\n"
    "buffer_load_dwordx4 v243, s[40:43], s51 offen lds\n"
    "v_mfma_f32_16x16x32_bf16 v[220:223], v[92:95], v[100:103], 0\n"
    "s_add_u32 m0, s52, 81184\n"
    "ds_read_b128 v[104:107], v242 offset:0\n"
    "s_waitcnt lgkmcnt(9)\n"
    "v_mfma_f32_16x16x32_bf16 v[176:179], v[96:99], v[128:131], 0\n"
    "s_add_u32 s48, %8, 556416\n"
    "ds_read_b128 v[108:111], v242 offset:2048\n"
    "buffer_load_dwordx4 v246, s[44:47], s48 offen lds\n"
    "v_mfma_f32_16x16x32_bf16 v[180:183], v[100:103], v[128:131], 0\n"
    "s_add_u32 m0, s56, 92672\n"
    "ds_read_b64_tr_b16 v[124:125], v232 offset:9472\n"
    "v_mfma_f32_16x16x32_bf16 v[224:227], v[96:99], v[96:99], 0\n"
    "s_add_u32 s49, %7, 27904\n"
    "ds_read_b64_tr_b16 v[126:127], v232 offset:9728\n"
    "buffer_load_dwordx4 v243, s[40:43], s49 offen lds\n"
    "s_waitcnt lgkmcnt(10)\n"
    "v_mfma_f32_16x16x32_bf16 v[184:187], v[96:99], v[132:135], 0\n"
    "ds_read_b64_tr_b16 v[128:129], v233 offset:9472\n"
    "s_add_u32 m0, s55, 79104\n"
    "s_add_u32 s50, %9, 442368\n"
    "s_mov_b32 exec_hi, 0\n"
    "buffer_load_dwordx4 v247, s[44:47], s50 offen lds\n"
    "s_mov_b32 exec_hi, -1\n"
    "v_mfma_f32_16x16x32_bf16 v[188:191], v[100:103], v[132:135], 0\n"
    "s_add_u32 m0, s52, 105984\n"
    "ds_read_b64_tr_b16 v[130:131], v233 offset:9728\n"
    "v_mfma_f32_16x16x32_bf16 v[228:231], v[100:103], v[100:103], 0\n"
    "s_add_u32 s51, %8, 663552\n"
    "buffer_load_dwordx4 v246, s[44:47], s51 offen lds\n"
    "s_waitcnt lgkmcnt(10)\n"
    "v_mfma_f32_16x16x32_bf16 v[192:195], v[96:99], v[112:115], 0\n"
    "s_add_u32 m0, s56, 124928\n"
    "ds_read_b64_tr_b16 v[132:133], v234 offset:9472\n"
    "v_mfma_f32_16x16x32_bf16 v[196:199], v[100:103], v[112:115], 0\n"
    "s_add_u32 s48, %7, 384\n"
    "ds_read_b64_tr_b16 v[134:135], v234 offset:9728\n"
    "s_waitcnt lgkmcnt(10)\n"
    "v_mfma_f32_16x16x32_bf16 v[200:203], v[96:99], v[116:119], 0\n"
    "ds_read_b64_tr_b16 v[112:113], v235 offset:9472\n"
    "v_mfma_f32_16x16x32_bf16 v[204:207], v[100:103], v[116:119], 0\n"
    "ds_read_b64_tr_b16 v[114:115], v235 offset:9728\n"
    "s_waitcnt lgkmcnt(10)\n"
    "v_mfma_f32_16x16x32_bf16 v[208:211], v[96:99], v[120:123], 0\n"
    "ds_read_b64_tr_b16 v[116:117], v236 offset:9472\n"
    "v_mfma_f32_16x16x32_bf16 v[212:215], v[100:103], v[120:123], 0\n"
    "ds_read_b64_tr_b16 v[118:119], v236 offset:9728\n"
    "v_mfma_f32_16x16x32_bf16 v[88:91], v[120:123], v[92:95], 0\n"
    "buffer_load_dwordx4 v243, s[40:43], s48 offen lds\n"
    "s_waitcnt vmcnt(14)\n"
    "s_barrier\n"
    "s_waitcnt lgkmcnt(8)\n"
    "v_mfma_f32_16x16x32_bf16 v[144:147], v[104:107], v[124:127], v[144:147]\n"
    "s_add_u32 m0, s52, 107040\n"
    "ds_read_b64_tr_b16 v[120:121], v237 offset:9472\n"
    "v_mfma_f32_16x16x32_bf16 v[148:151], v[108:111], v[124:127], v[148:151]\n"
    "s_add_u32 s49, %8, 667008\n"
    "ds_read_b64_tr_b16 v[122:123], v237 offset:9728\n"
    "v_mfma_f32_16x16x32_bf16 v[80:83], v[124:127], v[92:95], v[80:83]\n"
    "buffer_load_dwordx4 v246, s[44:47], s49 offen lds\n"
    "s_waitcnt lgkmcnt(8)\n"
    "v_mfma_f32_16x16x32_bf16 v[152:155], v[104:107], v[128:131], v[152:155]\n"
    "s_add_u32 m0, s56, 125952\n"
    "ds_read_b64_tr_b16 v[124:125], v238 offset:9472\n"
    "v_mfma_f32_16x16x32_bf16 v[156:159], v[108:111], v[128:131], v[156:159]\n"
    "s_add_u32 s50, %7, 9600\n"
    "ds_read_b64_tr_b16 v[126:127], v238 offset:9728\n"
    "v_mfma_f32_16x16x32_bf16 v[84:87], v[128:131], v[92:95], v[84:87]\n"
    "buffer_load_dwordx4 v243, s[40:43], s50 offen lds\n"
    "s_waitcnt lgkmcnt(8)\n"
    "v_mfma_f32_16x16x32_bf16 v[160:163], v[104:107], v[132:135], v[160:163]\n"
    "s_add_u32 m0, s52, 115456\n"
    "ds_read_b64_tr_b16 v[128:129], v239 offset:9472\n"
    "v_mfma_f32_16x16x32_bf16 v[164:167], v[108:111], v[132:135], v[164:167]\n"
    "s_add_u32 s51, %8, 774144\n"
    "ds_read_b64_tr_b16 v[130:131], v239 offset:9728\n"
    "v_mfma_f32_16x16x32_bf16 v[216:219], v[92:95], v[104:107], v[216:219]\n"
    "buffer_load_dwordx4 v246, s[44:47], s51 offen lds\n"
    "s_waitcnt lgkmcnt(8)\n"
    "v_mfma_f32_16x16x32_bf16 v[168:171], v[104:107], v[112:115], v[168:171]\n"
    "s_add_u32 m0, s56, 126976\n"
    "ds_read_b64_tr_b16 v[132:133], v240 offset:9472\n"
    "v_mfma_f32_16x16x32_bf16 v[172:175], v[108:111], v[112:115], v[172:175]\n"
    "s_add_u32 s48, %7, 18816\n"
    "ds_read_b64_tr_b16 v[134:135], v240 offset:9728\n"
    "v_mfma_f32_16x16x32_bf16 v[220:223], v[92:95], v[108:111], v[220:223]\n"
    "ds_read_b128 v[96:99], v241 offset:35328\n"
    "s_waitcnt lgkmcnt(9)\n"
    "v_mfma_f32_16x16x32_bf16 v[176:179], v[104:107], v[116:119], v[176:179]\n"
    "ds_read_b128 v[100:103], v241 offset:37376\n"
    "v_mfma_f32_16x16x32_bf16 v[180:183], v[108:111], v[116:119], v[180:183]\n"
    "ds_read_b64_tr_b16 v[112:113], v232 offset:35328\n"
    "v_mfma_f32_16x16x32_bf16 v[224:227], v[104:107], v[104:107], v[224:227]\n"
    "ds_read_b64_tr_b16 v[114:115], v232 offset:35584\n"
    "s_waitcnt lgkmcnt(10)\n"
    "v_mfma_f32_16x16x32_bf16 v[184:187], v[104:107], v[120:123], v[184:187]\n"
    "ds_read_b64_tr_b16 v[116:117], v233 offset:35328\n"
    "v_mfma_f32_16x16x32_bf16 v[188:191], v[108:111], v[120:123], v[188:191]\n"
    "ds_read_b64_tr_b16 v[118:119], v233 offset:35584\n"
    "v_mfma_f32_16x16x32_bf16 v[228:231], v[108:111], v[108:111], v[228:231]\n"
    "buffer_load_dwordx4 v243, s[40:43], s48 offen lds\n"
    "s_waitcnt lgkmcnt(10)\n"
    "v_mfma_f32_16x16x32_bf16 v[192:195], v[104:107], v[124:127], v[192:195]\n"
    "s_add_u32 m0, s52, 116512\n"
    "ds_read_b64_tr_b16 v[120:121], v234 offset:35328\n"
    "v_mfma_f32_16x16x32_bf16 v[196:199], v[108:111], v[124:127], v[196:199]\n"
    "s_add_u32 s49, %8, 777600\n"
    "ds_read_b64_tr_b16 v[122:123], v234 offset:35584\n"
    "s_waitcnt lgkmcnt(10)\n"
    "v_mfma_f32_16x16x32_bf16 v[200:203], v[104:107], v[128:131], v[200:203]\n"
    "ds_read_b64_tr_b16 v[124:125], v235 offset:35328\n"
    "v_mfma_f32_16x16x32_bf16 v[204:207], v[108:111], v[128:131], v[204:207]\n"
    "ds_read_b64_tr_b16 v[126:127], v235 offset:35584\n"
    "s_waitcnt lgkmcnt(10)\n"
    "v_mfma_f32_16x16x32_bf16 v[208:211], v[104:107], v[132:135], v[208:211]\n"
    "ds_read_b64_tr_b16 v[128:129], v236 offset:35328\n"
    "v_mfma_f32_16x16x32_bf16 v[212:215], v[108:111], v[132:135], v[212:215]\n"
    "ds_read_b64_tr_b16 v[130:131], v236 offset:35584\n"
    "v_mfma_f32_16x16x32_bf16 v[88:91], v[132:135], v[92:95], v[88:91]\n"
    "buffer_load_dwordx4 v246, s[44:47], s49 offen lds\n"
    "s_waitcnt lgkmcnt(8)\n"
    "v_mfma_f32_16x16x32_bf16 v[144:147], v[96:99], v[112:115], v[144:147]\n"
    "s_add_u32 m0, s56, 128000\n"
    "ds_read_b64_tr_b16 v[132:133], v237 offset:35328\n"
    "v_mfma_f32_16x16x32_bf16 v[148:151], v[100:103], v[112:115], v[148:151]\n"
    "s_add_u32 s50, %7, 28032\n"
    "ds_read_b64_tr_b16 v[134:135], v237 offset:35584\n"
    "v_mfma_f32_16x16x32_bf16 v[80:83], v[112:115], v[92:95], v[80:83]\n"
    "buffer_load_dwordx4 v243, s[40:43], s50 offen lds\n"
    "s_waitcnt lgkmcnt(8)\n"
    "v_mfma_f32_16x16x32_bf16 v[152:155], v[96:99], v[116:119], v[152:155]\n"
    "ds_read_b64_tr_b16 v[112:113], v238 offset:35328\n"
    "v_mfma_f32_16x16x32_bf16 v[156:159], v[100:103], v[116:119], v[156:159]\n"
    "ds_read_b64_tr_b16 v[114:115], v238 offset:35584\n"
    "v_mfma_f32_16x16x32_bf16 v[84:87], v[116:119], v[92:95], v[84:87]\n"
    "s_add_u32 m0, s55, 114432\n"
    "s_add_u32 s51, %9, 663552\n"
    "s_mov_b32 exec_hi, 0\n"
    "buffer_load_dwordx4 v247, s[44:47], s51 offen lds\n"
    "s_mov_b32 exec_hi, -1\n"
    "s_waitcnt lgkmcnt(8)\n"
    "v_mfma_f32_16x16x32_bf16 v[160:163], v[96:99], v[120:123], v[160:163]\n"
    "ds_read_b64_tr_b16 v[116:117], v239 offset:35328\n"
    "v_mfma_f32_16x16x32_bf16 v[164:167], v[100:103], v[120:123], v[164:167]\n"
    "ds_read_b64_tr_b16 v[118:119], v239 offset:35584\n"
    "v_mfma_f32_16x16x32_bf16 v[216:219], v[92:95], v[96:99], v[216:219]\n"
    "s_waitcnt lgkmcnt(8)\n"
    "v_mfma_f32_16x16x32_bf16 v[168:171], v[96:99], v[124:127], v[168:171]\n"
    "ds_read_b64_tr_b16 v[120:121], v240 offset:35328\n"
    "v_mfma_f32_16x16x32_bf16 v[172:175], v[100:103], v[124:127], v[172:175]\n"
    "ds_read_b64_tr_b16 v[122:123], v240 offset:35584\n"
    "v_mfma_f32_16x16x32_bf16 v[220:223], v[92:95], v[100:103], v[220:223]\n"
    "ds_read_b128 v[104:107], v242 offset:35328\n"
    "s_waitcnt lgkmcnt(9)\n"
    "v_mfma_f32_16x16x32_bf16 v[176:179], v[96:99], v[128:131], v[176:179]\n"
    "ds_read_b128 v[108:111], v242 offset:37376\n"
    "v_mfma_f32_16x16x32_bf16 v[180:183], v[100:103], v[128:131], v[180:183]\n"
    "ds_read_b64_tr_b16 v[124:125], v232 offset:44800\n"
    "v_mfma_f32_16x16x32_bf16 v[224:227], v[96:99], v[96:99], v[224:227]\n"
    "ds_read_b64_tr_b16 v[126:127], v232 offset:45056\n"
    "s_waitcnt lgkmcnt(10)\n"
    "v_mfma_f32_16x16x32_bf16 v[184:187], v[96:99], v[132:135], v[184:187]\n"
    "ds_read_b64_tr_b16 v[128:129], v233 offset:44800\n"
    "v_mfma_f32_16x16x32_bf16 v[188:191], v[100:103], v[132:135], v[188:191]\n"
    "ds_read_b64_tr_b16 v[130:131], v233 offset:45056\n"
    "v_mfma_f32_16x16x32_bf16 v[228:231], v[100:103], v[100:103], v[228:231]\n"
    "s_waitcnt lgkmcnt(10)\n"
    "v_mfma_f32_16x16x32_bf16 v[192:195], v[96:99], v[112:115], v[192:195]\n"
    "ds_read_b64_tr_b16 v[132:133], v234 offset:44800\n"
    "v_mfma_f32_16x16x32_bf16 v[196:199], v[100:103], v[112:115], v[196:199]\n"
    "ds_read_b64_tr_b16 v[134:135], v234 offset:45056\n"
    "s_waitcnt lgkmcnt(10)\n"
    "v_mfma_f32_16x16x32_bf16 v[200:203], v[96:99], v[116:119], v[200:203]\n"
    "ds_read_b64_tr_b16 v[112:113], v235 offset:44800\n"
    "v_mfma_f32_16x16x32_bf16 v[204:207], v[100:103], v[116:119], v[204:207]\n"
    "ds_read_b64_tr_b16 v[114:115], v235 offset:45056\n"
    "s_waitcnt lgkmcnt(10)\n"
    "v_mfma_f32_16x16x32_bf16 v[208:211], v[96:99], v[120:123], v[208:211]\n"
    "ds_read_b64_tr_b16 v[116:117], v236 offset:44800\n"
    "v_mfma_f32_16x16x32_bf16 v[212:215], v[100:103], v[120:123], v[212:215]\n"
    "ds_read_b64_tr_b16 v[118:119], v236 offset:45056\n"
    "v_mfma_f32_16x16x32_bf16 v[88:91], v[120:123], v[92:95], v[88:91]\n"
    "s_waitcnt vmcnt(9)\n"
    "s_barrier\n"
    "s_waitcnt lgkmcnt(8)\n"
    "v_mfma_f32_16x16x32_bf16 v[144:147], v[104:107], v[124:127], v[144:147]\n"
    "s_add_u32 m0, s52, 0\n"
    "ds_read_b64_tr_b16 v[120:121], v237 offset:44800\n"
    "v_mfma_f32_16x16x32_bf16 v[148:151], v[108:111], v[124:127], v[148:151]\n"
    "s_add_u32 s48, %8, 884736\n"
    "ds_read_b64_tr_b16 v[122:123], v237 offset:45056\n"
    "v_mfma_f32_16x16x32_bf16 v[80:83], v[124:127], v[92:95], v[80:83]\n"
    "buffer_load_dwordx4 v246, s[44:47], s48 offen lds\n"
    "s_waitcnt lgkmcnt(8)\n"
    "v_mfma_f32_16x16x32_bf16 v[152:155], v[104:107], v[128:131], v[152:155]\n"
    "s_add_u32 m0, s56, 18944\n"
    "ds_read_b64_tr_b16 v[124:125], v238 offset:44800\n"
    "v_mfma_f32_16x16x32_bf16 v[156:159], v[108:111], v[128:131], v[156:159]\n"
    "s_add_u32 s49, %7, 512\n"
    "ds_read_b64_tr_b16 v[126:127], v238 offset:45056\n"
    "v_mfma_f32_16x16x32_bf16 v[84:87], v[128:131], v[92:95], v[84:87]\n"
    "buffer_load_dwordx4 v243, s[40:43], s49 offen lds\n"
    "s_waitcnt lgkmcnt(8)\n"
    "v_mfma_f32_16x16x32_bf16 v[160:163], v[104:107], v[132:135], v[160:163]\n"
    "s_add_u32 m0, s52, 1056\n"
    "ds_read_b64_tr_b16 v[128:129], v239 offset:44800\n"
    "v_mfma_f32_16x16x32_bf16 v[164:167], v[108:111], v[132:135], v[164:167]\n"
    "s_add_u32 s50, %8, 888192\n"
    "ds_read_b64_tr_b16 v[130:131], v239 offset:45056\n"
    "v_mfma_f32_16x16x32_bf16 v[216:219], v[92:95], v[104:107], v[216:219]\n"
    "buffer_load_dwordx4 v246, s[44:47], s50 offen lds\n"
    "s_waitcnt lgkmcnt(8)\n"
    "v_mfma_f32_16x16x32_bf16 v[168:171], v[104:107], v[112:115], v[168:171]\n"
    "s_add_u32 m0, s56, 19968\n"
    "ds_read_b64_tr_b16 v[132:133], v240 offset:44800\n"
    "v_mfma_f32_16x16x32_bf16 v[172:175], v[108:111], v[112:115], v[172:175]\n"
    "s_add_u32 s51, %7, 9728\n"
    "ds_read_b64_tr_b16 v[134:135], v240 offset:45056\n"
    "v_mfma_f32_16x16x32_bf16 v[220:223], v[92:95], v[108:111], v[220:223]\n"
    "ds_read_b128 v[96:99], v69 offset:0\n"
    "s_waitcnt lgkmcnt(9)\n"
    "v_mfma_f32_16x16x32_bf16 v[176:179], v[104:107], v[116:119], v[176:179]\n"
    "ds_read_b128 v[100:103], v69 offset:2048\n"
    "v_mfma_f32_16x16x32_bf16 v[180:183], v[108:111], v[116:119], v[180:183]\n"
    "ds_read_b64_tr_b16 v[112:113], v60 offset:0\n"
    "v_mfma_f32_16x16x32_bf16 v[224:227], v[104:107], v[104:107], v[224:227]\n"
    "ds_read_b64_tr_b16 v[114:115], v60 offset:256\n"
    "s_waitcnt lgkmcnt(10)\n"
    "v_mfma_f32_16x16x32_bf16 v[184:187], v[104:107], v[120:123], v[184:187]\n"
    "ds_read_b64_tr_b16 v[116:117], v61 offset:0\n"
    "v_mfma_f32_16x16x32_bf16 v[188:191], v[108:111], v[120:123], v[188:191]\n"
    "ds_read_b64_tr_b16 v[118:119], v61 offset:256\n"
    "v_mfma_f32_16x16x32_bf16 v[228:231], v[108:111], v[108:111], v[228:231]\n"
    "buffer_load_dwordx4 v243, s[40:43], s51 offen lds\n"
    "s_waitcnt lgkmcnt(10)\n"
    "v_mfma_f32_16x16x32_bf16 v[192:195], v[104:107], v[124:127], v[192:195]\n"
    "s_add_u32 m0, s52, 9472\n"
    "ds_read_b64_tr_b16 v[120:121], v62 offset:0\n"
    "v_mfma_f32_16x16x32_bf16 v[196:199], v[108:111], v[124:127], v[196:199]\n"
    "s_add_u32 s48, %8, 995328\n"
    "ds_read_b64_tr_b16 v[122:123], v62 offset:256\n"
    "s_waitcnt lgkmcnt(10)\n"
    "v_mfma_f32_16x16x32_bf16 v[200:203], v[104:107], v[128:131], v[200:203]\n"
    "ds_read_b64_tr_b16 v[124:125], v63 offset:0\n"
    "v_mfma_f32_16x16x32_bf16 v[204:207], v[108:111], v[128:131], v[204:207]\n"
    "ds_read_b64_tr_b16 v[126:127], v63 offset:256\n"
    "s_waitcnt lgkmcnt(10)\n"
    "v_mfma_f32_16x16x32_bf16 v[208:211], v[104:107], v[132:135], v[208:211]\n"
    "ds_read_b64_tr_b16 v[128:129], v64 offset:0\n"
    "v_mfma_f32_16x16x32_bf16 v[212:215], v[108:111], v[132:135], v[212:215]\n"
    "ds_read_b64_tr_b16 v[130:131], v64 offset:256\n"
    "v_mfma_f32_16x16x32_bf16 v[88:91], v[132:135], v[92:95], v[88:91]\n"
    "buffer_load_dwordx4 v246, s[44:47], s48 offen lds\n"
    "s_waitcnt lgkmcnt(8)\n"
    "v_mfma_f32_16x16x32_bf16 v[144:147], v[96:99], v[112:115], v[144:147]\n"
    "s_add_u32 m0, s56, 20992\n"
    "ds_read_b64_tr_b16 v[132:133], v65 offset:0\n"
    "v_mfma_f32_16x16x32_bf16 v[148:151], v[100:103], v[112:115], v[148:151]\n"
    "s_add_u32 s49, %7, 18944\n"
    "ds_read_b64_tr_b16 v[134:135], v65 offset:256\n"
    "v_mfma_f32_16x16x32_bf16 v[80:83], v[112:115], v[92:95], v[80:83]\n"
    "buffer_load_dwordx4 v243, s[40:43], s49 offen lds\n"
    "s_waitcnt lgkmcnt(8)\n"
    "v_mfma_f32_16x16x32_bf16 v[152:155], v[96:99], v[116:119], v[152:155]\n"
    "s_add_u32 m0, s52, 10528\n"
    "ds_read_b64_tr_b16 v[112:113], v66 offset:0\n"
    "v_mfma_f32_16x16x32_bf16 v[156:159], v[100:103], v[116:119], v[156:159]\n"
    "s_add_u32 s50, %8, 998784\n"
    "ds_read_b64_tr_b16 v[114:115], v66 offset:256\n"
    "v_mfma_f32_16x16x32_bf16 v[84:87], v[116:119], v[92:95], v[84:87]\n"
    "buffer_load_dwordx4 v246, s[44:47], s50 offen lds\n"
    "s_waitcnt lgkmcnt(8)\n"
    "v_mfma_f32_16x16x32_bf16 v[160:163], v[96:99], v[120:123], v[160:163]\n"
    "s_add_u32 m0, s56, 22016\n"
    "ds_read_b64_tr_b16 v[116:117], v67 offset:0\n"
    "v_mfma_f32_16x16x32_bf16 v[164:167], v[100:103], v[120:123], v[164:167]\n"
    "s_add_u32 s51, %7, 28160\n"
    "ds_read_b64_tr_b16 v[118:119], v67 offset:256\n"
    "v_mfma_f32_16x16x32_bf16 v[216:219], v[92:95], v[96:99], v[216:219]\n"
    "buffer_load_dwordx4 v243, s[40:43], s51 offen lds\n"
    "s_waitcnt lgkmcnt(8)\n"
    "v_mfma_f32_16x16x32_bf16 v[168:171], v[96:99], v[124:127], v[168:171]\n"
    "ds_read_b64_tr_b16 v[120:121], v68 offset:0\n"
    "v_mfma_f32_16x16x32_bf16 v[172:175], v[100:103], v[124:127], v[172:175]\n"
    "ds_read_b64_tr_b16 v[122:123], v68 offset:256\n"
    "v_mfma_f32_16x16x32_bf16 v[220:223], v[92:95], v[100:103], v[220:223]\n"
    "ds_read_b128 v[104:107], v70 offset:0\n"
    "s_waitcnt lgkmcnt(9)\n"
    "v_mfma_f32_16x16x32_bf16 v[176:179], v[96:99], v[128:131], v[176:179]\n"
    "ds_read_b128 v[108:111], v70 offset:2048\n"
    "v_mfma_f32_16x16x32_bf16 v[180:183], v[100:103], v[128:131], v[180:183]\n"
    "ds_read_b64_tr_b16 v[124:125], v60 offset:9472\n"
    "v_mfma_f32_16x16x32_bf16 v[224:227], v[96:99], v[96:99], v[224:227]\n"
    "ds_read_b64_tr_b16 v[126:127], v60 offset:9728\n"
    "s_waitcnt lgkmcnt(10)\n"
    "v_mfma_f32_16x16x32_bf16 v[184:187], v[96:99], v[132:135], v[184:187]\n"
    "ds_read_b64_tr_b16 v[128:129], v61 offset:9472\n"
    "v_mfma_f32_16x16x32_bf16 v[188:191], v[100:103], v[132:135], v[188:191]\n"
    "ds_read_b64_tr_b16 v[130:131], v61 offset:9728\n"
    "v_mfma_f32_16x16x32_bf16 v[228:231], v[100:103], v[100:103], v[228:231]\n"
    "s_add_u32 m0, s55, 8448\n"
    "s_add_u32 s48, %9, 884736\n"
    "s_mov_b32 exec_hi, 0\n"
    "buffer_load_dwordx4 v247, s[44:47], s48 offen lds\n"
    "s_mov_b32 exec_hi, -1\n"
    "s_waitcnt lgkmcnt(10)\n"
    "v_mfma_f32_16x16x32_bf16 v[192:195], v[96:99], v[112:115], v[192:195]\n"
    "ds_read_b64_tr_b16 v[132:133], v62 offset:9472\n"
    "v_mfma_f32_16x16x32_bf16 v[196:199], v[100:103], v[112:115], v[196:199]\n"
    "ds_read_b64_tr_b16 v[134:135], v62 offset:9728\n"
    "s_waitcnt lgkmcnt(10)\n"
    "v_mfma_f32_16x16x32_bf16 v[200:203], v[96:99], v[116:119], v[200:203]\n"
    "ds_read_b64_tr_b16 v[112:113], v63 offset:9472\n"
    "v_mfma_f32_16x16x32_bf16 v[204:207], v[100:103], v[116:119], v[204:207]\n"
    "ds_read_b64_tr_b16 v[114:115], v63 offset:9728\n"
    "s_waitcnt lgkmcnt(10)\n"
    "v_mfma_f32_16x16x32_bf16 v[208:211], v[96:99], v[120:123], v[208:211]\n"
    "ds_read_b64_tr_b16 v[116:117], v64 offset:9472\n"
    "v_mfma_f32_16x16x32_bf16 v[212:215], v[100:103], v[120:123], v[212:215]\n"
    "ds_read_b64_tr_b16 v[118:119], v64 offset:9728\n"
    "v_mfma_f32_16x16x32_bf16 v[88:91], v[120:123], v[92:95], v[88:91]\n"
    "s_waitcnt vmcnt(9)\n"
    "s_barrier\n"
    "s_waitcnt lgkmcnt(8)\n"
    "v_mfma_f32_16x16x32_bf16 v[144:147], v[104:107], v[124:127], v[144:147]\n"
    "s_add_u32 m0, s52, 35328\n"
    "ds_read_b64_tr_b16 v[120:121], v65 offset:9472\n"
    "v_mfma_f32_16x16x32_bf16 v[148:151], v[108:111], v[124:127], v[148:151]\n"
    "s_add_u32 s49, %8, 1105920\n"
    "ds_read_b64_tr_b16 v[122:123], v65 offset:9728\n"
    "v_mfma_f32_16x16x32_bf16 v[80:83], v[124:127], v[92:95], v[80:83]\n"
    "buffer_load_dwordx4 v246, s[44:47], s49 offen lds\n"
    "s_waitcnt lgkmcnt(8)\n"
    "v_mfma_f32_16x16x32_bf16 v[152:155], v[104:107], v[128:131], v[152:155]\n"
    "s_add_u32 m0, s56, 54272\n"
    "ds_read_b64_tr_b16 v[124:125], v66 offset:9472\n"
    "v_mfma_f32_16x16x32_bf16 v[156:159], v[108:111], v[128:131], v[156:159]\n"
    "s_add_u32 s50, %7, 640\n"
    "ds_read_b64_tr_b16 v[126:127], v66 offset:9728\n"
    "v_mfma_f32_16x16x32_bf16 v[84:87], v[128:131], v[92:95], v[84:87]\n"
    "buffer_load_dwordx4 v243, s[40:43], s50 offen lds\n"
    "s_waitcnt lgkmcnt(8)\n"
    "v_mfma_f32_16x16x32_bf16 v[160:163], v[104:107], v[132:135], v[160:163]\n"
    "s_add_u32 m0, s52, 36384\n"
    "ds_read_b64_tr_b16 v[128:129], v67 offset:9472\n"
    "v_mfma_f32_16x16x32_bf16 v[164:167], v[108:111], v[132:135], v[164:167]\n"
    "s_add_u32 s51, %8, 1109376\n"
    "ds_read_b64_tr_b16 v[130:131], v67 offset:9728\n"
    "v_mfma_f32_16x16x32_bf16 v[216:219], v[92:95], v[104:107], v[216:219]\n"
    "buffer_load_dwordx4 v246, s[44:47], s51 offen lds\n"
    "s_waitcnt lgkmcnt(8)\n"
    "v_mfma_f32_16x16x32_bf16 v[168:171], v[104:107], v[112:115], v[168:171]\n"
    "s_add_u32 m0, s56, 55296\n"
    "ds_read_b64_tr_b16 v[132:133], v68 offset:9472\n"
    "v_mfma_f32_16x16x32_bf16 v[172:175], v[108:111], v[112:115], v[172:175]\n"
    "s_add_u32 s48, %7, 9856\n"
    "ds_read_b64_tr_b16 v[134:135], v68 offset:9728\n"
    "v_mfma_f32_16x16x32_bf16 v[220:223], v[92:95], v[108:111], v[220:223]\n"
    "ds_read_b128 v[96:99], v69 offset:35328\n"
    "s_waitcnt lgkmcnt(9)\n"
    "v_mfma_f32_16x16x32_bf16 v[176:179], v[104:107], v[116:119], v[176:179]\n"
    "ds_read_b128 v[100:103], v69 offset:37376\n"
    "v_mfma_f32_16x16x32_bf16 v[180:183], v[108:111], v[116:119], v[180:183]\n"
    "ds_read_b64_tr_b16 v[112:113], v60 offset:35328\n"
    "v_mfma_f32_16x16x32_bf16 v[224:227], v[104:107], v[104:107], v[224:227]\n"
    "ds_read_b64_tr_b16 v[114:115], v60 offset:35584\n"
    "s_waitcnt lgkmcnt(10)\n"
    "v_mfma_f32_16x16x32_bf16 v[184:187], v[104:107], v[120:123], v[184:187]\n"
    "ds_read_b64_tr_b16 v[116:117], v61 offset:35328\n"
    "v_mfma_f32_16x16x32_bf16 v[188:191], v[108:111], v[120:123], v[188:191]\n"
    "ds_read_b64_tr_b16 v[118:119], v61 offset:35584\n"
    "v_mfma_f32_16x16x32_bf16 v[228:231], v[108:111], v[108:111], v[228:231]\n"
    "buffer_load_dwordx4 v243, s[40:43], s48 offen lds\n"
    "s_waitcnt lgkmcnt(10)\n"
    "v_mfma_f32_16x16x32_bf16 v[192:195], v[104:107], v[124:127], v[192:195]\n"
    "s_add_u32 m0, s52, 44800\n"
    "ds_read_b64_tr_b16 v[120:121], v62 offset:35328\n"
    "v_mfma_f32_16x16x32_bf16 v[196:199], v[108:111], v[124:127], v[196:199]\n"
    "s_add_u32 s49, %8, 1216512\n"
    "ds_read_b64_tr_b16 v[122:123], v62 offset:35584\n"
    "s_waitcnt lgkmcnt(10)\n"
    "v_mfma_f32_16x16x32_bf16 v[200:203], v[104:107], v[128:131], v[200:203]\n"
    "ds_read_b64_tr_b16 v[124:125], v63 offset:35328\n"
    "v_mfma_f32_16x16x32_bf16 v[204:207], v[108:111], v[128:131], v[204:207]\n"
    "ds_read_b64_tr_b16 v[126:127], v63 offset:35584\n"
    "s_waitcnt lgkmcnt(10)\n"
    "v_mfma_f32_16x16x32_bf16 v[208:211], v[104:107], v[132:135], v[208:211]\n"
    "ds_read_b64_tr_b16 v[128:129], v64 offset:35328\n"
    "v_mfma_f32_16x16x32_bf16 v[212:215], v[108:111], v[132:135], v[212:215]\n"
    "ds_read_b64_tr_b16 v[130:131], v64 offset:35584\n"
    "v_mfma_f32_16x16x32_bf16 v[88:91], v[132:135], v[92:95], v[88:91]\n"
    "buffer_load_dwordx4 v246, s[44:47], s49 offen lds\n"
    "s_waitcnt lgkmcnt(8)\n"
    "v_mfma_f32_16x16x32_bf16 v[144:147], v[96:99], v[112:115], v[144:147]\n"
    "s_add_u32 m0, s56, 56320\n"
    "ds_read_b64_tr_b16 v[132:133], v65 offset:35328\n"
    "v_mfma_f32_16x16x32_bf16 v[148:151], v[100:103], v[112:115], v[148:151]\n"
    "s_add_u32 s50, %7, 19072\n"
    "ds_read_b64_tr_b16 v[134:135], v65 offset:35584\n"
    "v_mfma_f32_16x16x32_bf16 v[80:83], v[112:115], v[92:95], v[80:83]\n"
    "buffer_load_dwordx4 v243, s[40:43], s50 offen lds\n"
    "s_waitcnt lgkmcnt(8)\n"
    "v_mfma_f32_16x16x32_bf16 v[152:155], v[96:99], v[116:119], v[152:155]\n"
    "s_add_u32 m0, s52, 45856\n"
    "ds_read_b64_tr_b16 v[112:113], v66 offset:35328\n"
    "v_mfma_f32_16x16x32_bf16 v[156:159], v[100:103], v[116:119], v[156:159]\n"
    "s_add_u32 s51, %8, 1219968\n"
    "ds_read_b64_tr_b16 v[114:115], v66 offset:35584\n"
    "v_mfma_f32_16x16x32_bf16 v[84:87], v[116:119], v[92:95], v[84:87]\n"
    "buffer_load_dwordx4 v246, s[44:47], s51 offen lds\n"
    "s_waitcnt lgkmcnt(8)\n"
    "v_mfma_f32_16x16x32_bf16 v[160:163], v[96:99], v[120:123], v[160:163]\n"
    "s_add_u32 m0, s56, 57344\n"
    "ds_read_b64_tr_b16 v[116:117], v67 offset:35328\n"
    "v_mfma_f32_16x16x32_bf16 v[164:167], v[100:103], v[120:123], v[164:167]\n"
    "s_add_u32 s48, %7, 28288\n"
    "ds_read_b64_tr_b16 v[118:119], v67 offset:35584\n"
    "v_mfma_f32_16x16x32_bf16 v[216:219], v[92:95], v[96:99], v[216:219]\n"
    "buffer_load_dwordx4 v243, s[40:43], s48 offen lds\n"
    "s_waitcnt lgkmcnt(8)\n"
    "v_mfma_f32_16x16x32_bf16 v[168:171], v[96:99], v[124:127], v[168:171]\n"
    "ds_read_b64_tr_b16 v[120:121], v68 offset:35328\n"
    "v_mfma_f32_16x16x32_bf16 v[172:175], v[100:103], v[124:127], v[172:175]\n"
    "ds_read_b64_tr_b16 v[122:123], v68 offset:35584\n"
    "v_mfma_f32_16x16x32_bf16 v[220:223], v[92:95], v[100:103], v[220:223]\n"
    "ds_read_b128 v[104:107], v70 offset:35328\n"
    "s_waitcnt lgkmcnt(9)\n"
    "v_mfma_f32_16x16x32_bf16 v[176:179], v[96:99], v[128:131], v[176:179]\n"
    "ds_read_b128 v[108:111], v70 offset:37376\n"
    "v_mfma_f32_16x16x32_bf16 v[180:183], v[100:103], v[128:131], v[180:183]\n"
    "ds_read_b64_tr_b16 v[124:125], v60 offset:44800\n"
    "v_mfma_f32_16x16x32_bf16 v[224:227], v[96:99], v[96:99], v[224:227]\n"
    "ds_read_b64_tr_b16 v[126:127], v60 offset:45056\n"
    "s_waitcnt lgkmcnt(10)\n"
    "v_mfma_f32_16x16x32_bf16 v[184:187], v[96:99], v[132:135], v[184:187]\n"
    "ds_read_b64_tr_b16 v[128:129], v61 offset:44800\n"
    "v_mfma_f32_16x16x32_bf16 v[188:191], v[100:103], v[132:135], v[188:191]\n"
    "ds_read_b64_tr_b16 v[130:131], v61 offset:45056\n"
    "v_mfma_f32_16x16x32_bf16 v[228:231], v[100:103], v[100:103], v[228:231]\n"
    "s_add_u32 m0, s55, 43776\n"
    "s_add_u32 s49, %9, 1105920\n"
    "s_mov_b32 exec_hi, 0\n"
    "buffer_load_dwordx4 v247, s[44:47], s49 offen lds\n"
    "s_mov_b32 exec_hi, -1\n"
    "s_waitcnt lgkmcnt(10)\n"
    "v_mfma_f32_16x16x32_bf16 v[192:195], v[96:99], v[112:115], v[192:195]\n"
    "ds_read_b64_tr_b16 v[132:133], v62 offset:44800\n"
    "v_mfma_f32_16x16x32_bf16 v[196:199], v[100:103], v[112:115], v[196:199]\n"
    "ds_read_b64_tr_b16 v[134:135], v62 offset:45056\n"
    "s_waitcnt lgkmcnt(10)\n"
    "v_mfma_f32_16x16x32_bf16 v[200:203], v[96:99], v[116:119], v[200:203]\n"
    "ds_read_b64_tr_b16 v[112:113], v63 offset:44800\n"
    "v_mfma_f32_16x16x32_bf16 v[204:207], v[100:103], v[116:119], v[204:207]\n"
    "ds_read_b64_tr_b16 v[114:115], v63 offset:45056\n"
    "s_waitcnt lgkmcnt(10)\n"
    "v_mfma_f32_16x16x32_bf16 v[208:211], v[96:99], v[120:123], v[208:211]\n"
    "ds_read_b64_tr_b16 v[116:117], v64 offset:44800\n"
    "v_mfma_f32_16x16x32_bf16 v[212:215], v[100:103], v[120:123], v[212:215]\n"
    "ds_read_b64_tr_b16 v[118:119], v64 offset:45056\n"
    "v_mfma_f32_16x16x32_bf16 v[88:91], v[120:123], v[92:95], v[88:91]\n"
    "s_waitcnt vmcnt(9)\n"
    "s_barrier\n"
    "s_waitcnt lgkmcnt(8)\n"
    "v_mfma_f32_16x16x32_bf16 v[144:147], v[104:107], v[124:127], v[144:147]\n"
    "s_add_u32 m0, s52, 70656\n"
    "ds_read_b64_tr_b16 v[120:121], v65 offset:44800\n"
    "v_mfma_f32_16x16x32_bf16 v[148:151], v[108:111], v[124:127], v[148:151]\n"
    "s_add_u32 s50, %8, 1327104\n"
    "ds_read_b64_tr_b16 v[122:123], v65 offset:45056\n"
    "v_mfma_f32_16x16x32_bf16 v[80:83], v[124:127], v[92:95], v[80:83]\n"
    "buffer_load_dwordx4 v246, s[44:47], s50 offen lds\n"
    "s_waitcnt lgkmcnt(8)\n"
    "v_mfma_f32_16x16x32_bf16 v[152:155], v[104:107], v[128:131], v[152:155]\n"
    "s_add_u32 m0, s56, 89600\n"
    "ds_read_b64_tr_b16 v[124:125], v66 offset:44800\n"
    "v_mfma_f32_16x16x32_bf16 v[156:159], v[108:111], v[128:131], v[156:159]\n"
    "s_add_u32 s51, %7, 768\n"
    "ds_read_b64_tr_b16 v[126:127], v66 offset:45056\n"
    "v_mfma_f32_16x16x32_bf16 v[84:87], v[128:131], v[92:95], v[84:87]\n"
    "buffer_load_dwordx4 v243, s[40:43], s51 offen lds\n"
    "s_waitcnt lgkmcnt(8)\n"
    "v_mfma_f32_16x16x32_bf16 v[160:163], v[104:107], v[132:135], v[160:163]\n"
    "s_add_u32 m0, s52, 71712\n"
    "ds_read_b64_tr_b16 v[128:129], v67 offset:44800\n"
    "v_mfma_f32_16x16x32_bf16 v[164:167], v[108:111], v[132:135], v[164:167]\n"
    "s_add_u32 s48, %8, 1330560\n"
    "ds_read_b64_tr_b16 v[130:131], v67 offset:45056\n"
    "v_mfma_f32_16x16x32_bf16 v[216:219], v[92:95], v[104:107], v[216:219]\n"
    "buffer_load_dwordx4 v246, s[44:47], s48 offen lds\n"
    "s_waitcnt lgkmcnt(8)\n"
    "v_mfma_f32_16x16x32_bf16 v[168:171], v[104:107], v[112:115], v[168:171]\n"
    "s_add_u32 m0, s56, 90624\n"
    "ds_read_b64_tr_b16 v[132:133], v68 offset:44800\n"
    "v_mfma_f32_16x16x32_bf16 v[172:175], v[108:111], v[112:115], v[172:175]\n"
    "s_add_u32 s49, %7, 9984\n"
    "ds_read_b64_tr_b16 v[134:135], v68 offset:45056\n"
    "v_mfma_f32_16x16x32_bf16 v[220:223], v[92:95], v[108:111], v[220:223]\n"
    "ds_read_b128 v[96:99], v241 offset:0\n"
    "s_waitcnt lgkmcnt(9)\n"
    "v_mfma_f32_16x16x32_bf16 v[176:179], v[104:107], v[116:119], v[176:179]\n"
    "ds_read_b128 v[100:103], v241 offset:2048\n"
    "v_mfma_f32_16x16x32_bf16 v[180:183], v[108:111], v[116:119], v[180:183]\n"
    "ds_read_b64_tr_b16 v[112:113], v232 offset:0\n"
    "v_mfma_f32_16x16x32_bf16 v[224:227], v[104:107], v[104:107], v[224:227]\n"
    "ds_read_b64_tr_b16 v[114:115], v232 offset:256\n"
    "s_waitcnt lgkmcnt(10)\n"
    "v_mfma_f32_16x16x32_bf16 v[184:187], v[104:107], v[120:123], v[184:187]\n"
    "ds_read_b64_tr_b16 v[116:117], v233 offset:0\n"
    "v_mfma_f32_16x16x32_bf16 v[188:191], v[108:111], v[120:123], v[188:191]\n"
    "ds_read_b64_tr_b16 v[118:119], v233 offset:256\n"
    "v_mfma_f32_16x16x32_bf16 v[228:231], v[108:111], v[108:111], v[228:231]\n"
    "buffer_load_dwordx4 v243, s[40:43], s49 offen lds\n"
    "s_waitcnt lgkmcnt(10)\n"
    "v_mfma_f32_16x16x32_bf16 v[192:195], v[104:107], v[124:127], v[192:195]\n"
    "s_add_u32 m0, s52, 80128\n"
    "ds_read_b64_tr_b16 v[120:121], v234 offset:0\n"
    "v_mfma_f32_16x16x32_bf16 v[196:199], v[108:111], v[124:127], v[196:199]\n"
    "s_add_u32 s50, %8, 1437696\n"
    "ds_read_b64_tr_b16 v[122:123], v234 offset:256\n"
    "s_waitcnt lgkmcnt(10)\n"
    "v_mfma_f32_16x16x32_bf16 v[200:203], v[104:107], v[128:131], v[200:203]\n"
    "ds_read_b64_tr_b16 v[124:125], v235 offset:0\n"
    "v_mfma_f32_16x16x32_bf16 v[204:207], v[108:111], v[128:131], v[204:207]\n"
    "ds_read_b64_tr_b16 v[126:127], v235 offset:256\n"
    "s_waitcnt lgkmcnt(10)\n"
    "v_mfma_f32_16x16x32_bf16 v[208:211], v[104:107], v[132:135], v[208:211]\n"
    "ds_read_b64_tr_b16 v[128:129], v236 offset:0\n"
    "v_mfma_f32_16x16x32_bf16 v[212:215], v[108:111], v[132:135], v[212:215]\n"
    "ds_read_b64_tr_b16 v[130:131], v236 offset:256\n"
    "v_mfma_f32_16x16x32_bf16 v[88:91], v[132:135], v[92:95], v[88:91]\n"
    "buffer_load_dwordx4 v246, s[44:47], s50 offen lds\n"
    "s_waitcnt lgkmcnt(8)\n"
    "v_mfma_f32_16x16x32_bf16 v[144:147], v[96:99], v[112:115], v[144:147]\n"
    "s_add_u32 m0, s56, 91648\n"
    "ds_read_b64_tr_b16 v[132:133], v237 offset:0\n"
    "v_mfma_f32_16x16x32_bf16 v[148:151], v[100:103], v[112:115], v[148:151]\n"
    "s_add_u32 s51, %7, 19200\n"
    "ds_read_b64_tr_b16 v[134:135], v237 offset:256\n"
    "v_mfma_f32_16x16x32_bf16 v[80:83], v[112:115], v[92:95], v[80:83]\n"
    "buffer_load_dwordx4 v243, s[40:43], s51 offen lds\n"
    "s_waitcnt lgkmcnt(8)\n"
    "v_mfma_f32_16x16x32_bf16 v[152:155], v[96:99], v[116:119], v[152:155]\n"
    "s_add_u32 m0, s52, 81184\n"
    "ds_read_b64_tr_b16 v[112:113], v238 offset:0\n"
    "v_mfma_f32_16x16x32_bf16 v[156:159], v[100:103], v[116:119], v[156:159]\n"
    "s_add_u32 s48, %8, 1441152\n"
    "ds_read_b64_tr_b16 v[114:115], v238 offset:256\n"
    "v_mfma_f32_16x16x32_bf16 v[84:87], v[116:119], v[92:95], v[84:87]\n"
    "buffer_load_dwordx4 v246, s[44:47], s48 offen lds\n"
    "s_waitcnt lgkmcnt(8)\n"
    "v_mfma_f32_16x16x32_bf16 v[160:163], v[96:99], v[120:123], v[160:163]\n"
    "s_add_u32 m0, s56, 92672\n"
    "ds_read_b64_tr_b16 v[116:117], v239 offset:0\n"
    "v_mfma_f32_16x16x32_bf16 v[164:167], v[100:103], v[120:123], v[164:167]\n"
    "s_add_u32 s49, %7, 28416\n"
    "ds_read_b64_tr_b16 v[118:119], v239 offset:256\n"
    "v_mfma_f32_16x16x32_bf16 v[216:219], v[92:95], v[96:99], v[216:219]\n"
    "buffer_load_dwordx4 v243, s[40:43], s49 offen lds\n"
    "s_waitcnt lgkmcnt(8)\n"
    "v_mfma_f32_16x16x32_bf16 v[168:171], v[96:99], v[124:127], v[168:171]\n"
    "ds_read_b64_tr_b16 v[120:121], v240 offset:0\n"
    "v_mfma_f32_16x16x32_bf16 v[172:175], v[100:103], v[124:127], v[172:175]\n"
    "ds_read_b64_tr_b16 v[122:123], v240 offset:256\n"
    "v_mfma_f32_16x16x32_bf16 v[220:223], v[92:95], v[100:103], v[220:223]\n"
    "ds_read_b128 v[104:107], v242 offset:0\n"
    "s_waitcnt lgkmcnt(9)\n"
    "v_mfma_f32_16x16x32_bf16 v[176:179], v[96:99], v[128:131], v[176:179]\n"
    "ds_read_b128 v[108:111], v242 offset:2048\n"
    "v_mfma_f32_16x16x32_bf16 v[180:183], v[100:103], v[128:131], v[180:183]\n"
    "ds_read_b64_tr_b16 v[124:125], v232 offset:9472\n"
    "v_mfma_f32_16x16x32_bf16 v[224:227], v[96:99], v[96:99], v[224:227]\n"
    "ds_read_b64_tr_b16 v[126:127], v232 offset:9728\n"
    "s_waitcnt lgkmcnt(10)\n"
    "v_mfma_f32_16x16x32_bf16 v[184:187], v[96:99], v[132:135], v[184:187]\n"
    "ds_read_b64_tr_b16 v[128:129], v233 offset:9472\n"
    "v_mfma_f32_16x16x32_bf16 v[188:191], v[100:103], v[132:135], v[188:191]\n"
    "ds_read_b64_tr_b16 v[130:131], v233 offset:9728\n"
    "v_mfma_f32_16x16x32_bf16 v[228:231], v[100:103], v[100:103], v[228:231]\n"
    "s_add_u32 m0, s55, 79104\n"
    "s_add_u32 s50, %9, 1327104\n"
    "s_mov_b32 exec_hi, 0\n"
    "buffer_load_dwordx4 v247, s[44:47], s50 offen lds\n"
    "s_mov_b32 exec_hi, -1\n"
    "s_waitcnt lgkmcnt(10)\n"
    "v_mfma_f32_16x16x32_bf16 v[192:195], v[96:99], v[112:115], v[192:195]\n"
    "ds_read_b64_tr_b16 v[132:133], v234 offset:9472\n"
    "v_mfma_f32_16x16x32_bf16 v[196:199], v[100:103], v[112:115], v[196:199]\n"
    "ds_read_b64_tr_b16 v[134:135], v234 offset:9728\n"
    "s_waitcnt lgkmcnt(10)\n"
    "v_mfma_f32_16x16x32_bf16 v[200:203], v[96:99], v[116:119], v[200:203]\n"
    "ds_read_b64_tr_b16 v[112:113], v235 offset:9472\n"
    "v_mfma_f32_16x16x32_bf16 v[204:207], v[100:103], v[116:119], v[204:207]\n"
    "ds_read_b64_tr_b16 v[114:115], v235 offset:9728\n"
    "s_waitcnt lgkmcnt(10)\n"
    "v_mfma_f32_16x16x32_bf16 v[208:211], v[96:99], v[120:123], v[208:211]\n"
    "ds_read_b64_tr_b16 v[116:117], v236 offset:9472\n"
    "v_mfma_f32_16x16x32_bf16 v[212:215], v[100:103], v[120:123], v[212:215]\n"
    "ds_read_b64_tr_b16 v[118:119], v236 offset:9728\n"
    "v_mfma_f32_16x16x32_bf16 v[88:91], v[120:123], v[92:95], v[88:91]\n"
    "s_waitcnt vmcnt(9)\n"
    "s_barrier\n"
    "s_waitcnt lgkmcnt(8)\n"
    "v_mfma_f32_16x16x32_bf16 v[144:147], v[104:107], v[124:127], v[144:147]\n"
    "s_add_u32 m0, s52, 105984\n"
    "ds_read_b64_tr_b16 v[120:121], v237 offset:9472\n"
    "v_mfma_f32_16x16x32_bf16 v[148:151], v[108:111], v[124:127], v[148:151]\n"
    "s_add_u32 s51, %8, 1548288\n"
    "ds_read_b64_tr_b16 v[122:123], v237 offset:9728\n"
    "v_mfma_f32_16x16x32_bf16 v[80:83], v[124:127], v[92:95], v[80:83]\n"
    "buffer_load_dwordx4 v246, s[44:47], s51 offen lds\n"
    "s_waitcnt lgkmcnt(8)\n"
    "v_mfma_f32_16x16x32_bf16 v[152:155], v[104:107], v[128:131], v[152:155]\n"
    "s_add_u32 m0, s56, 124928\n"
    "ds_read_b64_tr_b16 v[124:125], v238 offset:9472\n"
    "v_mfma_f32_16x16x32_bf16 v[156:159], v[108:111], v[128:131], v[156:159]\n"
    "s_add_u32 s48, %7, 896\n"
    "ds_read_b64_tr_b16 v[126:127], v238 offset:9728\n"
    "v_mfma_f32_16x16x32_bf16 v[84:87], v[128:131], v[92:95], v[84:87]\n"
    "buffer_load_dwordx4 v243, s[40:43], s48 offen lds\n"
    "s_waitcnt lgkmcnt(8)\n"
    "v_mfma_f32_16x16x32_bf16 v[160:163], v[104:107], v[132:135], v[160:163]\n"
    "s_add_u32 m0, s52, 107040\n"
    "ds_read_b64_tr_b16 v[128:129], v239 offset:9472\n"
    "v_mfma_f32_16x16x32_bf16 v[164:167], v[108:111], v[132:135], v[164:167]\n"
    "s_add_u32 s49, %8, 1551744\n"
    "ds_read_b64_tr_b16 v[130:131], v239 offset:9728\n"
    "v_mfma_f32_16x16x32_bf16 v[216:219], v[92:95], v[104:107], v[216:219]\n"
    "buffer_load_dwordx4 v246, s[44:47], s49 offen lds\n"
    "s_waitcnt lgkmcnt(8)\n"
    "v_mfma_f32_16x16x32_bf16 v[168:171], v[104:107], v[112:115], v[168:171]\n"
    "s_add_u32 m0, s56, 125952\n"
    "ds_read_b64_tr_b16 v[132:133], v240 offset:9472\n"
    "v_mfma_f32_16x16x32_bf16 v[172:175], v[108:111], v[112:115], v[172:175]\n"
    "s_add_u32 s50, %7, 10112\n"
    "ds_read_b64_tr_b16 v[134:135], v240 offset:9728\n"
    "v_mfma_f32_16x16x32_bf16 v[220:223], v[92:95], v[108:111], v[220:223]\n"
    "ds_read_b128 v[96:99], v241 offset:35328\n"
    "s_waitcnt lgkmcnt(9)\n"
    "v_mfma_f32_16x16x32_bf16 v[176:179], v[104:107], v[116:119], v[176:179]\n"
    "ds_read_b128 v[100:103], v241 offset:37376\n"
    "v_mfma_f32_16x16x32_bf16 v[180:183], v[108:111], v[116:119], v[180:183]\n"
    "ds_read_b64_tr_b16 v[112:113], v232 offset:35328\n"
    "v_mfma_f32_16x16x32_bf16 v[224:227], v[104:107], v[104:107], v[224:227]\n"
    "ds_read_b64_tr_b16 v[114:115], v232 offset:35584\n"
    "s_waitcnt lgkmcnt(10)\n"
    "v_mfma_f32_16x16x32_bf16 v[184:187], v[104:107], v[120:123], v[184:187]\n"
    "ds_read_b64_tr_b16 v[116:117], v233 offset:35328\n"
    "v_mfma_f32_16x16x32_bf16 v[188:191], v[108:111], v[120:123], v[188:191]\n"
    "ds_read_b64_tr_b16 v[118:119], v233 offset:35584\n"
    "v_mfma_f32_16x16x32_bf16 v[228:231], v[108:111], v[108:111], v[228:231]\n"
    "buffer_load_dwordx4 v243, s[40:43], s50 offen lds\n"
    "s_waitcnt lgkmcnt(10)\n"
    "v_mfma_f32_16x16x32_bf16 v[192:195], v[104:107], v[124:127], v[192:195]\n"
    "s_add_u32 m0, s52, 115456\n"
    "ds_read_b64_tr_b16 v[120:121], v234 offset:35328\n"
    "v_mfma_f32_16x16x32_bf16 v[196:199], v[108:111], v[124:127], v[196:199]\n"
    "s_add_u32 s51, %8, 1658880\n"
    "ds_read_b64_tr_b16 v[122:123], v234 offset:35584\n"
    "s_waitcnt lgkmcnt(10)\n"
    "v_mfma_f32_16x16x32_bf16 v[200:203], v[104:107], v[128:131], v[200:203]\n"
    "ds_read_b64_tr_b16 v[124:125], v235 offset:35328\n"
    "v_mfma_f32_16x16x32_bf16 v[204:207], v[108:111], v[128:131], v[204:207]\n"
    "ds_read_b64_tr_b16 v[126:127], v235 offset:35584\n"
    "s_waitcnt lgkmcnt(10)\n"
    "v_mfma_f32_16x16x32_bf16 v[208:211], v[104:107], v[132:135], v[208:211]\n"
    "ds_read_b64_tr_b16 v[128:129], v236 offset:35328\n"
    "v_mfma_f32_16x16x32_bf16 v[212:215], v[108:111], v[132:135], v[212:215]\n"
    "ds_read_b64_tr_b16 v[130:131], v236 offset:35584\n"
    "v_mfma_f32_16x16x32_bf16 v[88:91], v[132:135], v[92:95], v[88:91]\n"
    "buffer_load_dwordx4 v246, s[44:47], s51 offen lds\n"
    "s_waitcnt lgkmcnt(8)\n"
    "v_mfma_f32_16x16x32_bf16 v[144:147], v[96:99], v[112:115], v[144:147]\n"
    "s_add_u32 m0, s56, 126976\n"
    "ds_read_b64_tr_b16 v[132:133], v237 offset:35328\n"
    "v_mfma_f32_16x16x32_bf16 v[148:151], v[100:103], v[112:115], v[148:151]\n"
    "s_add_u32 s48, %7, 19328\n"
    "ds_read_b64_tr_b16 v[134:135], v237 offset:35584\n"
    "v_mfma_f32_16x16x32_bf16 v[80:83], v[112:115], v[92:95], v[80:83]\n"
    "buffer_load_dwordx4 v243, s[40:43], s48 offen lds\n"
    "s_waitcnt lgkmcnt(8)\n"
    "v_mfma_f32_16x16x32_bf16 v[152:155], v[96:99], v[116:119], v[152:155]\n"
    "s_add_u32 m0, s52, 116512\n"
    "ds_read_b64_tr_b16 v[112:113], v238 offset:35328\n"
    "v_mfma_f32_16x16x32_bf16 v[156:159], v[100:103], v[116:119], v[156:159]\n"
    "s_add_u32 s49, %8, 1662336\n"
    "ds_read_b64_tr_b16 v[114:115], v238 offset:35584\n"
    "v_mfma_f32_16x16x32_bf16 v[84:87], v[116:119], v[92:95], v[84:87]\n"
    "buffer_load_dwordx4 v246, s[44:47], s49 offen lds\n"
    "s_waitcnt lgkmcnt(8)\n"
    "v_mfma_f32_16x16x32_bf16 v[160:163], v[96:99], v[120:123], v[160:163]\n"
    "s_add_u32 m0, s56, 128000\n"
    "ds_read_b64_tr_b16 v[116:117], v239 offset:35328\n"
    "v_mfma_f32_16x16x32_bf16 v[164:167], v[100:103], v[120:123], v[164:167]\n"
    "s_add_u32 s50, %7, 28544\n"
    "ds_read_b64_tr_b16 v[118:119], v239 offset:35584\n"
    "v_mfma_f32_16x16x32_bf16 v[216:219], v[92:95], v[96:99], v[216:219]\n"
    "buffer_load_dwordx4 v243, s[40:43], s50 offen lds\n"
    "s_waitcnt lgkmcnt(8)\n"
    "v_mfma_f32_16x16x32_bf16 v[168:171], v[96:99], v[124:127], v[168:171]\n"
    "ds_read_b64_tr_b16 v[120:121], v240 offset:35328\n"
    "v_mfma_f32_16x16x32_bf16 v[172:175], v[100:103], v[124:127], v[172:175]\n"
    "ds_read_b64_tr_b16 v[122:123], v240 offset:35584\n"
    "v_mfma_f32_16x16x32_bf16 v[220:223], v[92:95], v[100:103], v[220:223]\n"
    "ds_read_b128 v[104:107], v242 offset:35328\n"
    "s_waitcnt lgkmcnt(9)\n"
    "v_mfma_f32_16x16x32_bf16 v[176:179], v[96:99], v[128:131], v[176:179]\n"
    "ds_read_b128 v[108:111], v242 offset:37376\n"
    "v_mfma_f32_16x16x32_bf16 v[180:183], v[100:103], v[128:131], v[180:183]\n"
    "ds_read_b64_tr_b16 v[124:125], v232 offset:44800\n"
    "v_mfma_f32_16x16x32_bf16 v[224:227], v[96:99], v[96:99], v[224:227]\n"
    "ds_read_b64_tr_b16 v[126:127], v232 offset:45056\n"
    "s_waitcnt lgkmcnt(10)\n"
    "v_mfma_f32_16x16x32_bf16 v[184:187], v[96:99], v[132:135], v[184:187]\n"
    "ds_read_b64_tr_b16 v[128:129], v233 offset:44800\n"
    "v_mfma_f32_16x16x32_bf16 v[188:191], v[100:103], v[132:135], v[188:191]\n"
    "ds_read_b64_tr_b16 v[130:131], v233 offset:45056\n"
    "v_mfma_f32_16x16x32_bf16 v[228:231], v[100:103], v[100:103], v[228:231]\n"
    "s_add_u32 m0, s55, 114432\n"
    "s_add_u32 s51, %9, 1548288\n"
    "s_mov_b32 exec_hi, 0\n"
    "buffer_load_dwordx4 v247, s[44:47], s51 offen lds\n"
    "s_mov_b32 exec_hi, -1\n"
    "s_waitcnt lgkmcnt(10)\n"
    "v_mfma_f32_16x16x32_bf16 v[192:195], v[96:99], v[112:115], v[192:195]\n"
    "ds_read_b64_tr_b16 v[132:133], v234 offset:44800\n"
    "v_mfma_f32_16x16x32_bf16 v[196:199], v[100:103], v[112:115], v[196:199]\n"
    "ds_read_b64_tr_b16 v[134:135], v234 offset:45056\n"
    "s_waitcnt lgkmcnt(10)\n"
    "v_mfma_f32_16x16x32_bf16 v[200:203], v[96:99], v[116:119], v[200:203]\n"
    "ds_read_b64_tr_b16 v[112:113], v235 offset:44800\n"
    "v_mfma_f32_16x16x32_bf16 v[204:207], v[100:103], v[116:119], v[204:207]\n"
    "ds_read_b64_tr_b16 v[114:115], v235 offset:45056\n"
    "s_waitcnt lgkmcnt(10)\n"
    "v_mfma_f32_16x16x32_bf16 v[208:211], v[96:99], v[120:123], v[208:211]\n"
    "ds_read_b64_tr_b16 v[116:117], v236 offset:44800\n"
    "v_mfma_f32_16x16x32_bf16 v[212:215], v[100:103], v[120:123], v[212:215]\n"
    "ds_read_b64_tr_b16 v[118:119], v236 offset:45056\n"
    "v_mfma_f32_16x16x32_bf16 v[88:91], v[120:123], v[92:95], v[88:91]\n"
    "s_waitcnt vmcnt(9)\n"
    "s_barrier\n"
    "s_waitcnt lgkmcnt(8)\n"
    "v_mfma_f32_16x16x32_bf16 v[144:147], v[104:107], v[124:127], v[144:147]\n"
    "s_add_u32 m0, s52, 0\n"
    "ds_read_b64_tr_b16 v[120:121], v237 offset:44800\n"
    "v_mfma_f32_16x16x32_bf16 v[148:151], v[108:111], v[124:127], v[148:151]\n"
    "s_add_u32 s48, %8, 1769472\n"
    "ds_read_b64_tr_b16 v[122:123], v237 offset:45056\n"
    "v_mfma_f32_16x16x32_bf16 v[80:83], v[124:127], v[92:95], v[80:83]\n"
    "buffer_load_dwordx4 v246, s[44:47], s48 offen lds\n"
    "s_waitcnt lgkmcnt(8)\n"
    "v_mfma_f32_16x16x32_bf16 v[152:155], v[104:107], v[128:131], v[152:155]\n"
    "s_add_u32 m0, s56, 18944\n"
    "ds_read_b64_tr_b16 v[124:125], v238 offset:44800\n"
    "v_mfma_f32_16x16x32_bf16 v[156:159], v[108:111], v[128:131], v[156:159]\n"
    "s_add_u32 s49, %7, 1024\n"
    "ds_read_b64_tr_b16 v[126:127], v238 offset:45056\n"
    "v_mfma_f32_16x16x32_bf16 v[84:87], v[128:131], v[92:95], v[84:87]\n"
    "buffer_load_dwordx4 v243, s[40:43], s49 offen lds\n"
    "s_waitcnt lgkmcnt(8)\n"
    "v_mfma_f32_16x16x32_bf16 v[160:163], v[104:107], v[132:135], v[160:163]\n"
    "s_add_u32 m0, s52, 1056\n"
    "ds_read_b64_tr_b16 v[128:129], v239 offset:44800\n"
    "v_mfma_f32_16x16x32_bf16 v[164:167], v[108:111], v[132:135], v[164:167]\n"
    "s_add_u32 s50, %8, 1772928\n"
    "ds_read_b64_tr_b16 v[130:131], v239 offset:45056\n"
    "v_mfma_f32_16x16x32_bf16 v[216:219], v[92:95], v[104:107], v[216:219]\n"
    "buffer_load_dwordx4 v246, s[44:47], s50 offen lds\n"
    "s_waitcnt lgkmcnt(8)\n"
    "v_mfma_f32_16x16x32_bf16 v[168:171], v[104:107], v[112:115], v[168:171]\n"
    "s_add_u32 m0, s56, 19968\n"
    "ds_read_b64_tr_b16 v[132:133], v240 offset:44800\n"
    "v_mfma_f32_16x16x32_bf16 v[172:175], v[108:111], v[112:115], v[172:175]\n"
    "s_add_u32 s51, %7, 10240\n"
    "ds_read_b64_tr_b16 v[134:135], v240 offset:45056\n"
    "v_mfma_f32_16x16x32_bf16 v[220:223], v[92:95], v[108:111], v[220:223]\n"
    "ds_read_b128 v[96:99], v69 offset:0\n"
    "s_waitcnt lgkmcnt(9)\n"
    "v_mfma_f32_16x16x32_bf16 v[176:179], v[104:107], v[116:119], v[176:179]\n"
    "ds_read_b128 v[100:103], v69 offset:2048\n"
    "v_mfma_f32_16x16x32_bf16 v[180:183], v[108:111], v[116:119], v[180:183]\n"
    "ds_read_b64_tr_b16 v[112:113], v60 offset:0\n"
    "v_mfma_f32_16x16x32_bf16 v[224:227], v[104:107], v[104:107], v[224:227]\n"
    "ds_read_b64_tr_b16 v[114:115], v60 offset:256\n"
    "s_waitcnt lgkmcnt(10)\n"
    "v_mfma_f32_16x16x32_bf16 v[184:187], v[104:107], v[120:123], v[184:187]\n"
    "ds_read_b64_tr_b16 v[116:117], v61 offset:0\n"
    "v_mfma_f32_16x16x32_bf16 v[188:191], v[108:111], v[120:123], v[188:191]\n"
    "ds_read_b64_tr_b16 v[118:119], v61 offset:256\n"
    "v_mfma_f32_16x16x32_bf16 v[228:231], v[108:111], v[108:111], v[228:231]\n"
    "buffer_load_dwordx4 v243, s[40:43], s51 offen lds\n"
    "s_waitcnt lgkmcnt(10)\n"
    "v_mfma_f32_16x16x32_bf16 v[192:195], v[104:107], v[124:127], v[192:195]\n"
    "s_add_u32 m0, s52, 9472\n"
    "ds_read_b64_tr_b16 v[120:121], v62 offset:0\n"
    "v_mfma_f32_16x16x32_bf16 v[196:199], v[108:111], v[124:127], v[196:199]\n"
    "s_add_u32 s48, %8, 1880064\n"
    "ds_read_b64_tr_b16 v[122:123], v62 offset:256\n"
    "s_waitcnt lgkmcnt(10)\n"
    "v_mfma_f32_16x16x32_bf16 v[200:203], v[104:107], v[128:131], v[200:203]\n"
    "ds_read_b64_tr_b16 v[124:125], v63 offset:0\n"
    "v_mfma_f32_16x16x32_bf16 v[204:207], v[108:111], v[128:131], v[204:207]\n"
    "ds_read_b64_tr_b16 v[126:127], v63 offset:256\n"
    "s_waitcnt lgkmcnt(10)\n"
    "v_mfma_f32_16x16x32_bf16 v[208:211], v[104:107], v[132:135], v[208:211]\n"
    "ds_read_b64_tr_b16 v[128:129], v64 offset:0\n"
    "v_mfma_f32_16x16x32_bf16 v[212:215], v[108:111], v[132:135], v[212:215]\n"
    "ds_read_b64_tr_b16 v[130:131], v64 offset:256\n"
    "v_mfma_f32_16x16x32_bf16 v[88:91], v[132:135], v[92:95], v[88:91]\n"
    "buffer_load_dwordx4 v246, s[44:47], s48 offen lds\n"
    "s_waitcnt lgkmcnt(8)\n"
    "v_mfma_f32_16x16x32_bf16 v[144:147], v[96:99], v[112:115], v[144:147]\n"
    "s_add_u32 m0, s56, 20992\n"
    "ds_read_b64_tr_b16 v[132:133], v65 offset:0\n"
    "v_mfma_f32_16x16x32_bf16 v[148:151], v[100:103], v[112:115], v[148:151]\n"
    "s_add_u32 s49, %7, 19456\n"
    "ds_read_b64_tr_b16 v[134:135], v65 offset:256\n"
    "v_mfma_f32_16x16x32_bf16 v[80:83], v[112:115], v[92:95], v[80:83]\n"
    "buffer_load_dwordx4 v243, s[40:43], s49 offen lds\n"
    "s_waitcnt lgkmcnt(8)\n"
    "v_mfma_f32_16x16x32_bf16 v[152:155], v[96:99], v[116:119], v[152:155]\n"
    "s_add_u32 m0, s52, 10528\n"
    "ds_read_b64_tr_b16 v[112:113], v66 offset:0\n"
    "v_mfma_f32_16x16x32_bf16 v[156:159], v[100:103], v[116:119], v[156:159]\n"
    "s_add_u32 s50, %8, 1883520\n"
    "ds_read_b64_tr_b16 v[114:115], v66 offset:256\n"
    "v_mfma_f32_16x16x32_bf16 v[84:87], v[116:119], v[92:95], v[84:87]\n"
    "buffer_load_dwordx4 v246, s[44:47], s50 offen lds\n"
    "s_waitcnt lgkmcnt(8)\n"
    "v_mfma_f32_16x16x32_bf16 v[160:163], v[96:99], v[120:123], v[160:163]\n"
    "s_add_u32 m0, s56, 22016\n"
    "ds_read_b64_tr_b16 v[116:117], v67 offset:0\n"
    "v_mfma_f32_16x16x32_bf16 v[164:167], v[100:103], v[120:123], v[164:167]\n"
    "s_add_u32 s51, %7, 28672\n"
    "ds_read_b64_tr_b16 v[118:119], v67 offset:256\n"
    "v_mfma_f32_16x16x32_bf16 v[216:219], v[92:95], v[96:99], v[216:219]\n"
    "buffer_load_dwordx4 v243, s[40:43], s51 offen lds\n"
    "s_waitcnt lgkmcnt(8)\n"
    "v_mfma_f32_16x16x32_bf16 v[168:171], v[96:99], v[124:127], v[168:171]\n"
    "ds_read_b64_tr_b16 v[120:121], v68 offset:0\n"
    "v_mfma_f32_16x16x32_bf16 v[172:175], v[100:103], v[124:127], v[172:175]\n"
    "ds_read_b64_tr_b16 v[122:123], v68 offset:256\n"
    "v_mfma_f32_16x16x32_bf16 v[220:223], v[92:95], v[100:103], v[220:223]\n"
    "ds_read_b128 v[104:107], v70 offset:0\n"
    "s_waitcnt lgkmcnt(9)\n"
    "v_mfma_f32_16x16x32_bf16 v[176:179], v[96:99], v[128:131], v[176:179]\n"
    "ds_read_b128 v[108:111], v70 offset:2048\n"
    "v_mfma_f32_16x16x32_bf16 v[180:183], v[100:103], v[128:131], v[180:183]\n"
    "ds_read_b64_tr_b16 v[124:125], v60 offset:9472\n"
    "v_mfma_f32_16x16x32_bf16 v[224:227], v[96:99], v[96:99], v[224:227]\n"
    "ds_read_b64_tr_b16 v[126:127], v60 offset:9728\n"
    "s_waitcnt lgkmcnt(10)\n"
    "v_mfma_f32_16x16x32_bf16 v[184:187], v[96:99], v[132:135], v[184:187]\n"
    "ds_read_b64_tr_b16 v[128:129], v61 offset:9472\n"
    "v_mfma_f32_16x16x32_bf16 v[188:191], v[100:103], v[132:135], v[188:191]\n"
    "ds_read_b64_tr_b16 v[130:131], v61 offset:9728\n"
    "v_mfma_f32_16x16x32_bf16 v[228:231], v[100:103], v[100:103], v[228:231]\n"
    "s_add_u32 m0, s55, 8448\n"
    "s_add_u32 s48, %9, 1769472\n"
    "s_mov_b32 exec_hi, 0\n"
    "buffer_load_dwordx4 v247, s[44:47], s48 offen lds\n"
    "s_mov_b32 exec_hi, -1\n"
    "s_waitcnt lgkmcnt(10)\n"
    "v_mfma_f32_16x16x32_bf16 v[192:195], v[96:99], v[112:115], v[192:195]\n"
    "ds_read_b64_tr_b16 v[132:133], v62 offset:9472\n"
    "v_mfma_f32_16x16x32_bf16 v[196:199], v[100:103], v[112:115], v[196:199]\n"
    "ds_read_b64_tr_b16 v[134:135], v62 offset:9728\n"
    "s_waitcnt lgkmcnt(10)\n"
    "v_mfma_f32_16x16x32_bf16 v[200:203], v[96:99], v[116:119], v[200:203]\n"
    "ds_read_b64_tr_b16 v[112:113], v63 offset:9472\n"
    "v_mfma_f32_16x16x32_bf16 v[204:207], v[100:103], v[116:119], v[204:207]\n"
    "ds_read_b64_tr_b16 v[114:115], v63 offset:9728\n"
    "s_waitcnt lgkmcnt(10)\n"
    "v_mfma_f32_16x16x32_bf16 v[208:211], v[96:99], v[120:123], v[208:211]\n"
    "ds_read_b64_tr_b16 v[116:117], v64 offset:9472\n"
    "v_mfma_f32_16x16x32_bf16 v[212:215], v[100:103], v[120:123], v[212:215]\n"
    "ds_read_b64_tr_b16 v[118:119], v64 offset:9728\n"
    "v_mfma_f32_16x16x32_bf16 v[88:91], v[120:123], v[92:95], v[88:91]\n"
    "s_waitcnt vmcnt(9)\n"
    "s_barrier\n"
    "s_waitcnt lgkmcnt(8)\n"
    "v_mfma_f32_16x16x32_bf16 v[144:147], v[104:107], v[124:127], v[144:147]\n"
    "ds_read_b64_tr_b16 v[120:121], v65 offset:9472\n"
    "v_mfma_f32_16x16x32_bf16 v[148:151], v[108:111], v[124:127], v[148:151]\n"
    "ds_read_b64_tr_b16 v[122:123], v65 offset:9728\n"
    "v_mfma_f32_16x16x32_bf16 v[80:83], v[124:127], v[92:95], v[80:83]\n"
    "s_waitcnt lgkmcnt(8)\n"
    "v_mfma_f32_16x16x32_bf16 v[152:155], v[104:107], v[128:131], v[152:155]\n"
    "ds_read_b64_tr_b16 v[124:125], v66 offset:9472\n"
    "v_mfma_f32_16x16x32_bf16 v[156:159], v[108:111], v[128:131], v[156:159]\n"
    "ds_read_b64_tr_b16 v[126:127], v66 offset:9728\n"
    "v_mfma_f32_16x16x32_bf16 v[84:87], v[128:131], v[92:95], v[84:87]\n"
    "s_waitcnt lgkmcnt(8)\n"
    "v_mfma_f32_16x16x32_bf16 v[160:163], v[104:107], v[132:135], v[160:163]\n"
    "ds_read_b64_tr_b16 v[128:129], v67 offset:9472\n"
    "v_mfma_f32_16x16x32_bf16 v[164:167], v[108:111], v[132:135], v[164:167]\n"
    "ds_read_b64_tr_b16 v[130:131], v67 offset:9728\n"
    "v_mfma_f32_16x16x32_bf16 v[216:219], v[92:95], v[104:107], v[216:219]\n"
    "s_waitcnt lgkmcnt(8)\n"
    "v_mfma_f32_16x16x32_bf16 v[168:171], v[104:107], v[112:115], v[168:171]\n"
    "ds_read_b64_tr_b16 v[132:133], v68 offset:9472\n"
    "v_mfma_f32_16x16x32_bf16 v[172:175], v[108:111], v[112:115], v[172:175]\n"
    "ds_read_b64_tr_b16 v[134:135], v68 offset:9728\n"
    "v_mfma_f32_16x16x32_bf16 v[220:223], v[92:95], v[108:111], v[220:223]\n"
    "ds_read_b128 v[96:99], v69 offset:35328\n"
    "s_waitcnt lgkmcnt(9)\n"
    "v_mfma_f32_16x16x32_bf16 v[176:179], v[104:107], v[116:119], v[176:179]\n"
    "ds_read_b128 v[100:103], v69 offset:37376\n"
    "v_mfma_f32_16x16x32_bf16 v[180:183], v[108:111], v[116:119], v[180:183]\n"
    "ds_read_b64_tr_b16 v[112:113], v60 offset:35328\n"
    "v_mfma_f32_16x16x32_bf16 v[224:227], v[104:107], v[104:107], v[224:227]\n"
    "ds_read_b64_tr_b16 v[114:115], v60 offset:35584\n"
    "s_waitcnt lgkmcnt(10)\n"
    "v_mfma_f32_16x16x32_bf16 v[184:187], v[104:107], v[120:123], v[184:187]\n"
    "ds_read_b64_tr_b16 v[116:117], v61 offset:35328\n"
    "v_mfma_f32_16x16x32_bf16 v[188:191], v[108:111], v[120:123], v[188:191]\n"
    "ds_read_b64_tr_b16 v[118:119], v61 offset:35584\n"
    "v_mfma_f32_16x16x32_bf16 v[228:231], v[108:111], v[108:111], v[228:231]\n"
    "s_waitcnt lgkmcnt(10)\n"
    "v_mfma_f32_16x16x32_bf16 v[192:195], v[104:107], v[124:127], v[192:195]\n"
    "ds_read_b64_tr_b16 v[120:121], v62 offset:35328\n"
    "v_mfma_f32_16x16x32_bf16 v[196:199], v[108:111], v[124:127], v[196:199]\n"
    "ds_read_b64_tr_b16 v[122:123], v62 offset:35584\n"
    "s_waitcnt lgkmcnt(10)\n"
    "v_mfma_f32_16x16x32_bf16 v[200:203], v[104:107], v[128:131], v[200:203]\n"
    "ds_read_b64_tr_b16 v[124:125], v63 offset:35328\n"
    "v_mfma_f32_16x16x32_bf16 v[204:207], v[108:111], v[128:131], v[204:207]\n"
    "ds_read_b64_tr_b16 v[126:127], v63 offset:35584\n"
    "s_waitcnt lgkmcnt(10)\n"
    "v_mfma_f32_16x16x32_bf16 v[208:211], v[104:107], v[132:135], v[208:211]\n"
    "ds_read_b64_tr_b16 v[128:129], v64 offset:35328\n"
    "v_mfma_f32_16x16x32_bf16 v[212:215], v[108:111], v[132:135], v[212:215]\n"
    "ds_read_b64_tr_b16 v[130:131], v64 offset:35584\n"
    "v_mfma_f32_16x16x32_bf16 v[88:91], v[132:135], v[92:95], v[88:91]\n"
    "s_waitcnt lgkmcnt(8)\n"
    "v_mfma_f32_16x16x32_bf16 v[144:147], v[96:99], v[112:115], v[144:147]\n"
    "ds_read_b64_tr_b16 v[132:133], v65 offset:35328\n"
    "v_mfma_f32_16x16x32_bf16 v[148:151], v[100:103], v[112:115], v[148:151]\n"
    "ds_read_b64_tr_b16 v[134:135], v65 offset:35584\n"
    "v_mfma_f32_16x16x32_bf16 v[80:83], v[112:115], v[92:95], v[80:83]\n"
    "s_waitcnt lgkmcnt(8)\n"
    "v_mfma_f32_16x16x32_bf16 v[152:155], v[96:99], v[116:119], v[152:155]\n"
    "ds_read_b64_tr_b16 v[112:113], v66 offset:35328\n"
    "v_mfma_f32_16x16x32_bf16 v[156:159], v[100:103], v[116:119], v[156:159]\n"
    "ds_read_b64_tr_b16 v[114:115], v66 offset:35584\n"
    "v_mfma_f32_16x16x32_bf16 v[84:87], v[116:119], v[92:95], v[84:87]\n"
    "s_waitcnt lgkmcnt(8)\n"
    "v_mfma_f32_16x16x32_bf16 v[160:163], v[96:99], v[120:123], v[160:163]\n"
    "ds_read_b64_tr_b16 v[116:117], v67 offset:35328\n"
    "v_mfma_f32_16x16x32_bf16 v[164:167], v[100:103], v[120:123], v[164:167]\n"
    "ds_read_b64_tr_b16 v[118:119], v67 offset:35584\n"
    "v_mfma_f32_16x16x32_bf16 v[216:219], v[92:95], v[96:99], v[216:219]\n"
    "s_waitcnt lgkmcnt(8)\n"
    "v_mfma_f32_16x16x32_bf16 v[168:171], v[96:99], v[124:127], v[168:171]\n"
    "ds_read_b64_tr_b16 v[120:121], v68 offset:35328\n"
    "v_mfma_f32_16x16x32_bf16 v[172:175], v[100:103], v[124:127], v[172:175]\n"
    "ds_read_b64_tr_b16 v[122:123], v68 offset:35584\n"
    "v_mfma_f32_16x16x32_bf16 v[220:223], v[92:95], v[100:103], v[220:223]\n"
    "ds_read_b128 v[104:107], v70 offset:35328\n"
    "s_waitcnt lgkmcnt(9)\n"
    "v_mfma_f32_16x16x32_bf16 v[176:179], v[96:99], v[128:131], v[176:179]\n"
    "ds_read_b128 v[108:111], v70 offset:37376\n"
    "v_mfma_f32_16x16x32_bf16 v[180:183], v[100:103], v[128:131], v[180:183]\n"
    "ds_read_b64_tr_b16 v[124:125], v60 offset:44800\n"
    "v_mfma_f32_16x16x32_bf16 v[224:227], v[96:99], v[96:99], v[224:227]\n"
    "ds_read_b64_tr_b16 v[126:127], v60 offset:45056\n"
    "s_waitcnt lgkmcnt(10)\n"
    "v_mfma_f32_16x16x32_bf16 v[184:187], v[96:99], v[132:135], v[184:187]\n"
    "ds_read_b64_tr_b16 v[128:129], v61 offset:44800\n"
    "v_mfma_f32_16x16x32_bf16 v[188:191], v[100:103], v[132:135], v[188:191]\n"
    "ds_read_b64_tr_b16 v[130:131], v61 offset:45056\n"
    "v_mfma_f32_16x16x32_bf16 v[228:231], v[100:103], v[100:103], v[228:231]\n"
    "s_waitcnt lgkmcnt(10)\n"
    "v_mfma_f32_16x16x32_bf16 v[192:195], v[96:99], v[112:115], v[192:195]\n"
    "ds_read_b64_tr_b16 v[132:133], v62 offset:44800\n"
    "v_mfma_f32_16x16x32_bf16 v[196:199], v[100:103], v[112:115], v[196:199]\n"
    "ds_read_b64_tr_b16 v[134:135], v62 offset:45056\n"
    "s_waitcnt lgkmcnt(10)\n"
    "v_mfma_f32_16x16x32_bf16 v[200:203], v[96:99], v[116:119], v[200:203]\n"
    "ds_read_b64_tr_b16 v[112:113], v63 offset:44800\n"
    "v_mfma_f32_16x16x32_bf16 v[204:207], v[100:103], v[116:119], v[204:207]\n"
    "ds_read_b64_tr_b16 v[114:115], v63 offset:45056\n"
    "s_waitcnt lgkmcnt(10)\n"
    "v_mfma_f32_16x16x32_bf16 v[208:211], v[96:99], v[120:123], v[208:211]\n"
    "ds_read_b64_tr_b16 v[116:117], v64 offset:44800\n"
    "v_mfma_f32_16x16x32_bf16 v[212:215], v[100:103], v[120:123], v[212:215]\n"
    "ds_read_b64_tr_b16 v[118:119], v64 offset:45056\n"
    "v_mfma_f32_16x16x32_bf16 v[88:91], v[120:123], v[92:95], v[88:91]\n"
    "s_waitcnt vmcnt(0)\n"
    "s_barrier\n"
    "s_cmp_ge_u32 %6, 4\n"
    "s_cbranch_scc1 .Llp1_%=\n"
    "global_load_dword v244, v245, %19\n"
    "s_nop 2\n"
    ".Llp1_%=:\n"
    "s_waitcnt lgkmcnt(8)\n"
    "v_mfma_f32_16x16x32_bf16 v[144:147], v[104:107], v[124:127], v[144:147]\n"
    "ds_read_b64_tr_b16 v[120:121], v65 offset:44800\n"
    "v_mfma_f32_16x16x32_bf16 v[148:151], v[108:111], v[124:127], v[148:151]\n"
    "ds_read_b64_tr_b16 v[122:123], v65 offset:45056\n"
    "v_mfma_f32_16x16x32_bf16 v[80:83], v[124:127], v[92:95], v[80:83]\n"
    "s_waitcnt lgkmcnt(8)\n"
    "v_mfma_f32_16x16x32_bf16 v[152:155], v[104:107], v[128:131], v[152:155]\n"
    "ds_read_b64_tr_b16 v[124:125], v66 offset:44800\n"
    "v_mfma_f32_16x16x32_bf16 v[156:159], v[108:111], v[128:131], v[156:159]\n"
    "ds_read_b64_tr_b16 v[126:127], v66 offset:45056\n"
    "v_mfma_f32_16x16x32_bf16 v[84:87], v[128:131], v[92:95], v[84:87]\n"
    "s_waitcnt lgkmcnt(8)\n"
    "v_mfma_f32_16x16x32_bf16 v[160:163], v[104:107], v[132:135], v[160:163]\n"
    "ds_read_b64_tr_b16 v[128:129], v67 offset:44800\n"
    "v_mfma_f32_16x16x32_bf16 v[164:167], v[108:111], v[132:135], v[164:167]\n"
    "ds_read_b64_tr_b16 v[130:131], v67 offset:45056\n"
    "v_mfma_f32_16x16x32_bf16 v[216:219], v[92:95], v[104:107], v[216:219]\n"
    "s_waitcnt lgkmcnt(8)\n"
    "v_mfma_f32_16x16x32_bf16 v[168:171], v[104:107], v[112:115], v[168:171]\n"
    "ds_read_b64_tr_b16 v[132:133], v68 offset:44800\n"
    "v_mfma_f32_16x16x32_bf16 v[172:175], v[108:111], v[112:115], v[172:175]\n"
    "ds_read_b64_tr_b16 v[134:135], v68 offset:45056\n"
    "v_mfma_f32_16x16x32_bf16 v[220:223], v[92:95], v[108:111], v[220:223]\n"
    "ds_read_b128 v[96:99], v241 offset:0\n"
    "s_waitcnt lgkmcnt(9)\n"
    "v_mfma_f32_16x16x32_bf16 v[176:179], v[104:107], v[116:119], v[176:179]\n"
    "ds_read_b128 v[100:103], v241 offset:2048\n"
    "v_mfma_f32_16x16x32_bf16 v[180:183], v[108:111], v[116:119], v[180:183]\n"
    "ds_read_b64_tr_b16 v[112:113], v232 offset:0\n"
    "v_mfma_f32_16x16x32_bf16 v[224:227], v[104:107], v[104:107], v[224:227]\n"
    "ds_read_b64_tr_b16 v[114:115], v232 offset:256\n"
    "s_waitcnt lgkmcnt(10)\n"
    "v_mfma_f32_16x16x32_bf16 v[184:187], v[104:107], v[120:123], v[184:187]\n"
    "ds_read_b64_tr_b16 v[116:117], v233 offset:0\n"
    "v_mfma_f32_16x16x32_bf16 v[188:191], v[108:111], v[120:123], v[188:191]\n"
    "ds_read_b64_tr_b16 v[118:119], v233 offset:256\n"
    "v_mfma_f32_16x16x32_bf16 v[228:231], v[108:111], v[108:111], v[228:231]\n"
    "s_waitcnt lgkmcnt(10)\n"
    "v_mfma_f32_16x16x32_bf16 v[192:195], v[104:107], v[124:127], v[192:195]\n"
    "ds_read_b64_tr_b16 v[120:121], v234 offset:0\n"
    "v_mfma_f32_16x16x32_bf16 v[196:199], v[108:111], v[124:127], v[196:199]\n"
    "ds_read_b64_tr_b16 v[122:123], v234 offset:256\n"
    "s_waitcnt lgkmcnt(10)\n"
    "v_mfma_f32_16x16x32_bf16 v[200:203], v[104:107], v[128:131], v[200:203]\n"
    "ds_read_b64_tr_b16 v[124:125], v235 offset:0\n"
    "v_mfma_f32_16x16x32_bf16 v[204:207], v[108:111], v[128:131], v[204:207]\n"
    "ds_read_b64_tr_b16 v[126:127], v235 offset:256\n"
    "s_waitcnt lgkmcnt(10)\n"
    "v_mfma_f32_16x16x32_bf16 v[208:211], v[104:107], v[132:135], v[208:211]\n"
    "ds_read_b64_tr_b16 v[128:129], v236 offset:0\n"
    "v_mfma_f32_16x16x32_bf16 v[212:215], v[108:111], v[132:135], v[212:215]\n"
    "ds_read_b64_tr_b16 v[130:131], v236 offset:256\n"
    "v_mfma_f32_16x16x32_bf16 v[88:91], v[132:135], v[92:95], v[88:91]\n"
    "s_waitcnt lgkmcnt(8)\n"
    "v_mfma_f32_16x16x32_bf16 v[144:147], v[96:99], v[112:115], v[144:147]\n"
    "ds_read_b64_tr_b16 v[132:133], v237 offset:0\n"
    "v_mfma_f32_16x16x32_bf16 v[148:151], v[100:103], v[112:115], v[148:151]\n"
    "ds_read_b64_tr_b16 v[134:135], v237 offset:256\n"
    "v_mfma_f32_16x16x32_bf16 v[80:83], v[112:115], v[92:95], v[80:83]\n"
    "s_waitcnt lgkmcnt(8)\n"
    "v_mfma_f32_16x16x32_bf16 v[152:155], v[96:99], v[116:119], v[152:155]\n"
    "ds_read_b64_tr_b16 v[112:113], v238 offset:0\n"
    "v_mfma_f32_16x16x32_bf16 v[156:159], v[100:103], v[116:119], v[156:159]\n"
    "ds_read_b64_tr_b16 v[114:115], v238 offset:256\n"
    "v_mfma_f32_16x16x32_bf16 v[84:87], v[116:119], v[92:95], v[84:87]\n"
    "s_waitcnt lgkmcnt(8)\n"
    "v_mfma_f32_16x16x32_bf16 v[160:163], v[96:99], v[120:123], v[160:163]\n"
    "ds_read_b64_tr_b16 v[116:117], v239 offset:0\n"
    "v_mfma_f32_16x16x32_bf16 v[164:167], v[100:103], v[120:123], v[164:167]\n"
    "ds_read_b64_tr_b16 v[118:119], v239 offset:256\n"
    "v_mfma_f32_16x16x32_bf16 v[216:219], v[92:95], v[96:99], v[216:219]\n"
    "s_waitcnt lgkmcnt(8)\n"
    "v_mfma_f32_16x16x32_bf16 v[168:171], v[96:99], v[124:127], v[168:171]\n"
    "ds_read_b64_tr_b16 v[120:121], v240 offset:0\n"
    "v_mfma_f32_16x16x32_bf16 v[172:175], v[100:103], v[124:127], v[172:175]\n"
    "ds_read_b64_tr_b16 v[122:123], v240 offset:256\n"
    "v_mfma_f32_16x16x32_bf16 v[220:223], v[92:95], v[100:103], v[220:223]\n"
    "ds_read_b128 v[104:107], v242 offset:0\n"
    "s_waitcnt lgkmcnt(9)\n"
    "v_mfma_f32_16x16x32_bf16 v[176:179], v[96:99], v[128:131], v[176:179]\n"
    "ds_read_b128 v[108:111], v242 offset:2048\n"
    "v_mfma_f32_16x16x32_bf16 v[180:183], v[100:103], v[128:131], v[180:183]\n"
    "ds_read_b64_tr_b16 v[124:125], v232 offset:9472\n"
    "v_mfma_f32_16x16x32_bf16 v[224:227], v[96:99], v[96:99], v[224:227]\n"
    "ds_read_b64_tr_b16 v[126:127], v232 offset:9728\n"
    "s_waitcnt lgkmcnt(10)\n"
    "v_mfma_f32_16x16x32_bf16 v[184:187], v[96:99], v[132:135], v[184:187]\n"
    "ds_read_b64_tr_b16 v[128:129], v233 offset:9472\n"
    "v_mfma_f32_16x16x32_bf16 v[188:191], v[100:103], v[132:135], v[188:191]\n"
    "ds_read_b64_tr_b16 v[130:131], v233 offset:9728\n"
    "v_mfma_f32_16x16x32_bf16 v[228:231], v[100:103], v[100:103], v[228:231]\n"
    "s_waitcnt lgkmcnt(10)\n"
    "v_mfma_f32_16x16x32_bf16 v[192:195], v[96:99], v[112:115], v[192:195]\n"
    "ds_read_b64_tr_b16 v[132:133], v234 offset:9472\n"
    "v_mfma_f32_16x16x32_bf16 v[196:199], v[100:103], v[112:115], v[196:199]\n"
    "ds_read_b64_tr_b16 v[134:135], v234 offset:9728\n"
    "s_waitcnt lgkmcnt(10)\n"
    "v_mfma_f32_16x16x32_bf16 v[200:203], v[96:99], v[116:119], v[200:203]\n"
    "ds_read_b64_tr_b16 v[112:113], v235 offset:9472\n"
    "v_mfma_f32_16x16x32_bf16 v[204:207], v[100:103], v[116:119], v[204:207]\n"
    "ds_read_b64_tr_b16 v[114:115], v235 offset:9728\n"
    "s_waitcnt lgkmcnt(10)\n"
    "v_mfma_f32_16x16x32_bf16 v[208:211], v[96:99], v[120:123], v[208:211]\n"
    "ds_read_b64_tr_b16 v[116:117], v236 offset:9472\n"
    "v_mfma_f32_16x16x32_bf16 v[212:215], v[100:103], v[120:123], v[212:215]\n"
    "ds_read_b64_tr_b16 v[118:119], v236 offset:9728\n"
    "v_mfma_f32_16x16x32_bf16 v[88:91], v[120:123], v[92:95], v[88:91]\n"
    "s_waitcnt lgkmcnt(8)\n"
    "v_mfma_f32_16x16x32_bf16 v[144:147], v[104:107], v[124:127], v[144:147]\n"
    "ds_read_b64_tr_b16 v[120:121], v237 offset:9472\n"
    "v_mfma_f32_16x16x32_bf16 v[148:151], v[108:111], v[124:127], v[148:151]\n"
    "ds_read_b64_tr_b16 v[122:123], v237 offset:9728\n"
    "v_mfma_f32_16x16x32_bf16 v[80:83], v[124:127], v[92:95], v[80:83]\n"
    "s_waitcnt lgkmcnt(8)\n"
    "v_mfma_f32_16x16x32_bf16 v[152:155], v[104:107], v[128:131], v[152:155]\n"
    "ds_read_b64_tr_b16 v[124:125], v238 offset:9472\n"
    "v_mfma_f32_16x16x32_bf16 v[156:159], v[108:111], v[128:131], v[156:159]\n"
    "ds_read_b64_tr_b16 v[126:127], v238 offset:9728\n"
    "v_mfma_f32_16x16x32_bf16 v[84:87], v[128:131], v[92:95], v[84:87]\n"
    "s_waitcnt lgkmcnt(8)\n"
    "v_mfma_f32_16x16x32_bf16 v[160:163], v[104:107], v[132:135], v[160:163]\n"
    "ds_read_b64_tr_b16 v[128:129], v239 offset:9472\n"
    "v_mfma_f32_16x16x32_bf16 v[164:167], v[108:111], v[132:135], v[164:167]\n"
    "ds_read_b64_tr_b16 v[130:131], v239 offset:9728\n"
    "v_mfma_f32_16x16x32_bf16 v[216:219], v[92:95], v[104:107], v[216:219]\n"
    "s_waitcnt lgkmcnt(8)\n"
    "v_mfma_f32_16x16x32_bf16 v[168:171], v[104:107], v[112:115], v[168:171]\n"
    "ds_read_b64_tr_b16 v[132:133], v240 offset:9472\n"
    "v_mfma_f32_16x16x32_bf16 v[172:175], v[108:111], v[112:115], v[172:175]\n"
    "ds_read_b64_tr_b16 v[134:135], v240 offset:9728\n"
    "v_mfma_f32_16x16x32_bf16 v[220:223], v[92:95], v[108:111], v[220:223]\n"
    "s_waitcnt lgkmcnt(8)\n"
    "v_mfma_f32_16x16x32_bf16 v[176:179], v[104:107], v[116:119], v[176:179]\n"
    "v_mfma_f32_16x16x32_bf16 v[180:183], v[108:111], v[116:119], v[180:183]\n"
    "v_mfma_f32_16x16x32_bf16 v[224:227], v[104:107], v[104:107], v[224:227]\n"
    "s_waitcnt lgkmcnt(6)\n"
    "v_mfma_f32_16x16x32_bf16 v[184:187], v[104:107], v[120:123], v[184:187]\n"
    "v_mfma_f32_16x16x32_bf16 v[188:191], v[108:111], v[120:123], v[188:191]\n"
    "v_mfma_f32_16x16x32_bf16 v[228:231], v[108:111], v[108:111], v[228:231]\n"
    "s_waitcnt lgkmcnt(4)\n"
    "v_mfma_f32_16x16x32_bf16 v[192:195], v[104:107], v[124:127], v[192:195]\n"
    "v_mfma_f32_16x16x32_bf16 v[196:199], v[108:111], v[124:127], v[196:199]\n"
    "s_waitcnt lgkmcnt(2)\n"
    "v_mfma_f32_16x16x32_bf16 v[200:203], v[104:107], v[128:131], v[200:203]\n"
    "v_mfma_f32_16x16x32_bf16 v[204:207], v[108:111], v[128:131], v[204:207]\n"
    "s_waitcnt lgkmcnt(0)\n"
    "v_mfma_f32_16x16x32_bf16 v[208:211], v[104:107], v[132:135], v[208:211]\n"
    "v_mfma_f32_16x16x32_bf16 v[212:215], v[108:111], v[132:135], v[212:215]\n"
    "v_mfma_f32_16x16x32_bf16 v[88:91], v[132:135], v[92:95], v[88:91]\n"
    "s_mov_b32 exec_lo, 0x10001\n"
    "s_mov_b32 exec_hi, 0x10001\n"
    "ds_write_b128 v8, v[80:83]\n"
    "ds_write_b128 v8, v[84:87] offset:64\n"
    "s_nop 3\n"
    "ds_write_b128 v9, v[88:91] offset:512\n"
    "s_mov_b64 exec, -1\n"
    "s_mov_b32 vcc_lo, 0x22222222\n"
    "s_mov_b32 vcc_hi, 0x22222222\n"
    "v_cndmask_b32 v224, v224, v225, vcc\n"
    "v_cndmask_b32 v228, v228, v229, vcc\n"
    "s_mov_b32 vcc_lo, 0x44444444\n"
    "s_mov_b32 vcc_hi, 0x44444444\n"
    "v_cndmask_b32 v224, v224, v226, vcc\n"
    "v_cndmask_b32 v228, v228, v230, vcc\n"
    "s_mov_b32 vcc_lo, 0x88888888\n"
    "s_mov_b32 vcc_hi, 0x88888888\n"
    "v_cndmask_b32 v224, v224, v227, vcc\n"
    "v_cndmask_b32 v228, v228, v231, vcc\n"
    "s_waitcnt lgkmcnt(0)\n"
    "s_barrier\n"
    "ds_read_b32 v16, v76 offset:0\n"
    "ds_read_b32 v18, v76 offset:64\n"
    "ds_read_b32 v20, v77 offset:0\n"
    "ds_read_b32 v22, v77 offset:64\n"
    "ds_read_b32 v24, v78 offset:0\n"
    "ds_read_b32 v26, v78 offset:64\n"
    "ds_read_b32 v28, v79 offset:0\n"
    "ds_read_b32 v30, v79 offset:64\n"
    "ds_read_b32 v32, v10 offset:512\n"
    "v_mul_f32 v248, 0xbae38e39, v216\n"
    "v_mul_f32 v252, 0x3ae38e39, v224\n"
    "v_mul_f32 v250, 0xbae38e39, v220\n"
    "v_mul_f32 v140, 0x3ae38e39, v228\n"
    "v_fma_f32 v252, -v248, v248, v252\n"
    "v_fma_f32 v140, -v250, v250, v140\n"
    "v_max_f32 v252, 0, v252\n"
    "v_max_f32 v140, 0, v140\n"
    "v_cvt_pk_bf16_f32 v252, v252, v252\n"
    "v_cvt_pk_bf16_f32 v140, v140, v140\n"
    "v_lshlrev_b32 v252, 16, v252\n"
    "v_lshlrev_b32 v140, 16, v140\n"
    "v_add_f32 v252, 0x3727c5ac, v252\n"
    "v_add_f32 v140, 0x3727c5ac, v140\n"
    "v_cvt_pk_bf16_f32 v252, v252, v252\n"
    "v_cvt_pk_bf16_f32 v140, v140, v140\n"
    "v_lshlrev_b32 v252, 16, v252\n"
    "v_lshlrev_b32 v140, 16, v140\n"
    "v_sqrt_f32 v252, v252\n"
    "v_sqrt_f32 v140, v140\n"
    "v_cvt_pk_bf16_f32 v252, v252, v252\n"
    "v_cvt_pk_bf16_f32 v140, v140, v140\n"
    "v_lshlrev_b32 v252, 16, v252\n"
    "v_lshlrev_b32 v140, 16, v140\n"
    "v_rcp_f32 v252, v252\n"
    "v_rcp_f32 v140, v140\n"
    "s_mov_b32 exec_lo, 0x00f0000f\n"
    "s_mov_b32 exec_hi, 0xf0000f00\n"
    "ds_write_b32 v11, v248 offset:0\n"
    "ds_write_b32 v11, v252 offset:64\n"
    "ds_write_b32 v11, v250 offset:128\n"
    "ds_write_b32 v11, v140 offset:192\n"
    "s_mov_b64 exec, -1\n"
    "ds_read_b128 v[56:59], v12 offset:0\n"
    "ds_read_b128 v[60:63], v12 offset:64\n"
    "ds_read_b128 v[64:67], v12 offset:128\n"
    "ds_read_b128 v[68:71], v12 offset:192\n"
    "s_waitcnt lgkmcnt(0)\n"
    "v_pk_fma_f32 v[144:145], v[16:17], v[56:57], v[144:145] op_sel_hi:[0,1,1]\n"
    "v_pk_fma_f32 v[146:147], v[16:17], v[58:59], v[146:147] op_sel_hi:[0,1,1]\n"
    "v_pk_fma_f32 v[148:149], v[16:17], v[64:65], v[148:149] op_sel_hi:[0,1,1]\n"
    "v_pk_fma_f32 v[150:151], v[16:17], v[66:67], v[150:151] op_sel_hi:[0,1,1]\n"
    "v_pk_mul_f32 v[144:145], v[60:61], v[144:145]\n"
    "v_pk_mul_f32 v[146:147], v[62:63], v[146:147]\n"
    "v_pk_mul_f32 v[148:149], v[68:69], v[148:149]\n"
    "v_pk_mul_f32 v[150:151], v[70:71], v[150:151]\n"
    "v_cvt_pk_bf16_f32 v144, v144, v145\n"
    "v_cvt_pk_bf16_f32 v145, v146, v147\n"
    "buffer_store_dwordx2 v[144:145], v13, s[36:39], s72 offen offset:0\n"
    "v_cvt_pk_bf16_f32 v148, v148, v149\n"
    "v_cvt_pk_bf16_f32 v149, v150, v151\n"
    "buffer_store_dwordx2 v[148:149], v13, s[36:39], s72 offen offset:608\n"
    "v_pk_fma_f32 v[152:153], v[18:19], v[56:57], v[152:153] op_sel_hi:[0,1,1]\n"
    "v_pk_fma_f32 v[154:155], v[18:19], v[58:59], v[154:155] op_sel_hi:[0,1,1]\n"
    "v_pk_fma_f32 v[156:157], v[18:19], v[64:65], v[156:157] op_sel_hi:[0,1,1]\n"
    "v_pk_fma_f32 v[158:159], v[18:19], v[66:67], v[158:159] op_sel_hi:[0,1,1]\n"
    "v_pk_mul_f32 v[152:153], v[60:61], v[152:153]\n"
    "v_pk_mul_f32 v[154:155], v[62:63], v[154:155]\n"
    "v_pk_mul_f32 v[156:157], v[68:69], v[156:157]\n"
    "v_pk_mul_f32 v[158:159], v[70:71], v[158:159]\n"
    "v_cvt_pk_bf16_f32 v152, v152, v153\n"
    "v_cvt_pk_bf16_f32 v153, v154, v155\n"
    "buffer_store_dwordx2 v[152:153], v13, s[36:39], s73 offen offset:0\n"
    "v_cvt_pk_bf16_f32 v156, v156, v157\n"
    "v_cvt_pk_bf16_f32 v157, v158, v159\n"
    "buffer_store_dwordx2 v[156:157], v13, s[36:39], s73 offen offset:608\n"
    "v_pk_fma_f32 v[160:161], v[20:21], v[56:57], v[160:161] op_sel_hi:[0,1,1]\n"
    "v_pk_fma_f32 v[162:163], v[20:21], v[58:59], v[162:163] op_sel_hi:[0,1,1]\n"
    "v_pk_fma_f32 v[164:165], v[20:21], v[64:65], v[164:165] op_sel_hi:[0,1,1]\n"
    "v_pk_fma_f32 v[166:167], v[20:21], v[66:67], v[166:167] op_sel_hi:[0,1,1]\n"
    "v_pk_mul_f32 v[160:161], v[60:61], v[160:161]\n"
    "v_pk_mul_f32 v[162:163], v[62:63], v[162:163]\n"
    "v_pk_mul_f32 v[164:165], v[68:69], v[164:165]\n"
    "v_pk_mul_f32 v[166:167], v[70:71], v[166:167]\n"
    "v_cvt_pk_bf16_f32 v160, v160, v161\n"
    "v_cvt_pk_bf16_f32 v161, v162, v163\n"
    "buffer_store_dwordx2 v[160:161], v13, s[36:39], s74 offen offset:0\n"
    "v_cvt_pk_bf16_f32 v164, v164, v165\n"
    "v_cvt_pk_bf16_f32 v165, v166, v167\n"
    "buffer_store_dwordx2 v[164:165], v13, s[36:39], s74 offen offset:608\n"
    "v_pk_fma_f32 v[168:169], v[22:23], v[56:57], v[168:169] op_sel_hi:[0,1,1]\n"
    "v_pk_fma_f32 v[170:171], v[22:23], v[58:59], v[170:171] op_sel_hi:[0,1,1]\n"
    "v_pk_fma_f32 v[172:173], v[22:23], v[64:65], v[172:173] op_sel_hi:[0,1,1]\n"
    "v_pk_fma_f32 v[174:175], v[22:23], v[66:67], v[174:175] op_sel_hi:[0,1,1]\n"
    "v_pk_mul_f32 v[168:169], v[60:61], v[168:169]\n"
    "v_pk_mul_f32 v[170:171], v[62:63], v[170:171]\n"
    "v_pk_mul_f32 v[172:173], v[68:69], v[172:173]\n"
    "v_pk_mul_f32 v[174:175], v[70:71], v[174:175]\n"
    "v_cvt_pk_bf16_f32 v168, v168, v169\n"
    "v_cvt_pk_bf16_f32 v169, v170, v171\n"
    "buffer_store_dwordx2 v[168:169], v13, s[36:39], s75 offen offset:0\n"
    "v_cvt_pk_bf16_f32 v172, v172, v173\n"
    "v_cvt_pk_bf16_f32 v173, v174, v175\n"
    "buffer_store_dwordx2 v[172:173], v13, s[36:39], s75 offen offset:608\n"
    "v_pk_fma_f32 v[176:177], v[24:25], v[56:57], v[176:177] op_sel_hi:[0,1,1]\n"
    "v_pk_fma_f32 v[178:179], v[24:25], v[58:59], v[178:179] op_sel_hi:[0,1,1]\n"
    "v_pk_fma_f32 v[180:181], v[24:25], v[64:65], v[180:181] op_sel_hi:[0,1,1]\n"
    "v_pk_fma_f32 v[182:183], v[24:25], v[66:67], v[182:183] op_sel_hi:[0,1,1]\n"
    "v_pk_mul_f32 v[176:177], v[60:61], v[176:177]\n"
    "v_pk_mul_f32 v[178:179], v[62:63], v[178:179]\n"
    "v_pk_mul_f32 v[180:181], v[68:69], v[180:181]\n"
    "v_pk_mul_f32 v[182:183], v[70:71], v[182:183]\n"
    "v_cvt_pk_bf16_f32 v176, v176, v177\n"
    "v_cvt_pk_bf16_f32 v177, v178, v179\n"
    "buffer_store_dwordx2 v[176:177], v13, s[36:39], s76 offen offset:0\n"
    "v_cvt_pk_bf16_f32 v180, v180, v181\n"
    "v_cvt_pk_bf16_f32 v181, v182, v183\n"
    "buffer_store_dwordx2 v[180:181], v13, s[36:39], s76 offen offset:608\n"
    "v_pk_fma_f32 v[184:185], v[26:27], v[56:57], v[184:185] op_sel_hi:[0,1,1]\n"
    "v_pk_fma_f32 v[186:187], v[26:27], v[58:59], v[186:187] op_sel_hi:[0,1,1]\n"
    "v_pk_fma_f32 v[188:189], v[26:27], v[64:65], v[188:189] op_sel_hi:[0,1,1]\n"
    "v_pk_fma_f32 v[190:191], v[26:27], v[66:67], v[190:191] op_sel_hi:[0,1,1]\n"
    "v_pk_mul_f32 v[184:185], v[60:61], v[184:185]\n"
    "v_pk_mul_f32 v[186:187], v[62:63], v[186:187]\n"
    "v_pk_mul_f32 v[188:189], v[68:69], v[188:189]\n"
    "v_pk_mul_f32 v[190:191], v[70:71], v[190:191]\n"
    "v_cvt_pk_bf16_f32 v184, v184, v185\n"
    "v_cvt_pk_bf16_f32 v185, v186, v187\n"
    "buffer_store_dwordx2 v[184:185], v13, s[36:39], s77 offen offset:0\n"
    "v_cvt_pk_bf16_f32 v188, v188, v189\n"
    "v_cvt_pk_bf16_f32 v189, v190, v191\n"
    "buffer_store_dwordx2 v[188:189], v13, s[36:39], s77 offen offset:608\n"
    "v_pk_fma_f32 v[192:193], v[28:29], v[56:57], v[192:193] op_sel_hi:[0,1,1]\n"
    "v_pk_fma_f32 v[194:195], v[28:29], v[58:59], v[194:195] op_sel_hi:[0,1,1]\n"
    "v_pk_fma_f32 v[196:197], v[28:29], v[64:65], v[196:197] op_sel_hi:[0,1,1]\n"
    "v_pk_fma_f32 v[198:199], v[28:29], v[66:67], v[198:199] op_sel_hi:[0,1,1]\n"
    "v_pk_mul_f32 v[192:193], v[60:61], v[192:193]\n"
    "v_pk_mul_f32 v[194:195], v[62:63], v[194:195]\n"
    "v_pk_mul_f32 v[196:197], v[68:69], v[196:197]\n"
    "v_pk_mul_f32 v[198:199], v[70:71], v[198:199]\n"
    "v_cvt_pk_bf16_f32 v192, v192, v193\n"
    "v_cvt_pk_bf16_f32 v193, v194, v195\n"
    "buffer_store_dwordx2 v[192:193], v13, s[36:39], s78 offen offset:0\n"
    "v_cvt_pk_bf16_f32 v196, v196, v197\n"
    "v_cvt_pk_bf16_f32 v197, v198, v199\n"
    "buffer_store_dwordx2 v[196:197], v13, s[36:39], s78 offen offset:608\n"
    "v_pk_fma_f32 v[200:201], v[30:31], v[56:57], v[200:201] op_sel_hi:[0,1,1]\n"
    "v_pk_fma_f32 v[202:203], v[30:31], v[58:59], v[202:203] op_sel_hi:[0,1,1]\n"
    "v_pk_fma_f32 v[204:205], v[30:31], v[64:65], v[204:205] op_sel_hi:[0,1,1]\n"
    "v_pk_fma_f32 v[206:207], v[30:31], v[66:67], v[206:207] op_sel_hi:[0,1,1]\n"
    "v_pk_mul_f32 v[200:201], v[60:61], v[200:201]\n"
    "v_pk_mul_f32 v[202:203], v[62:63], v[202:203]\n"
    "v_pk_mul_f32 v[204:205], v[68:69], v[204:205]\n"
    "v_pk_mul_f32 v[206:207], v[70:71], v[206:207]\n"
    "v_cvt_pk_bf16_f32 v200, v200, v201\n"
    "v_cvt_pk_bf16_f32 v201, v202, v203\n"
    "buffer_store_dwordx2 v[200:201], v13, s[36:39], s79 offen offset:0\n"
    "v_cvt_pk_bf16_f32 v204, v204, v205\n"
    "v_cvt_pk_bf16_f32 v205, v206, v207\n"
    "buffer_store_dwordx2 v[204:205], v13, s[36:39], s79 offen offset:608\n"
    "v_pk_fma_f32 v[208:209], v[32:33], v[56:57], v[208:209] op_sel_hi:[0,1,1]\n"
    "v_pk_fma_f32 v[210:211], v[32:33], v[58:59], v[210:211] op_sel_hi:[0,1,1]\n"
    "v_pk_fma_f32 v[212:213], v[32:33], v[64:65], v[212:213] op_sel_hi:[0,1,1]\n"
    "v_pk_fma_f32 v[214:215], v[32:33], v[66:67], v[214:215] op_sel_hi:[0,1,1]\n"
    "v_pk_mul_f32 v[208:209], v[60:61], v[208:209]\n"
    "v_pk_mul_f32 v[210:211], v[62:63], v[210:211]\n"
    "v_pk_mul_f32 v[212:213], v[68:69], v[212:213]\n"
    "v_pk_mul_f32 v[214:215], v[70:71], v[214:215]\n"
    "v_cvt_pk_bf16_f32 v208, v208, v209\n"
    "v_cvt_pk_bf16_f32 v209, v210, v211\n"
    "buffer_store_dwordx2 v[208:209], v14, s[36:39], %11 offen offset:256\n"
    "v_cvt_pk_bf16_f32 v212, v212, v213\n"
    "v_cvt_pk_bf16_f32 v213, v214, v215\n"
    "buffer_store_dwordx2 v[212:213], v14, s[36:39], %11 offen offset:864\n"
    ".Ldone_%=:\n"
        :: "s"((u32)(size_t)ws), "s"((u32)((size_t)ws >> 32)), "s"((u32)(size_t)x), "s"((u32)((size_t)x >> 32)), "s"((u32)(size_t)wqkv), "s"((u32)((size_t)wqkv >> 32)),
           "s"(w), "s"(row0 * 1152), "s"((576 * which + 144 * hg) * 2 + (8 * (w >> 1) + 2 * (w & 1)) * 3456),
           "s"((576 * which + 144 * hg) * 2 + 256 + (32 * (w >> 1) + 16 * (w & 1)) * 3456), "s"(which >> 1), "s"(omain), "s"(oleft), "s"(77824u), "s"(scale),
           "s"((u32)OFF_MEAN1 + row0 * 4), "s"(nt), "s"(0u),
           "s"(pfx), "s"(rfl64((u64)(w & 1 ? wu : wg) + (idx + 24 * (w >> 1)) * 73728 + (xcd >> 1) * 2304 + (xcd & 1) * 1024)), "s"(0u), "s"(pfw)
        : "v0", "v1", "v2", "v3", "v4", "v5", "v6", "v7", "v8", "v9", "v10", "v11", "v12", "v13", "v14", "v15", "v16", "v17", "v18", "v19", "v20", "v21", "v22", "v23", "v24", "v25", "v26", "v27", "v28", "v29", "v30", "v31", "v32", "v33", "v34", "v35", "v36", "v37", "v38", "v39", "v40", "v41", "v42", "v43", "v44", "v45", "v46", "v47", "v48", "v49", "v50", "v51", "v52", "v53", "v54", "v55", "v56", "v57", "v58", "v59", "v60", "v61", "v62", "v63", "v64", "v65", "v66", "v67", "v68", "v69", "v70", "v71", "v72", "v73", "v74", "v75", "v76", "v77", "v78", "v79", "v80", "v81", "v82", "v83", "v84", "v85", "v86", "v87", "v88", "v89", "v90", "v91", "v92", "v93", "v94", "v95", "v96", "v97", "v98", "v99", "v100", "v101", "v102", "v103", "v104", "v105", "v106", "v107", "v108", "v109", "v110", "v111", "v112", "v113", "v114", "v115", "v116", "v117", "v118", "v119", "v120", "v121", "v122", "v123", "v124", "v125", "v126", "v127", "v128", "v129", "v130", "v131", "v132", "v133", "v134", "v135", "v136", "v137", "v138", "v139", "v140", "v141", "v142", "v143", "v144", "v145", "v146", "v147", "v148", "v149", "v150", "v151", "v152", "v153", "v154", "v155", "v156", "v157", "v158", "v159", "v160", "v161", "v162", "v163", "v164", "v165", "v166", "v167", "v168", "v169", "v170", "v171", "v172", "v173", "v174", "v175", "v176", "v177", "v178", "v179", "v180", "v181", "v182", "v183", "v184", "v185", "v186", "v187", "v188", "v189", "v190", "v191", "v192", "v193", "v194", "v195", "v196", "v197", "v198", "v199", "v200", "v201", "v202", "v203", "v204", "v205", "v206", "v207", "v208", "v209", "v210", "v211", "v212", "v213", "v214", "v215", "v216", "v217", "v218", "v219", "v220", "v221", "v222", "v223", "v224", "v225", "v226", "v227", "v228", "v229", "v230", "v231", "v232", "v233", "v234", "v235", "v236", "v237", "v238", "v239", "v240", "v241", "v242", "v243", "v244", "v245", "v246", "v247", "v248", "v249", "v250", "v251", "v252", "v253", "v254", "v255", "s36", "s37", "s38", "s39", "s40", "s41", "s42", "s43", "s44", "s45", "s46", "s47", "s48", "s49", "s50", "s51", "s52", "s53", "s54", "s55", "s56", "s57", "s58", "s59", "s60", "s61", "s62", "s63", "s64", "s65", "s66", "s67", "s68", "s69", "s70", "s71", "s72", "s73", "s74", "s75", "s76", "s77", "s78", "s79", "s80", "s81", "s82", "s83", "s84", "s85", "s86", "s87", "s88", "s89", "s90", "s91", "s92", "s93", "s94", "s95", "s96", "s97", "s98", "s99", "vcc", "scc", "memory");
    } else {
        const u32 lane = threadIdx.x & 63;
        const u32 wid = (b - 192) * 4 + w;
        const auto rw = __builtin_amdgcn_make_buffer_rsrc(ws, 0, 0x8000000, 0x00020000);
        const u32 wg_ = b - 192, nj = (w == 0 && wg_ < 24) ? 4 : 3;
        const bool dowb = w == 3 || (w == 2 && wg_ >= 56);
        const u32 jw = w == 3 ? wg_ : wg_ + 8;
        typedef const __attribute__((address_space(1))) void* gptr;
        if (w < 2) {
            const u32 i = lane < 62 ? lane : 62;
            const char* pp = w ? (const char*)wqkv + pfw + (i / 9) * 6912 + ((i % 9) / 3) * 1152 + (i % 3) * 128 : (const char*)x + pfx + 128 + (lane >> 3) * 1152 + (lane & 7) * 128;
            __builtin_amdgcn_global_load_lds((gptr)pp, LDSP(void, 151552 + w * 256), 4, 0, 0);
        } else if (dowb) {
            const char* pp = (const char*)wo + (8 * jw + (lane >> 3)) * 1152 + (lane & 7) * 16;
#pragma unroll
            for (int i = 0; i < 9; ++i) __builtin_amdgcn_global_load_lds((gptr)(pp + 128 * i), LDSP(void, w * 37888 + 28416 + i * 1024), 16, 0, 0);
        }
#pragma unroll 1
        for (u32 i = 0; i < nj; ++i) job_load(k1_job(i < 3 ? wid + 256 * i : 768 + wg_, wo, wg, wu, wd), w * 37888 + i * 9472, lane);
#pragma unroll 1
        for (u32 i = 0; i < nj; ++i) {
            if (nj == 4) asm volatile("s_waitcnt vmcnt(27)" ::: "memory");
            else asm volatile("s_waitcnt vmcnt(18)" ::: "memory");
            job_store(k1_job(i < 3 ? wid + 256 * i : 768 + wg_, wo, wg, wu, wd), rw, w * 37888 + i * 9472, lane);
        }
#pragma unroll 1
        for (u32 i = 0; i < 9; ++i) __builtin_amdgcn_raw_buffer_store_b128((u32x4){~0u, ~0u, ~0u, ~0u}, rw, lane * 16, (u32)OFF_O32 + (wid * 9 + i) * 1024, 16);
        if (w == 1 && wg_ < 40) __builtin_amdgcn_raw_buffer_store_b128((u32x4){0u, 0u, 0u, 0u}, rw, lane * 16, (u32)OFF_FLAG + wg_ * 1024, 16);
        if (dowb) wbar_job(jw, w * 37888 + 28416, ws, lane);
    }
}

__global__ __launch_bounds__(768, 3) void k2_kernel(const u16* __restrict__ x, const u16* __restrict__ wg, const u16* __restrict__ wu, char* ws) {
    const u32 w = __builtin_amdgcn_readfirstlane(threadIdx.x >> 6);
    const u32 b = blockIdx.x, xcd = b & 7, idx = b >> 3, Tg = 8 * (xcd & 1) + (idx >> 2), hp = 4 * (xcd >> 1) + (idx & 3);
    const u32 qi = w & 3, Tp = 4 * Tg + (hp >> 2), nu = hp & 3;
    const u32 t0 = 4 * Tg - 8;
    if (w < 8) {
        const u32 hd = w >> 2, head = 2 * hp + hd, T = 4 * Tg + qi, blk = 4 + w;
        asm volatile(
    "att_code_begin:\n"
    "s_nop 4\n"
    "s_mov_b32 s36, %0\n"
    "s_and_b32 s37, %1, 0xffff\n"
    "s_mov_b32 s38, 134217728\n"
    "s_mov_b32 s39, 0x20000\n"
    "v_mbcnt_lo_u32_b32 v152, -1, 0\n"
    "v_mbcnt_hi_u32_b32 v152, -1, v152\n"
    "v_lshlrev_b32 v153, 4, v152\n"
    "v_and_b32 v159, 31, v152\n"
    "v_lshrrev_b32 v160, 5, v152\n"
    "v_lshlrev_b32 v161, 4, v160\n"
    "v_lshl_add_u32 v161, v159, 5, v161\n"
    "s_lshl_b32 s51, %4, 10\n"
    "s_lshr_b32 s48, %4, 2\n"
    "s_lshl_b32 s49, s48, 13\n"
    "s_add_u32 s49, s49, s51\n"
    "s_add_u32 m0, s49, 8192\n"
    "s_add_u32 s44, %5, 8192\n"
    "buffer_load_dwordx4 v161, s[36:39], s44 offen lds\n"
    "s_add_u32 m0, s49, 0\n"
    "s_add_u32 s45, %5, 0\n"
    "buffer_load_dwordx4 v161, s[36:39], s45 offen lds\n"
    "s_add_u32 m0, s49, 4096\n"
    "s_add_u32 s46, %5, 4096\n"
    "buffer_load_dwordx4 v161, s[36:39], s46 offen lds\n"
    "buffer_load_dwordx4 v[128:131], v161, s[36:39], %7 offen\n"
    "v_lshlrev_b32 v155, 3, v159\n"
    "buffer_load_dword v157, v155, s[36:39], %8 offen\n"
    "s_cmp_ge_u32 %4, 3\n"
    "s_cbranch_scc1 .Lkl_%=\n"
    "s_add_u32 m0, s51, 24576\n"
    "s_nop 0\n"
    "buffer_load_dwordx4 v153, s[36:39], %6 offen lds\n"
    ".Lkl_%=:\n"
    "v_lshlrev_b32 v165, 2, v159\n"
    "s_and_b32 s50, %4, 3\n"
    "v_add_u32 v154, s49, v153\n"
    "s_mov_b32 s64, 0xffffffff\n"
    "s_mov_b32 s65, 0xfffffff0\n"
    "s_mov_b32 s66, 0xfffffffe\n"
    "s_mov_b32 s67, 0xffffffe0\n"
    "s_mov_b32 s68, 0xfffffffc\n"
    "s_mov_b32 s69, 0xffffffc0\n"
    "s_mov_b32 s70, 0xfffffff8\n"
    "s_mov_b32 s71, 0xffffff80\n"
    "s_mov_b32 s72, 0xffffff00\n"
    "s_mov_b32 s73, 0xfffff000\n"
    "s_mov_b32 s74, 0xfffffe00\n"
    "s_mov_b32 s75, 0xffffe000\n"
    "s_mov_b32 s76, 0xfffffc00\n"
    "s_mov_b32 s77, 0xffffc000\n"
    "s_mov_b32 s78, 0xfffff800\n"
    "s_mov_b32 s79, 0xffff8000\n"
    "s_mov_b32 s80, 0xffff0000\n"
    "s_mov_b32 s81, 0xfff00000\n"
    "s_mov_b32 s82, 0xfffe0000\n"
    "s_mov_b32 s83, 0xffe00000\n"
    "s_mov_b32 s84, 0xfffc0000\n"
    "s_mov_b32 s85, 0xffc00000\n"
    "s_mov_b32 s86, 0xfff80000\n"
    "s_mov_b32 s87, 0xff800000\n"
    "s_mov_b32 s88, 0xff000000\n"
    "s_mov_b32 s89, 0xf0000000\n"
    "s_mov_b32 s90, 0xfe000000\n"
    "s_mov_b32 s91, 0xe0000000\n"
    "s_mov_b32 s92, 0xfc000000\n"
    "s_mov_b32 s93, 0xc0000000\n"
    "s_mov_b32 s94, 0xf8000000\n"
    "s_mov_b32 s95, 0x80000000\n"
    "s_lshl_b32 s49, s50, 8\n"
    "s_add_u32 s49, s49, 24576\n"
    "v_add_u32 v155, s49, v155\n"
    "v_min_u32 v156, 18, v159\n"
    "v_lshlrev_b32 v156, 4, v156\n"
    "v_mul_u32_u24 v161, 304, v160\n"
    "s_lshl_b32 s49, s48, 14\n"
    "s_mul_i32 s50, s50, 1216\n"
    "s_add_u32 s49, s49, s50\n"
    "s_add_u32 s49, s49, 28672\n"
    "v_add3_u32 v156, v156, v161, s49\n"
    "v_mov_b32 v106, 0\n"
    "v_mov_b32 v107, 0\n"
    "v_mov_b32 v110, 0\n"
    "v_mov_b32 v111, 0\n"
    "v_mov_b32 v132, 0\n"
    "v_mov_b32 v133, 0\n"
    "v_mov_b32 v134, 0\n"
    "v_mov_b32 v135, 0\n"
    "s_setprio 3\n"
    "s_waitcnt vmcnt(0)\n"
    "s_mov_b32 exec_hi, 0\n"
    "v_mov_b32 v134, 0x3f80\n"
    "s_cmp_lt_u32 %4, 4\n"
    "s_cselect_b64 vcc, -1, 0\n"
    "v_cndmask_b32 v132, 0, v157, vcc\n"
    "v_cndmask_b32 v133, v157, v135, vcc\n"
    "s_mov_b32 exec_hi, -1\n"
    "s_barrier\n"
    "ds_read_b128 v[96:99], v154 offset:8192\n"
    "ds_read_b64 v[104:105], v155 offset:2048\n"
    "ds_read_b128 v[100:103], v154 offset:0\n"
    "ds_read_b64 v[108:109], v155 offset:0\n"
    "s_cmp_lt_u32 0, %9\n"
    "s_cselect_b32 s47, 0xf149, 0\n"
    "v_mov_b32 v110, s47\n"
    "s_waitcnt lgkmcnt(2)\n"
    "v_mfma_f32_32x32x16_bf16 v[32:47], v[96:99], v[128:131], 0\n"
    "v_mfma_f32_32x32x16_bf16 v[32:47], v[104:107], v[132:135], v[32:47]\n"
    "ds_read_b128 v[96:99], v154 offset:7168\n"
    "ds_read_b64 v[104:105], v155 offset:1792\n"
    "s_cmp_lt_u32 7, %9\n"
    "s_cselect_b32 s44, 0xf149, 0\n"
    "v_mov_b32 v106, s44\n"
    "s_waitcnt lgkmcnt(2)\n"
    "v_mfma_f32_32x32x16_bf16 v[0:15], v[100:103], v[128:131], 0\n"
    "v_mfma_f32_32x32x16_bf16 v[0:15], v[108:111], v[132:135], v[0:15]\n"
    "ds_read_b128 v[100:103], v154 offset:6144\n"
    "ds_read_b64 v[108:109], v155 offset:1536\n"
    "s_cmp_lt_u32 6, %9\n"
    "s_cselect_b32 s45, 0xf149, 0\n"
    "v_mov_b32 v110, s45\n"
    "s_waitcnt lgkmcnt(2)\n"
    "v_mfma_f32_32x32x16_bf16 v[16:31], v[96:99], v[128:131], 0\n"
    "v_mfma_f32_32x32x16_bf16 v[16:31], v[104:107], v[132:135], v[16:31]\n"
    "ds_read_b128 v[96:99], v154 offset:5120\n"
    "ds_read_b64 v[104:105], v155 offset:1280\n"
    "s_cmp_lt_u32 5, %9\n"
    "s_cselect_b32 s46, 0xf149, 0\n"
    "v_mov_b32 v106, s46\n"
    "s_waitcnt lgkmcnt(2)\n"
    "v_mfma_f32_32x32x16_bf16 v[48:63], v[100:103], v[128:131], 0\n"
    "v_cndmask_b32 v32, v0, v32, s[64:65]\n"
    "v_cndmask_b32 v33, v1, v33, s[66:67]\n"
    "v_cndmask_b32 v34, v2, v34, s[68:69]\n"
    "v_cndmask_b32 v35, v3, v35, s[70:71]\n"
    "v_cndmask_b32 v36, v4, v36, s[72:73]\n"
    "v_cndmask_b32 v37, v5, v37, s[74:75]\n"
    "v_cndmask_b32 v38, v6, v38, s[76:77]\n"
    "v_cndmask_b32 v39, v7, v39, s[78:79]\n"
    "v_mfma_f32_32x32x16_bf16 v[48:63], v[108:111], v[132:135], v[48:63]\n"
    "ds_read_b128 v[100:103], v154 offset:4096\n"
    "ds_read_b64 v[108:109], v155 offset:1024\n"
    "s_cmp_lt_u32 4, %9\n"
    "s_cselect_b32 s47, 0xf149, 0\n"
    "v_mov_b32 v110, s47\n"
    "v_cndmask_b32 v40, v8, v40, s[80:81]\n"
    "v_cndmask_b32 v41, v9, v41, s[82:83]\n"
    "v_cndmask_b32 v42, v10, v42, s[84:85]\n"
    "v_cndmask_b32 v43, v11, v43, s[86:87]\n"
    "v_cndmask_b32 v44, v12, v44, s[88:89]\n"
    "v_cndmask_b32 v45, v13, v45, s[90:91]\n"
    "v_cndmask_b32 v46, v14, v46, s[92:93]\n"
    "v_cndmask_b32 v47, v15, v47, s[94:95]\n"
    "s_waitcnt lgkmcnt(2)\n"
    "v_mfma_f32_32x32x16_bf16 v[0:15], v[96:99], v[128:131], 0\n"
    "v_max_f32 v157, v32, v33\n"
    "v_max_f32 v158, v34, v35\n"
    "v_max3_f32 v157, v157, v36, v37\n"
    "v_max3_f32 v158, v158, v38, v39\n"
    "v_max3_f32 v157, v157, v40, v41\n"
    "v_max3_f32 v158, v158, v42, v43\n"
    "v_mfma_f32_32x32x16_bf16 v[0:15], v[104:107], v[132:135], v[0:15]\n"
    "ds_read_b128 v[96:99], v154 offset:3072\n"
    "ds_read_b64 v[104:105], v155 offset:768\n"
    "s_cmp_lt_u32 3, %9\n"
    "s_cselect_b32 s44, 0xf149, 0\n"
    "v_mov_b32 v106, s44\n"
    "v_max3_f32 v157, v157, v44, v45\n"
    "v_max3_f32 v158, v158, v46, v47\n"
    "v_max3_f32 v157, v157, v16, v17\n"
    "v_max3_f32 v158, v158, v18, v19\n"
    "v_max3_f32 v157, v157, v20, v21\n"
    "v_max3_f32 v158, v158, v22, v23\n"
    "v_max3_f32 v157, v157, v24, v25\n"
    "v_max3_f32 v158, v158, v26, v27\n"
    "v_max3_f32 v157, v157, v28, v29\n"
    "v_max3_f32 v158, v158, v30, v31\n"
    "s_waitcnt lgkmcnt(2)\n"
    "v_mfma_f32_32x32x16_bf16 v[16:31], v[100:103], v[128:131], 0\n"
    "v_max3_f32 v157, v157, v48, v49\n"
    "v_max3_f32 v158, v158, v50, v51\n"
    "v_max3_f32 v157, v157, v52, v53\n"
    "v_max3_f32 v158, v158, v54, v55\n"
    "v_max3_f32 v157, v157, v56, v57\n"
    "v_max3_f32 v158, v158, v58, v59\n"
    "v_mfma_f32_32x32x16_bf16 v[16:31], v[108:111], v[132:135], v[16:31]\n"
    "ds_read_b128 v[100:103], v154 offset:2048\n"
    "ds_read_b64 v[108:109], v155 offset:512\n"
    "s_cmp_lt_u32 2, %9\n"
    "s_cselect_b32 s45, 0xf149, 0\n"
    "v_mov_b32 v110, s45\n"
    "v_max3_f32 v157, v157, v60, v61\n"
    "v_max3_f32 v158, v158, v62, v63\n"
    "s_waitcnt lgkmcnt(2)\n"
    "v_mfma_f32_32x32x16_bf16 v[48:63], v[96:99], v[128:131], 0\n"
    "v_max3_f32 v157, v157, v0, v1\n"
    "v_max3_f32 v158, v158, v2, v3\n"
    "v_max3_f32 v157, v157, v4, v5\n"
    "v_max3_f32 v158, v158, v6, v7\n"
    "v_max3_f32 v157, v157, v8, v9\n"
    "v_max3_f32 v158, v158, v10, v11\n"
    "v_mfma_f32_32x32x16_bf16 v[48:63], v[104:107], v[132:135], v[48:63]\n"
    "ds_read_b128 v[96:99], v154 offset:1024\n"
    "ds_read_b64 v[104:105], v155 offset:256\n"
    "s_cmp_lt_u32 1, %9\n"
    "s_cselect_b32 s46, 0xf149, 0\n"
    "v_mov_b32 v106, s46\n"
    "v_max3_f32 v157, v157, v12, v13\n"
    "v_max3_f32 v158, v158, v14, v15\n"
    "s_barrier\n"
    "s_waitcnt lgkmcnt(2)\n"
    "v_mfma_f32_32x32x16_bf16 v[0:15], v[100:103], v[128:131], 0\n"
    "v_max3_f32 v157, v157, v16, v17\n"
    "v_max3_f32 v158, v158, v18, v19\n"
    "v_max3_f32 v157, v157, v20, v21\n"
    "v_max3_f32 v158, v158, v22, v23\n"
    "v_max3_f32 v157, v157, v24, v25\n"
    "v_max3_f32 v158, v158, v26, v27\n"
    "v_mfma_f32_32x32x16_bf16 v[0:15], v[108:111], v[132:135], v[0:15]\n"
    "v_max3_f32 v157, v157, v28, v29\n"
    "v_max3_f32 v158, v158, v30, v31\n"
    "s_waitcnt lgkmcnt(0)\n"
    "v_mfma_f32_32x32x16_bf16 v[16:31], v[96:99], v[128:131], 0\n"
    "v_max3_f32 v157, v157, v48, v49\n"
    "v_max3_f32 v158, v158, v50, v51\n"
    "v_max3_f32 v157, v157, v52, v53\n"
    "v_max3_f32 v158, v158, v54, v55\n"
    "v_max3_f32 v157, v157, v56, v57\n"
    "v_max3_f32 v158, v158, v58, v59\n"
    "v_mfma_f32_32x32x16_bf16 v[16:31], v[104:107], v[132:135], v[16:31]\n"
    "v_max3_f32 v157, v157, v60, v61\n"
    "v_max3_f32 v158, v158, v62, v63\n"
    "ds_read_b128 v[100:103], v154 offset:7168\n"
    "ds_read_b64 v[108:109], v155 offset:1792\n"
    "s_cmp_lt_u32 7, %9\n"
    "s_cselect_b32 s47, 0xf149, 0\n"
    "v_mov_b32 v110, s47\n"
    "ds_read_b128 v[96:99], v154 offset:6144\n"
    "ds_read_b64 v[104:105], v155 offset:1536\n"
    "s_cmp_lt_u32 6, %9\n"
    "s_cselect_b32 s44, 0xf149, 0\n"
    "v_mov_b32 v106, s44\n"
    "ds_read_b128 v[112:115], v156 offset:9728\n"
    "ds_read_b128 v[116:119], v156 offset:10336\n"
    "ds_read_b128 v[120:123], v156 offset:0\n"
    "ds_read_b128 v[124:127], v156 offset:608\n"
    "ds_read_b128 v[136:139], v153 offset:61440\n"
    "ds_read_b128 v[140:143], v153 offset:62464\n"
    "v_max3_f32 v157, v157, v0, v1\n"
    "v_max3_f32 v158, v158, v2, v3\n"
    "v_max3_f32 v157, v157, v4, v5\n"
    "v_max3_f32 v158, v158, v6, v7\n"
    "v_max3_f32 v157, v157, v8, v9\n"
    "v_max3_f32 v158, v158, v10, v11\n"
    "v_max3_f32 v157, v157, v12, v13\n"
    "v_max3_f32 v158, v158, v14, v15\n"
    "v_max3_f32 v157, v157, v16, v17\n"
    "v_max3_f32 v158, v158, v18, v19\n"
    "v_max3_f32 v157, v157, v20, v21\n"
    "v_max3_f32 v158, v158, v22, v23\n"
    "v_max3_f32 v157, v157, v24, v25\n"
    "v_max3_f32 v158, v158, v26, v27\n"
    "v_max3_f32 v157, v157, v28, v29\n"
    "v_max3_f32 v158, v158, v30, v31\n"
    "v_max_f32 v157, v157, v158\n"
    "v_mov_b32 v158, v157\n"
    "s_nop 1\n"
    "v_permlane32_swap_b32 v157, v158\n"
    "v_max_f32 v157, v157, v158\n"
    "v_sub_f32 v48, 0, v157\n"
    "v_sub_f32 v49, 0, v157\n"
    "v_sub_f32 v50, 0, v157\n"
    "v_sub_f32 v51, 0, v157\n"
    "v_sub_f32 v52, 0, v157\n"
    "v_sub_f32 v53, 0, v157\n"
    "v_sub_f32 v54, 0, v157\n"
    "v_sub_f32 v55, 0, v157\n"
    "v_sub_f32 v56, 0, v157\n"
    "v_sub_f32 v57, 0, v157\n"
    "v_sub_f32 v58, 0, v157\n"
    "v_sub_f32 v59, 0, v157\n"
    "v_sub_f32 v60, 0, v157\n"
    "v_sub_f32 v61, 0, v157\n"
    "v_sub_f32 v62, 0, v157\n"
    "v_sub_f32 v63, 0, v157\n"
    "s_setprio 1\n"
    "s_waitcnt lgkmcnt(8)\n"
    "v_mfma_f32_32x32x16_bf16 v[0:15], v[100:103], v[128:131], v[48:63]\n"
    "v_sub_f32 v32, v32, v157\n"
    "v_sub_f32 v33, v33, v157\n"
    "v_sub_f32 v34, v34, v157\n"
    "v_sub_f32 v35, v35, v157\n"
    "v_sub_f32 v36, v36, v157\n"
    "v_sub_f32 v37, v37, v157\n"
    "v_sub_f32 v38, v38, v157\n"
    "v_sub_f32 v39, v39, v157\n"
    "v_mfma_f32_32x32x16_bf16 v[0:15], v[108:111], v[132:135], v[0:15]\n"
    "ds_read_b128 v[100:103], v154 offset:5120\n"
    "ds_read_b64 v[108:109], v155 offset:1280\n"
    "s_cmp_lt_u32 5, %9\n"
    "s_cselect_b32 s45, 0xf149, 0\n"
    "v_mov_b32 v110, s45\n"
    "v_sub_f32 v40, v40, v157\n"
    "v_sub_f32 v41, v41, v157\n"
    "v_sub_f32 v42, v42, v157\n"
    "v_sub_f32 v43, v43, v157\n"
    "v_sub_f32 v44, v44, v157\n"
    "v_sub_f32 v45, v45, v157\n"
    "v_sub_f32 v46, v46, v157\n"
    "v_sub_f32 v47, v47, v157\n"
    "v_exp_f32 v32, v32\n"
    "v_exp_f32 v33, v33\n"
    "v_exp_f32 v34, v34\n"
    "v_exp_f32 v35, v35\n"
    "v_exp_f32 v36, v36\n"
    "v_exp_f32 v37, v37\n"
    "v_exp_f32 v38, v38\n"
    "v_exp_f32 v39, v39\n"
    "v_cvt_pk_bf16_f32 v144, v32, v33\n"
    "v_cvt_pk_bf16_f32 v145, v34, v35\n"
    "v_exp_f32 v40, v40\n"
    "v_exp_f32 v41, v41\n"
    "v_exp_f32 v42, v42\n"
    "v_exp_f32 v43, v43\n"
    "v_cvt_pk_bf16_f32 v146, v36, v37\n"
    "v_cvt_pk_bf16_f32 v147, v38, v39\n"
    "v_exp_f32 v44, v44\n"
    "v_exp_f32 v45, v45\n"
    "v_exp_f32 v46, v46\n"
    "v_exp_f32 v47, v47\n"
    "v_cvt_pk_bf16_f32 v148, v40, v41\n"
    "v_cvt_pk_bf16_f32 v149, v42, v43\n"
    "v_cvt_pk_bf16_f32 v150, v44, v45\n"
    "v_cvt_pk_bf16_f32 v151, v46, v47\n"
    "s_waitcnt lgkmcnt(2)\n"
    "v_and_b32 v88, v136, v144\n"
    "v_and_b32 v89, v137, v145\n"
    "v_and_b32 v90, v138, v146\n"
    "v_and_b32 v91, v139, v147\n"
    "v_and_b32 v92, v140, v148\n"
    "v_and_b32 v93, v141, v149\n"
    "v_and_b32 v94, v142, v150\n"
    "v_and_b32 v95, v143, v151\n"
    "v_xor_b32 v144, v88, v144\n"
    "v_xor_b32 v145, v89, v145\n"
    "v_xor_b32 v146, v90, v146\n"
    "v_xor_b32 v147, v91, v147\n"
    "v_xor_b32 v148, v92, v148\n"
    "v_xor_b32 v149, v93, v149\n"
    "v_xor_b32 v150, v94, v150\n"
    "v_xor_b32 v151, v95, v151\n"
    "v_mfma_f32_32x32x16_bf16 v[64:79], v[112:115], v[88:91], 0\n"
    "v_exp_f32 v0, v0\n"
    "v_exp_f32 v1, v1\n"
    "v_mfma_f32_32x32x16_bf16 v[64:79], v[116:119], v[92:95], v[64:79]\n"
    "ds_read_b128 v[112:115], v156 offset:8512\n"
    "ds_read_b128 v[116:119], v156 offset:9120\n"
    "v_exp_f32 v2, v2\n"
    "v_exp_f32 v3, v3\n"
    "v_mfma_f32_32x32x16_bf16 v[16:31], v[96:99], v[128:131], v[48:63]\n"
    "v_exp_f32 v4, v4\n"
    "v_exp_f32 v5, v5\n"
    "v_exp_f32 v6, v6\n"
    "v_mfma_f32_32x32x16_bf16 v[16:31], v[104:107], v[132:135], v[16:31]\n"
    "ds_read_b128 v[96:99], v154 offset:4096\n"
    "ds_read_b64 v[104:105], v155 offset:1024\n"
    "s_cmp_lt_u32 4, %9\n"
    "s_cselect_b32 s46, 0xf149, 0\n"
    "v_mov_b32 v106, s46\n"
    "v_exp_f32 v7, v7\n"
    "v_cvt_pk_bf16_f32 v80, v0, v1\n"
    "v_cvt_pk_bf16_f32 v81, v2, v3\n"
    "v_mfma_f32_32x32x16_bf16 v[64:79], v[120:123], v[144:147], v[64:79]\n"
    "v_exp_f32 v8, v8\n"
    "v_exp_f32 v9, v9\n"
    "v_exp_f32 v10, v10\n"
    "v_exp_f32 v11, v11\n"
    "v_cvt_pk_bf16_f32 v82, v4, v5\n"
    "v_cvt_pk_bf16_f32 v83, v6, v7\n"
    "v_mfma_f32_32x32x16_bf16 v[64:79], v[124:127], v[148:151], v[64:79]\n"
    "ds_read_b128 v[120:123], v156 offset:7296\n"
    "ds_read_b128 v[124:127], v156 offset:7904\n"
    "v_exp_f32 v12, v12\n"
    "v_exp_f32 v13, v13\n"
    "v_exp_f32 v14, v14\n"
    "v_exp_f32 v15, v15\n"
    "v_cvt_pk_bf16_f32 v84, v8, v9\n"
    "v_cvt_pk_bf16_f32 v85, v10, v11\n"
    "v_cvt_pk_bf16_f32 v86, v12, v13\n"
    "v_cvt_pk_bf16_f32 v87, v14, v15\n"
    "s_waitcnt lgkmcnt(6)\n"
    "v_mfma_f32_32x32x16_bf16 v[0:15], v[100:103], v[128:131], v[48:63]\n"
    "v_exp_f32 v16, v16\n"
    "v_exp_f32 v17, v17\n"
    "v_exp_f32 v18, v18\n"
    "v_exp_f32 v19, v19\n"
    "v_mfma_f32_32x32x16_bf16 v[0:15], v[108:111], v[132:135], v[0:15]\n"
    "ds_read_b128 v[100:103], v154 offset:3072\n"
    "ds_read_b64 v[108:109], v155 offset:768\n"
    "s_cmp_lt_u32 3, %9\n"
    "s_cselect_b32 s47, 0xf149, 0\n"
    "v_mov_b32 v110, s47\n"
    "v_exp_f32 v20, v20\n"
    "v_exp_f32 v21, v21\n"
    "v_exp_f32 v22, v22\n"
    "v_exp_f32 v23, v23\n"
    "v_cvt_pk_bf16_f32 v88, v16, v17\n"
    "v_cvt_pk_bf16_f32 v89, v18, v19\n"
    "s_waitcnt lgkmcnt(6)\n"
    "v_mfma_f32_32x32x16_bf16 v[64:79], v[112:115], v[80:83], v[64:79]\n"
    "v_exp_f32 v24, v24\n"
    "v_exp_f32 v25, v25\n"
    "v_exp_f32 v26, v26\n"
    "v_exp_f32 v27, v27\n"
    "v_cvt_pk_bf16_f32 v90, v20, v21\n"
    "v_cvt_pk_bf16_f32 v91, v22, v23\n"
    "v_mfma_f32_32x32x16_bf16 v[64:79], v[116:119], v[84:87], v[64:79]\n"
    "ds_read_b128 v[112:115], v156 offset:6080\n"
    "ds_read_b128 v[116:119], v156 offset:6688\n"
    "v_exp_f32 v28, v28\n"
    "v_exp_f32 v29, v29\n"
    "v_exp_f32 v30, v30\n"
    "v_exp_f32 v31, v31\n"
    "v_cvt_pk_bf16_f32 v92, v24, v25\n"
    "v_cvt_pk_bf16_f32 v93, v26, v27\n"
    "v_cvt_pk_bf16_f32 v94, v28, v29\n"
    "v_cvt_pk_bf16_f32 v95, v30, v31\n"
    "s_waitcnt lgkmcnt(6)\n"
    "v_mfma_f32_32x32x16_bf16 v[16:31], v[96:99], v[128:131], v[48:63]\n"
    "v_exp_f32 v0, v0\n"
    "v_exp_f32 v1, v1\n"
    "v_exp_f32 v2, v2\n"
    "v_exp_f32 v3, v3\n"
    "v_mfma_f32_32x32x16_bf16 v[16:31], v[104:107], v[132:135], v[16:31]\n"
    "ds_read_b128 v[96:99], v154 offset:2048\n"
    "ds_read_b64 v[104:105], v155 offset:512\n"
    "s_cmp_lt_u32 2, %9\n"
    "s_cselect_b32 s44, 0xf149, 0\n"
    "v_mov_b32 v106, s44\n"
    "v_exp_f32 v4, v4\n"
    "v_exp_f32 v5, v5\n"
    "v_exp_f32 v6, v6\n"
    "v_exp_f32 v7, v7\n"
    "v_cvt_pk_bf16_f32 v80, v0, v1\n"
    "v_cvt_pk_bf16_f32 v81, v2, v3\n"
    "s_waitcnt lgkmcnt(6)\n"
    "v_mfma_f32_32x32x16_bf16 v[64:79], v[120:123], v[88:91], v[64:79]\n"
    "v_exp_f32 v8, v8\n"
    "v_exp_f32 v9, v9\n"
    "v_exp_f32 v10, v10\n"
    "v_exp_f32 v11, v11\n"
    "v_cvt_pk_bf16_f32 v82, v4, v5\n"
    "v_cvt_pk_bf16_f32 v83, v6, v7\n"
    "v_mfma_f32_32x32x16_bf16 v[64:79], v[124:127], v[92:95], v[64:79]\n"
    "ds_read_b128 v[120:123], v156 offset:4864\n"
    "ds_read_b128 v[124:127], v156 offset:5472\n"
    "v_exp_f32 v12, v12\n"
    "v_exp_f32 v13, v13\n"
    "v_exp_f32 v14, v14\n"
    "v_exp_f32 v15, v15\n"
    "v_cvt_pk_bf16_f32 v84, v8, v9\n"
    "v_cvt_pk_bf16_f32 v85, v10, v11\n"
    "v_cvt_pk_bf16_f32 v86, v12, v13\n"
    "v_cvt_pk_bf16_f32 v87, v14, v15\n"
    "s_waitcnt lgkmcnt(6)\n"
    "v_mfma_f32_32x32x16_bf16 v[0:15], v[100:103], v[128:131], v[48:63]\n"
    "v_exp_f32 v16, v16\n"
    "v_exp_f32 v17, v17\n"
    "v_exp_f32 v18, v18\n"
    "v_exp_f32 v19, v19\n"
    "v_mfma_f32_32x32x16_bf16 v[0:15], v[108:111], v[132:135], v[0:15]\n"
    "ds_read_b128 v[100:103], v154 offset:1024\n"
    "ds_read_b64 v[108:109], v155 offset:256\n"
    "s_cmp_lt_u32 1, %9\n"
    "s_cselect_b32 s45, 0xf149, 0\n"
    "v_mov_b32 v110, s45\n"
    "v_exp_f32 v20, v20\n"
    "v_exp_f32 v21, v21\n"
    "v_exp_f32 v22, v22\n"
    "v_exp_f32 v23, v23\n"
    "v_cvt_pk_bf16_f32 v88, v16, v17\n"
    "v_cvt_pk_bf16_f32 v89, v18, v19\n"
    "s_waitcnt lgkmcnt(6)\n"
    "v_mfma_f32_32x32x16_bf16 v[64:79], v[112:115], v[80:83], v[64:79]\n"
    "v_exp_f32 v24, v24\n"
    "v_exp_f32 v25, v25\n"
    "v_exp_f32 v26, v26\n"
    "v_exp_f32 v27, v27\n"
    "v_cvt_pk_bf16_f32 v90, v20, v21\n"
    "v_cvt_pk_bf16_f32 v91, v22, v23\n"
    "v_mfma_f32_32x32x16_bf16 v[64:79], v[116:119], v[84:87], v[64:79]\n"
    "ds_read_b128 v[112:115], v156 offset:3648\n"
    "ds_read_b128 v[116:119], v156 offset:4256\n"
    "v_exp_f32 v28, v28\n"
    "v_exp_f32 v29, v29\n"
    "v_exp_f32 v30, v30\n"
    "v_exp_f32 v31, v31\n"
    "v_cvt_pk_bf16_f32 v92, v24, v25\n"
    "v_cvt_pk_bf16_f32 v93, v26, v27\n"
    "v_cvt_pk_bf16_f32 v94, v28, v29\n"
    "v_cvt_pk_bf16_f32 v95, v30, v31\n"
    "s_waitcnt lgkmcnt(6)\n"
    "v_mfma_f32_32x32x16_bf16 v[16:31], v[96:99], v[128:131], v[48:63]\n"
    "v_exp_f32 v0, v0\n"
    "v_exp_f32 v1, v1\n"
    "v_exp_f32 v2, v2\n"
    "v_exp_f32 v3, v3\n"
    "v_mfma_f32_32x32x16_bf16 v[16:31], v[104:107], v[132:135], v[16:31]\n"
    "v_exp_f32 v4, v4\n"
    "v_exp_f32 v5, v5\n"
    "v_exp_f32 v6, v6\n"
    "v_exp_f32 v7, v7\n"
    "v_cvt_pk_bf16_f32 v80, v0, v1\n"
    "v_cvt_pk_bf16_f32 v81, v2, v3\n"
    "s_waitcnt lgkmcnt(4)\n"
    "v_mfma_f32_32x32x16_bf16 v[64:79], v[120:123], v[88:91], v[64:79]\n"
    "v_exp_f32 v8, v8\n"
    "v_exp_f32 v9, v9\n"
    "v_exp_f32 v10, v10\n"
    "v_exp_f32 v11, v11\n"
    "v_cvt_pk_bf16_f32 v82, v4, v5\n"
    "v_cvt_pk_bf16_f32 v83, v6, v7\n"
    "v_mfma_f32_32x32x16_bf16 v[64:79], v[124:127], v[92:95], v[64:79]\n"
    "ds_read_b128 v[120:123], v156 offset:2432\n"
    "ds_read_b128 v[124:127], v156 offset:3040\n"
    "v_exp_f32 v12, v12\n"
    "v_exp_f32 v13, v13\n"
    "v_exp_f32 v14, v14\n"
    "v_exp_f32 v15, v15\n"
    "v_cvt_pk_bf16_f32 v84, v8, v9\n"
    "v_cvt_pk_bf16_f32 v85, v10, v11\n"
    "v_cvt_pk_bf16_f32 v86, v12, v13\n"
    "v_cvt_pk_bf16_f32 v87, v14, v15\n"
    "s_waitcnt lgkmcnt(4)\n"
    "v_mfma_f32_32x32x16_bf16 v[0:15], v[100:103], v[128:131], v[48:63]\n"
    "v_exp_f32 v16, v16\n"
    "v_exp_f32 v17, v17\n"
    "v_exp_f32 v18, v18\n"
    "v_exp_f32 v19, v19\n"
    "v_mfma_f32_32x32x16_bf16 v[0:15], v[108:111], v[132:135], v[0:15]\n"
    "v_exp_f32 v20, v20\n"
    "v_exp_f32 v21, v21\n"
    "v_exp_f32 v22, v22\n"
    "v_exp_f32 v23, v23\n"
    "v_cvt_pk_bf16_f32 v88, v16, v17\n"
    "v_cvt_pk_bf16_f32 v89, v18, v19\n"
    "s_waitcnt lgkmcnt(2)\n"
    "v_mfma_f32_32x32x16_bf16 v[64:79], v[112:115], v[80:83], v[64:79]\n"
    "v_exp_f32 v24, v24\n"
    "v_exp_f32 v25, v25\n"
    "v_exp_f32 v26, v26\n"
    "v_exp_f32 v27, v27\n"
    "v_cvt_pk_bf16_f32 v90, v20, v21\n"
    "v_cvt_pk_bf16_f32 v91, v22, v23\n"
    "v_mfma_f32_32x32x16_bf16 v[64:79], v[116:119], v[84:87], v[64:79]\n"
    "ds_read_b128 v[112:115], v156 offset:1216\n"
    "ds_read_b128 v[116:119], v156 offset:1824\n"
    "v_exp_f32 v28, v28\n"
    "v_exp_f32 v29, v29\n"
    "v_exp_f32 v30, v30\n"
    "v_exp_f32 v31, v31\n"
    "v_cvt_pk_bf16_f32 v92, v24, v25\n"
    "v_cvt_pk_bf16_f32 v93, v26, v27\n"
    "v_cvt_pk_bf16_f32 v94, v28, v29\n"
    "v_cvt_pk_bf16_f32 v95, v30, v31\n"
    "s_waitcnt lgkmcnt(2)\n"
    "v_mfma_f32_32x32x16_bf16 v[64:79], v[120:123], v[88:91], v[64:79]\n"
    "v_exp_f32 v0, v0\n"
    "v_exp_f32 v1, v1\n"
    "v_exp_f32 v2, v2\n"
    "v_exp_f32 v3, v3\n"
    "v_mfma_f32_32x32x16_bf16 v[64:79], v[124:127], v[92:95], v[64:79]\n"
    "v_exp_f32 v4, v4\n"
    "v_exp_f32 v5, v5\n"
    "v_exp_f32 v6, v6\n"
    "v_exp_f32 v7, v7\n"
    "v_cvt_pk_bf16_f32 v80, v0, v1\n"
    "v_cvt_pk_bf16_f32 v81, v2, v3\n"
    "v_exp_f32 v8, v8\n"
    "v_exp_f32 v9, v9\n"
    "v_exp_f32 v10, v10\n"
    "v_exp_f32 v11, v11\n"
    "v_cvt_pk_bf16_f32 v82, v4, v5\n"
    "v_cvt_pk_bf16_f32 v83, v6, v7\n"
    "v_exp_f32 v12, v12\n"
    "v_exp_f32 v13, v13\n"
    "v_exp_f32 v14, v14\n"
    "v_exp_f32 v15, v15\n"
    "v_cvt_pk_bf16_f32 v84, v8, v9\n"
    "v_cvt_pk_bf16_f32 v85, v10, v11\n"
    "s_waitcnt lgkmcnt(0)\n"
    "v_mfma_f32_32x32x16_bf16 v[64:79], v[112:115], v[80:83], v[64:79]\n"
    "v_cvt_pk_bf16_f32 v86, v12, v13\n"
    "v_cvt_pk_bf16_f32 v87, v14, v15\n"
    "s_nop 1\n"
    "v_mfma_f32_32x32x16_bf16 v[64:79], v[116:119], v[84:87], v[64:79]\n"
    "s_nop 11\n"
    "v_mov_b32 v159, v74\n"
    "s_nop 1\n"
    "v_permlane32_swap_b32 v74, v159\n"
    "v_rcp_f32 v159, v74\n"
    "s_nop 0\n"
    "v_mul_f32 v64, v64, v159\n"
    "v_mul_f32 v65, v65, v159\n"
    "v_mul_f32 v66, v66, v159\n"
    "v_mul_f32 v67, v67, v159\n"
    "v_mul_f32 v68, v68, v159\n"
    "v_mul_f32 v69, v69, v159\n"
    "v_mul_f32 v70, v70, v159\n"
    "v_mul_f32 v71, v71, v159\n"
    "v_mul_f32 v72, v72, v159\n"
    "v_mul_f32 v73, v73, v159\n"
    "v_cvt_pk_bf16_f32 v140, v64, v65\n"
    "v_cvt_pk_bf16_f32 v141, v66, v67\n"
    "v_cvt_pk_bf16_f32 v142, v68, v69\n"
    "v_cvt_pk_bf16_f32 v143, v70, v71\n"
    "v_cvt_pk_bf16_f32 v144, v72, v73\n"
    "buffer_store_dwordx4 v[140:143], v153, s[36:39], %10 offen sc1\n"
    "s_mov_b32 exec_hi, 0\n"
    "buffer_store_dword v144, v165, s[36:39], %11 offen sc1\n"
    "s_mov_b32 exec_hi, -1\n"
    "s_setprio 2\n"
    "s_waitcnt lgkmcnt(0)\n"
    "s_cmp_ge_u32 %4, 4\n"
    "s_cbranch_scc1 .Lb_%=\n"
    "s_add_u32 s46, %13, 0\n"
    "buffer_load_dwordx4 v[36:39], v153, s[36:39], s46 offen\n"
    "s_add_u32 s47, %13, 8192\n"
    "buffer_load_dwordx4 v[40:43], v153, s[36:39], s47 offen\n"
    "s_add_u32 s44, %13, 16384\n"
    "buffer_load_dwordx4 v[44:47], v153, s[36:39], s44 offen\n"
    "s_add_u32 s45, %13, 24576\n"
    "buffer_load_dwordx4 v[48:51], v153, s[36:39], s45 offen\n"
    "s_add_u32 s46, %13, 32768\n"
    "buffer_load_dwordx4 v[52:55], v153, s[36:39], s46 offen\n"
    "s_add_u32 s47, %13, 40960\n"
    "buffer_load_dwordx4 v[56:59], v153, s[36:39], s47 offen\n"
    "s_add_u32 s44, %13, 49152\n"
    "buffer_load_dwordx4 v[60:63], v153, s[36:39], s44 offen\n"
    "s_add_u32 s45, %13, 57344\n"
    "buffer_load_dwordx4 v[64:67], v153, s[36:39], s45 offen\n"
    "s_add_u32 s46, %13, 65536\n"
    "buffer_load_dwordx4 v[68:71], v153, s[36:39], s46 offen\n"
    "s_add_u32 s47, %13, 73728\n"
    "buffer_load_dwordx4 v[72:75], v153, s[36:39], s47 offen\n"
    "s_add_u32 s44, %13, 81920\n"
    "buffer_load_dwordx4 v[76:79], v153, s[36:39], s44 offen\n"
    "s_add_u32 s45, %13, 90112\n"
    "buffer_load_dwordx4 v[80:83], v153, s[36:39], s45 offen\n"
    "s_add_u32 s46, %13, 98304\n"
    "buffer_load_dwordx4 v[84:87], v153, s[36:39], s46 offen\n"
    "s_add_u32 s47, %13, 106496\n"
    "buffer_load_dwordx4 v[88:91], v153, s[36:39], s47 offen\n"
    "s_add_u32 s44, %13, 114688\n"
    "buffer_load_dwordx4 v[92:95], v153, s[36:39], s44 offen\n"
    "s_add_u32 s45, %13, 122880\n"
    "buffer_load_dwordx4 v[96:99], v153, s[36:39], s45 offen\n"
    "s_add_u32 s46, %13, 131072\n"
    "buffer_load_dwordx4 v[100:103], v153, s[36:39], s46 offen\n"
    "s_add_u32 s47, %13, 139264\n"
    "buffer_load_dwordx4 v[104:107], v153, s[36:39], s47 offen\n"
    "s_mov_b32 s40, %2\n"
    "s_and_b32 s41, %3, 0xffff\n"
    "s_mov_b32 s42, 2359296\n"
    "s_mov_b32 s43, 0x20000\n"
    "v_and_b32 v159, 31, v152\n"
    "v_lshrrev_b32 v160, 5, v152\n"
    "v_mul_u32_u24 v163, 1152, v159\n"
    "v_lshl_add_u32 v163, v160, 4, v163\n"
    "buffer_load_dwordx4 v[148:151], v163, s[40:43], %14 offen\n"
    "buffer_load_dword v162, v165, s[36:39], %16 offen\n"
    "v_bfe_u32 v164, v152, 4, 1\n"
    "v_mul_u32_u24 v164, 18432, v164\n"
    "v_lshl_add_u32 v164, v160, 8, v164\n"
    "v_and_b32 v161, 15, v152\n"
    "v_lshl_add_u32 v164, v161, 4, v164\n"
    "v_lshrrev_b32 v160, 4, v152\n"
    "v_lshlrev_b32 v156, 9, v160\n"
    "v_lshl_add_u32 v156, v161, 4, v156\n"
    "v_add_u32 v156, 66048, v156\n"
    "s_lshl_b32 s49, %19, 8\n"
    "s_add_u32 s49, s49, 63616\n"
    "v_lshl_add_u32 v157, v152, 2, s49\n"
    "v_add_u32 v158, 63488, v165\n"
    "s_barrier\n"
    "ds_read_b128 v[116:119], v156 offset:0\n"
    "ds_read_b128 v[120:123], v156 offset:256\n"
    "ds_read_b128 v[124:127], v156 offset:2048\n"
    "ds_read_b128 v[128:131], v156 offset:2304\n"
    "ds_read_b128 v[132:135], v156 offset:4096\n"
    "ds_read_b128 v[136:139], v156 offset:4352\n"
    "ds_read_b128 v[140:143], v156 offset:6144\n"
    "ds_read_b128 v[144:147], v156 offset:6400\n"
    "ds_read_b128 v[0:3], v156 offset:8192\n"
    "ds_read_b128 v[4:7], v156 offset:8448\n"
    "ds_read_b128 v[8:11], v156 offset:10240\n"
    "ds_read_b128 v[12:15], v156 offset:10496\n"
    "s_waitcnt vmcnt(19) lgkmcnt(10)\n"
    "v_mfma_f32_16x16x32_bf16 v[108:111], v[36:39], v[116:119], 0\n"
    "ds_read_b128 v[16:19], v156 offset:12288\n"
    "ds_read_b128 v[20:23], v156 offset:12544\n"
    "v_mfma_f32_16x16x32_bf16 v[112:115], v[36:39], v[120:123], 0\n"
    "s_waitcnt vmcnt(18) lgkmcnt(10)\n"
    "v_mfma_f32_16x16x32_bf16 v[108:111], v[40:43], v[124:127], v[108:111]\n"
    "ds_read_b128 v[24:27], v156 offset:14336\n"
    "ds_read_b128 v[28:31], v156 offset:14592\n"
    "v_mfma_f32_16x16x32_bf16 v[112:115], v[40:43], v[128:131], v[112:115]\n"
    "s_waitcnt vmcnt(17) lgkmcnt(10)\n"
    "v_mfma_f32_16x16x32_bf16 v[108:111], v[44:47], v[132:135], v[108:111]\n"
    "ds_read_b128 v[116:119], v156 offset:16384\n"
    "ds_read_b128 v[120:123], v156 offset:16640\n"
    "v_mfma_f32_16x16x32_bf16 v[112:115], v[44:47], v[136:139], v[112:115]\n"
    "s_waitcnt vmcnt(16) lgkmcnt(10)\n"
    "v_mfma_f32_16x16x32_bf16 v[108:111], v[48:51], v[140:143], v[108:111]\n"
    "v_mfma_f32_16x16x32_bf16 v[112:115], v[48:51], v[144:147], v[112:115]\n"
    "s_waitcnt vmcnt(15) lgkmcnt(8)\n"
    "v_mfma_f32_16x16x32_bf16 v[108:111], v[52:55], v[0:3], v[108:111]\n"
    "v_mfma_f32_16x16x32_bf16 v[112:115], v[52:55], v[4:7], v[112:115]\n"
    "s_waitcnt vmcnt(14) lgkmcnt(6)\n"
    "v_mfma_f32_16x16x32_bf16 v[108:111], v[56:59], v[8:11], v[108:111]\n"
    "v_mfma_f32_16x16x32_bf16 v[112:115], v[56:59], v[12:15], v[112:115]\n"
    "s_waitcnt vmcnt(13) lgkmcnt(4)\n"
    "v_mfma_f32_16x16x32_bf16 v[108:111], v[60:63], v[16:19], v[108:111]\n"
    "v_mfma_f32_16x16x32_bf16 v[112:115], v[60:63], v[20:23], v[112:115]\n"
    "s_waitcnt vmcnt(12) lgkmcnt(2)\n"
    "v_mfma_f32_16x16x32_bf16 v[108:111], v[64:67], v[24:27], v[108:111]\n"
    "v_mfma_f32_16x16x32_bf16 v[112:115], v[64:67], v[28:31], v[112:115]\n"
    "s_waitcnt vmcnt(11) lgkmcnt(0)\n"
    "v_mfma_f32_16x16x32_bf16 v[108:111], v[68:71], v[116:119], v[108:111]\n"
    "v_mfma_f32_16x16x32_bf16 v[112:115], v[68:71], v[120:123], v[112:115]\n"
    "s_barrier\n"
    "ds_read_b128 v[124:127], v156 offset:18432\n"
    "ds_read_b128 v[128:131], v156 offset:18688\n"
    "ds_read_b128 v[132:135], v156 offset:20480\n"
    "ds_read_b128 v[136:139], v156 offset:20736\n"
    "ds_read_b128 v[140:143], v156 offset:22528\n"
    "ds_read_b128 v[144:147], v156 offset:22784\n"
    "ds_read_b128 v[0:3], v156 offset:24576\n"
    "ds_read_b128 v[4:7], v156 offset:24832\n"
    "ds_read_b128 v[8:11], v156 offset:26624\n"
    "ds_read_b128 v[12:15], v156 offset:26880\n"
    "ds_read_b128 v[16:19], v156 offset:28672\n"
    "ds_read_b128 v[20:23], v156 offset:28928\n"
    "s_waitcnt vmcnt(10) lgkmcnt(10)\n"
    "v_mfma_f32_16x16x32_bf16 v[108:111], v[72:75], v[124:127], v[108:111]\n"
    "ds_read_b128 v[24:27], v156 offset:30720\n"
    "ds_read_b128 v[28:31], v156 offset:30976\n"
    "v_mfma_f32_16x16x32_bf16 v[112:115], v[72:75], v[128:131], v[112:115]\n"
    "s_waitcnt vmcnt(9) lgkmcnt(10)\n"
    "v_mfma_f32_16x16x32_bf16 v[108:111], v[76:79], v[132:135], v[108:111]\n"
    "ds_read_b128 v[116:119], v156 offset:32768\n"
    "ds_read_b128 v[120:123], v156 offset:33024\n"
    "v_mfma_f32_16x16x32_bf16 v[112:115], v[76:79], v[136:139], v[112:115]\n"
    "s_waitcnt vmcnt(8) lgkmcnt(10)\n"
    "v_mfma_f32_16x16x32_bf16 v[108:111], v[80:83], v[140:143], v[108:111]\n"
    "ds_read_b128 v[124:127], v156 offset:34816\n"
    "ds_read_b128 v[128:131], v156 offset:35072\n"
    "v_mfma_f32_16x16x32_bf16 v[112:115], v[80:83], v[144:147], v[112:115]\n"
    "s_waitcnt vmcnt(7) lgkmcnt(10)\n"
    "v_mfma_f32_16x16x32_bf16 v[108:111], v[84:87], v[0:3], v[108:111]\n"
    "v_mfma_f32_16x16x32_bf16 v[112:115], v[84:87], v[4:7], v[112:115]\n"
    "s_waitcnt vmcnt(6) lgkmcnt(8)\n"
    "v_mfma_f32_16x16x32_bf16 v[108:111], v[88:91], v[8:11], v[108:111]\n"
    "v_mfma_f32_16x16x32_bf16 v[112:115], v[88:91], v[12:15], v[112:115]\n"
    "s_waitcnt vmcnt(5) lgkmcnt(6)\n"
    "v_mfma_f32_16x16x32_bf16 v[108:111], v[92:95], v[16:19], v[108:111]\n"
    "v_mfma_f32_16x16x32_bf16 v[112:115], v[92:95], v[20:23], v[112:115]\n"
    "s_waitcnt vmcnt(4) lgkmcnt(4)\n"
    "v_mfma_f32_16x16x32_bf16 v[108:111], v[96:99], v[24:27], v[108:111]\n"
    "v_mfma_f32_16x16x32_bf16 v[112:115], v[96:99], v[28:31], v[112:115]\n"
    "s_waitcnt vmcnt(3) lgkmcnt(2)\n"
    "v_mfma_f32_16x16x32_bf16 v[108:111], v[100:103], v[116:119], v[108:111]\n"
    "v_mfma_f32_16x16x32_bf16 v[112:115], v[100:103], v[120:123], v[112:115]\n"
    "s_waitcnt vmcnt(2) lgkmcnt(0)\n"
    "v_mfma_f32_16x16x32_bf16 v[108:111], v[104:107], v[124:127], v[108:111]\n"
    "v_mfma_f32_16x16x32_bf16 v[112:115], v[104:107], v[128:131], v[112:115]\n"
    "s_waitcnt vmcnt(0)\n"
    "s_nop 6\n"
    "v_permlane16_swap_b32 v108, v112\n"
    "v_permlane16_swap_b32 v109, v113\n"
    "v_permlane16_swap_b32 v110, v114\n"
    "v_permlane16_swap_b32 v111, v115\n"
    "v_lshlrev_b32 v160, 16, v148\n"
    "v_and_b32 v161, 0xffff0000, v148\n"
    "v_add_f32 v108, v108, v160\n"
    "v_add_f32 v109, v109, v161\n"
    "v_lshlrev_b32 v160, 16, v149\n"
    "v_and_b32 v161, 0xffff0000, v149\n"
    "v_add_f32 v110, v110, v160\n"
    "v_add_f32 v111, v111, v161\n"
    "v_lshlrev_b32 v160, 16, v150\n"
    "v_and_b32 v161, 0xffff0000, v150\n"
    "v_add_f32 v112, v112, v160\n"
    "v_add_f32 v113, v113, v161\n"
    "v_lshlrev_b32 v160, 16, v151\n"
    "v_and_b32 v161, 0xffff0000, v151\n"
    "v_add_f32 v114, v114, v160\n"
    "v_add_f32 v115, v115, v161\n"
    "s_barrier\n"
    "ds_read_b32 v159, v158\n"
    "s_waitcnt lgkmcnt(0)\n"
    "v_add_f32 v162, v162, v159\n"
    "v_sub_f32 v108, v108, v162\n"
    "v_sub_f32 v109, v109, v162\n"
    "v_sub_f32 v110, v110, v162\n"
    "v_sub_f32 v111, v111, v162\n"
    "v_sub_f32 v112, v112, v162\n"
    "v_sub_f32 v113, v113, v162\n"
    "v_sub_f32 v114, v114, v162\n"
    "v_sub_f32 v115, v115, v162\n"
    "v_cvt_pk_bf16_f32 v148, v108, v109\n"
    "v_cvt_pk_bf16_f32 v149, v110, v111\n"
    "v_cvt_pk_bf16_f32 v150, v112, v113\n"
    "v_cvt_pk_bf16_f32 v151, v114, v115\n"
    "buffer_store_dwordx4 v[148:151], v164, s[36:39], %15 offen\n"
    "v_dot2_f32_bf16 v160, v148, v148, 0\n"
    "v_dot2_f32_bf16 v160, v149, v149, v160\n"
    "v_dot2_f32_bf16 v160, v150, v150, v160\n"
    "v_dot2_f32_bf16 v160, v151, v151, v160\n"
    "s_nop 2\n"
    "ds_write_b32 v157, v160\n"
    "s_waitcnt lgkmcnt(0)\n"
    "s_barrier\n"
    "s_branch .Le_%=\n"
    ".Lb_%=:\n"
    "v_mov_b32 v159, 65984\n"
    "v_mov_b32 v160, 1\n"
    "s_mov_b64 exec, 1\n"
    "ds_add_u32 v159, v160\n"
    "s_mov_b64 exec, -1\n"
    "s_waitcnt lgkmcnt(0)\n"
    "s_cmp_ge_u32 %4, 6\n"
    "s_cbranch_scc1 .Lb6_%=\n"
    "s_mov_b32 s40, %2\n"
    "s_and_b32 s41, %3, 0xffff\n"
    "s_mov_b32 s42, 2359296\n"
    "s_mov_b32 s43, 0x20000\n"
    "v_and_b32 v159, 31, v152\n"
    "v_lshrrev_b32 v160, 5, v152\n"
    "v_mul_u32_u24 v163, 1152, v159\n"
    "v_lshl_add_u32 v163, v160, 4, v163\n"
    "buffer_load_dwordx4 v[148:151], v163, s[40:43], %14 offen\n"
    "buffer_load_dword v162, v165, s[36:39], %16 offen\n"
    "v_bfe_u32 v164, v152, 4, 1\n"
    "v_mul_u32_u24 v164, 18432, v164\n"
    "v_lshl_add_u32 v164, v160, 8, v164\n"
    "v_and_b32 v161, 15, v152\n"
    "v_lshl_add_u32 v164, v161, 4, v164\n"
    "v_lshrrev_b32 v160, 4, v152\n"
    "v_lshlrev_b32 v156, 9, v160\n"
    "v_lshl_add_u32 v156, v161, 4, v156\n"
    "v_add_u32 v156, 66048, v156\n"
    "s_lshl_b32 s49, %19, 8\n"
    "s_add_u32 s49, s49, 63616\n"
    "v_lshl_add_u32 v157, v152, 2, s49\n"
    "v_add_u32 v158, 63488, v165\n"
    "v_and_b32 v159, 1, v152\n"
    "v_lshrrev_b32 v160, 4, v152\n"
    "v_lshl_add_u32 v159, v160, 1, v159\n"
    "v_lshlrev_b32 v159, 4, v159\n"
    "v_add_u32 v159, 1024, v159\n"
    "s_cmp_eq_u32 %4, 4\n"
    "s_cselect_b64 vcc, -1, 0\n"
    "v_cndmask_b32 v154, v159, v153, vcc\n"
    "v_add_u32 v154, 102912, v154\n"
    "s_setprio 2\n"
    "s_barrier\n"
    "ds_read_b128 v[116:119], v156 offset:0\n"
    "ds_read_b128 v[120:123], v156 offset:256\n"
    "ds_read_b128 v[36:39], v154 offset:0\n"
    "ds_read_b128 v[124:127], v156 offset:2048\n"
    "ds_read_b128 v[128:131], v156 offset:2304\n"
    "ds_read_b128 v[40:43], v154 offset:1152\n"
    "ds_read_b128 v[132:135], v156 offset:4096\n"
    "ds_read_b128 v[136:139], v156 offset:4352\n"
    "ds_read_b128 v[44:47], v154 offset:2304\n"
    "ds_read_b128 v[140:143], v156 offset:6144\n"
    "ds_read_b128 v[144:147], v156 offset:6400\n"
    "ds_read_b128 v[48:51], v154 offset:3456\n"
    "s_waitcnt lgkmcnt(9)\n"
    "v_mfma_f32_16x16x32_bf16 v[108:111], v[36:39], v[116:119], 0\n"
    "ds_read_b128 v[0:3], v156 offset:8192\n"
    "ds_read_b128 v[4:7], v156 offset:8448\n"
    "ds_read_b128 v[52:55], v154 offset:4608\n"
    "v_mfma_f32_16x16x32_bf16 v[112:115], v[36:39], v[120:123], 0\n"
    "s_waitcnt lgkmcnt(9)\n"
    "v_mfma_f32_16x16x32_bf16 v[108:111], v[40:43], v[124:127], v[108:111]\n"
    "ds_read_b128 v[8:11], v156 offset:10240\n"
    "ds_read_b128 v[12:15], v156 offset:10496\n"
    "ds_read_b128 v[56:59], v154 offset:5760\n"
    "v_mfma_f32_16x16x32_bf16 v[112:115], v[40:43], v[128:131], v[112:115]\n"
    "s_waitcnt lgkmcnt(9)\n"
    "v_mfma_f32_16x16x32_bf16 v[108:111], v[44:47], v[132:135], v[108:111]\n"
    "ds_read_b128 v[16:19], v156 offset:12288\n"
    "ds_read_b128 v[20:23], v156 offset:12544\n"
    "ds_read_b128 v[60:63], v154 offset:6912\n"
    "v_mfma_f32_16x16x32_bf16 v[112:115], v[44:47], v[136:139], v[112:115]\n"
    "s_waitcnt lgkmcnt(9)\n"
    "v_mfma_f32_16x16x32_bf16 v[108:111], v[48:51], v[140:143], v[108:111]\n"
    "ds_read_b128 v[24:27], v156 offset:14336\n"
    "ds_read_b128 v[28:31], v156 offset:14592\n"
    "ds_read_b128 v[64:67], v154 offset:8064\n"
    "v_mfma_f32_16x16x32_bf16 v[112:115], v[48:51], v[144:147], v[112:115]\n"
    "s_waitcnt lgkmcnt(9)\n"
    "v_mfma_f32_16x16x32_bf16 v[108:111], v[52:55], v[0:3], v[108:111]\n"
    "ds_read_b128 v[116:119], v156 offset:16384\n"
    "ds_read_b128 v[120:123], v156 offset:16640\n"
    "ds_read_b128 v[68:71], v154 offset:9216\n"
    "v_mfma_f32_16x16x32_bf16 v[112:115], v[52:55], v[4:7], v[112:115]\n"
    "s_waitcnt lgkmcnt(9)\n"
    "v_mfma_f32_16x16x32_bf16 v[108:111], v[56:59], v[8:11], v[108:111]\n"
    "v_mfma_f32_16x16x32_bf16 v[112:115], v[56:59], v[12:15], v[112:115]\n"
    "s_waitcnt lgkmcnt(6)\n"
    "v_mfma_f32_16x16x32_bf16 v[108:111], v[60:63], v[16:19], v[108:111]\n"
    "v_mfma_f32_16x16x32_bf16 v[112:115], v[60:63], v[20:23], v[112:115]\n"
    "s_waitcnt lgkmcnt(3)\n"
    "v_mfma_f32_16x16x32_bf16 v[108:111], v[64:67], v[24:27], v[108:111]\n"
    "v_mfma_f32_16x16x32_bf16 v[112:115], v[64:67], v[28:31], v[112:115]\n"
    "s_waitcnt lgkmcnt(0)\n"
    "v_mfma_f32_16x16x32_bf16 v[108:111], v[68:71], v[116:119], v[108:111]\n"
    "v_mfma_f32_16x16x32_bf16 v[112:115], v[68:71], v[120:123], v[112:115]\n"
    "s_barrier\n"
    "ds_read_b128 v[124:127], v156 offset:18432\n"
    "ds_read_b128 v[128:131], v156 offset:18688\n"
    "ds_read_b128 v[72:75], v154 offset:10368\n"
    "ds_read_b128 v[132:135], v156 offset:20480\n"
    "ds_read_b128 v[136:139], v156 offset:20736\n"
    "ds_read_b128 v[76:79], v154 offset:11520\n"
    "ds_read_b128 v[140:143], v156 offset:22528\n"
    "ds_read_b128 v[144:147], v156 offset:22784\n"
    "ds_read_b128 v[80:83], v154 offset:12672\n"
    "ds_read_b128 v[0:3], v156 offset:24576\n"
    "ds_read_b128 v[4:7], v156 offset:24832\n"
    "ds_read_b128 v[84:87], v154 offset:13824\n"
    "s_waitcnt lgkmcnt(9)\n"
    "v_mfma_f32_16x16x32_bf16 v[108:111], v[72:75], v[124:127], v[108:111]\n"
    "ds_read_b128 v[8:11], v156 offset:26624\n"
    "ds_read_b128 v[12:15], v156 offset:26880\n"
    "ds_read_b128 v[88:91], v154 offset:14976\n"
    "v_mfma_f32_16x16x32_bf16 v[112:115], v[72:75], v[128:131], v[112:115]\n"
    "s_waitcnt lgkmcnt(9)\n"
    "v_mfma_f32_16x16x32_bf16 v[108:111], v[76:79], v[132:135], v[108:111]\n"
    "ds_read_b128 v[16:19], v156 offset:28672\n"
    "ds_read_b128 v[20:23], v156 offset:28928\n"
    "ds_read_b128 v[92:95], v154 offset:16128\n"
    "v_mfma_f32_16x16x32_bf16 v[112:115], v[76:79], v[136:139], v[112:115]\n"
    "s_waitcnt lgkmcnt(9)\n"
    "v_mfma_f32_16x16x32_bf16 v[108:111], v[80:83], v[140:143], v[108:111]\n"
    "ds_read_b128 v[24:27], v156 offset:30720\n"
    "ds_read_b128 v[28:31], v156 offset:30976\n"
    "ds_read_b128 v[96:99], v154 offset:17280\n"
    "v_mfma_f32_16x16x32_bf16 v[112:115], v[80:83], v[144:147], v[112:115]\n"
    "s_waitcnt lgkmcnt(9)\n"
    "v_mfma_f32_16x16x32_bf16 v[108:111], v[84:87], v[0:3], v[108:111]\n"
    "ds_read_b128 v[116:119], v156 offset:32768\n"
    "ds_read_b128 v[120:123], v156 offset:33024\n"
    "ds_read_b128 v[100:103], v154 offset:18432\n"
    "v_mfma_f32_16x16x32_bf16 v[112:115], v[84:87], v[4:7], v[112:115]\n"
    "s_waitcnt lgkmcnt(9)\n"
    "v_mfma_f32_16x16x32_bf16 v[108:111], v[88:91], v[8:11], v[108:111]\n"
    "ds_read_b128 v[124:127], v156 offset:34816\n"
    "ds_read_b128 v[128:131], v156 offset:35072\n"
    "ds_read_b128 v[104:107], v154 offset:19584\n"
    "v_mfma_f32_16x16x32_bf16 v[112:115], v[88:91], v[12:15], v[112:115]\n"
    "s_waitcnt lgkmcnt(9)\n"
    "v_mfma_f32_16x16x32_bf16 v[108:111], v[92:95], v[16:19], v[108:111]\n"
    "v_mfma_f32_16x16x32_bf16 v[112:115], v[92:95], v[20:23], v[112:115]\n"
    "s_waitcnt lgkmcnt(6)\n"
    "v_mfma_f32_16x16x32_bf16 v[108:111], v[96:99], v[24:27], v[108:111]\n"
    "v_mfma_f32_16x16x32_bf16 v[112:115], v[96:99], v[28:31], v[112:115]\n"
    "s_waitcnt lgkmcnt(3)\n"
    "v_mfma_f32_16x16x32_bf16 v[108:111], v[100:103], v[116:119], v[108:111]\n"
    "v_mfma_f32_16x16x32_bf16 v[112:115], v[100:103], v[120:123], v[112:115]\n"
    "s_waitcnt lgkmcnt(0)\n"
    "v_mfma_f32_16x16x32_bf16 v[108:111], v[104:107], v[124:127], v[108:111]\n"
    "v_mfma_f32_16x16x32_bf16 v[112:115], v[104:107], v[128:131], v[112:115]\n"
    "s_cmp_eq_u32 %4, 5\n"
    "s_cbranch_scc1 .Lb5_%=\n"
    "s_waitcnt vmcnt(0)\n"
    "s_nop 4\n"
    "v_permlane16_swap_b32 v108, v112\n"
    "v_permlane16_swap_b32 v109, v113\n"
    "v_permlane16_swap_b32 v110, v114\n"
    "v_permlane16_swap_b32 v111, v115\n"
    "v_lshlrev_b32 v160, 16, v148\n"
    "v_and_b32 v161, 0xffff0000, v148\n"
    "v_add_f32 v108, v108, v160\n"
    "v_add_f32 v109, v109, v161\n"
    "v_lshlrev_b32 v160, 16, v149\n"
    "v_and_b32 v161, 0xffff0000, v149\n"
    "v_add_f32 v110, v110, v160\n"
    "v_add_f32 v111, v111, v161\n"
    "v_lshlrev_b32 v160, 16, v150\n"
    "v_and_b32 v161, 0xffff0000, v150\n"
    "v_add_f32 v112, v112, v160\n"
    "v_add_f32 v113, v113, v161\n"
    "v_lshlrev_b32 v160, 16, v151\n"
    "v_and_b32 v161, 0xffff0000, v151\n"
    "v_add_f32 v114, v114, v160\n"
    "v_add_f32 v115, v115, v161\n"
    "s_barrier\n"
    "ds_read_b32 v159, v158\n"
    "s_waitcnt lgkmcnt(0)\n"
    "v_add_f32 v162, v162, v159\n"
    "v_sub_f32 v108, v108, v162\n"
    "v_sub_f32 v109, v109, v162\n"
    "v_sub_f32 v110, v110, v162\n"
    "v_sub_f32 v111, v111, v162\n"
    "v_sub_f32 v112, v112, v162\n"
    "v_sub_f32 v113, v113, v162\n"
    "v_sub_f32 v114, v114, v162\n"
    "v_sub_f32 v115, v115, v162\n"
    "v_cvt_pk_bf16_f32 v148, v108, v109\n"
    "v_cvt_pk_bf16_f32 v149, v110, v111\n"
    "v_cvt_pk_bf16_f32 v150, v112, v113\n"
    "v_cvt_pk_bf16_f32 v151, v114, v115\n"
    "buffer_store_dwordx4 v[148:151], v164, s[36:39], %15 offen\n"
    "v_dot2_f32_bf16 v160, v148, v148, 0\n"
    "v_dot2_f32_bf16 v160, v149, v149, v160\n"
    "v_dot2_f32_bf16 v160, v150, v150, v160\n"
    "v_dot2_f32_bf16 v160, v151, v151, v160\n"
    "s_nop 2\n"
    "ds_write_b32 v157, v160\n"
    "s_waitcnt lgkmcnt(0)\n"
    "s_barrier\n"
    "s_branch .Lbe_%=\n"
    ".Lb5_%=:\n"
    "s_nop 7\n"
    "v_add_f32 v108, v108, v109\n"
    "v_add_f32 v112, v112, v113\n"
    "s_mov_b32 vcc_lo, 0xffff0000\n"
    "s_mov_b32 vcc_hi, 0xffff0000\n"
    "v_cndmask_b32 v108, v108, v112, vcc\n"
    "ds_write_b32 v158, v108\n"
    "s_waitcnt lgkmcnt(0)\n"
    "s_barrier\n"
    "s_waitcnt vmcnt(0)\n"
    "v_add_f32 v162, v162, v108\n"
    "s_add_u32 s48, %16, 8192\n"
    "s_cmp_lg_u32 %18, 0\n"
    "s_cbranch_scc1 .Lb5e_%=\n"
    "buffer_store_dword v162, v165, s[36:39], s48 offen\n"
    ".Lb5e_%=:\n"
    "s_barrier\n"
    "s_branch .Lbe_%=\n"
    ".Lb6_%=:\n"
    "s_and_b32 s49, s51, 1024\n"
    "s_add_u32 s49, s49, 84480\n"
    "v_lshrrev_b32 v160, 5, v152\n"
    "v_lshl_add_u32 v155, v160, 9, s49\n"
    "v_and_b32 v160, 7, v152\n"
    "v_lshl_add_u32 v155, v160, 6, v155\n"
    "v_bfe_u32 v160, v152, 3, 2\n"
    "v_lshl_add_u32 v155, v160, 2, v155\n"
    "v_add_u32 v155, 0x4000, v155\n"
    "s_add_u32 s48, %12, 16384\n"
    "s_and_b32 s56, s51, 1024\n"
    "s_mul_i32 s56, s56, 7\n"
    "s_add_u32 s49, s49, s56\n"
    "s_add_u32 s56, s56, %12\n"
    "s_setprio 3\n"
    "s_barrier\n"
    "v_add_u32 v154, s49, v153\n"
    "s_movk_i32 s50, 0x2000\n"
    ".Lopo_%=:\n"
    "s_add_u32 s44, s56, 0\n"
    "buffer_load_dwordx4 v[0:3], v153, s[36:39], s44 offen sc1\n"
    "s_add_u32 s45, s56, 1024\n"
    "buffer_load_dwordx4 v[4:7], v153, s[36:39], s45 offen sc1\n"
    "s_add_u32 s46, s56, 2048\n"
    "buffer_load_dwordx4 v[8:11], v153, s[36:39], s46 offen sc1\n"
    "s_add_u32 s47, s56, 3072\n"
    "buffer_load_dwordx4 v[12:15], v153, s[36:39], s47 offen sc1\n"
    "s_add_u32 s44, s56, 4096\n"
    "buffer_load_dwordx4 v[16:19], v153, s[36:39], s44 offen sc1\n"
    "s_add_u32 s45, s56, 5120\n"
    "buffer_load_dwordx4 v[20:23], v153, s[36:39], s45 offen sc1\n"
    "s_add_u32 s46, s56, 6144\n"
    "buffer_load_dwordx4 v[24:27], v153, s[36:39], s46 offen sc1\n"
    "s_add_u32 s47, s56, 7168\n"
    "buffer_load_dwordx4 v[28:31], v153, s[36:39], s47 offen sc1\n"
    "buffer_load_dwordx4 v[32:35], v153, s[36:39], s48 offen sc1\n"
    "s_waitcnt vmcnt(8)\n"
    "v_max_u32 v159, v0, v1\n"
    "v_max3_u32 v159, v159, v2, v3\n"
    "ds_write_b128 v154, v[0:3] offset:0\n"
    "s_waitcnt vmcnt(7)\n"
    "v_max3_u32 v159, v159, v4, v5\n"
    "v_max3_u32 v159, v159, v6, v7\n"
    "ds_write_b128 v154, v[4:7] offset:1024\n"
    "s_waitcnt vmcnt(6)\n"
    "v_max3_u32 v159, v159, v8, v9\n"
    "v_max3_u32 v159, v159, v10, v11\n"
    "ds_write_b128 v154, v[8:11] offset:2048\n"
    "s_waitcnt vmcnt(5)\n"
    "v_max3_u32 v159, v159, v12, v13\n"
    "v_max3_u32 v159, v159, v14, v15\n"
    "ds_write_b128 v154, v[12:15] offset:3072\n"
    "s_waitcnt vmcnt(4)\n"
    "v_max3_u32 v159, v159, v16, v17\n"
    "v_max3_u32 v159, v159, v18, v19\n"
    "ds_write_b128 v154, v[16:19] offset:4096\n"
    "s_waitcnt vmcnt(3)\n"
    "v_max3_u32 v159, v159, v20, v21\n"
    "v_max3_u32 v159, v159, v22, v23\n"
    "ds_write_b128 v154, v[20:23] offset:5120\n"
    "s_waitcnt vmcnt(2)\n"
    "v_max3_u32 v159, v159, v24, v25\n"
    "v_max3_u32 v159, v159, v26, v27\n"
    "ds_write_b128 v154, v[24:27] offset:6144\n"
    "s_waitcnt vmcnt(1)\n"
    "v_max3_u32 v159, v159, v28, v29\n"
    "v_max3_u32 v159, v159, v30, v31\n"
    "ds_write_b128 v154, v[28:31] offset:7168\n"
    "s_waitcnt vmcnt(0)\n"
    "v_max3_u32 v159, v159, v32, v33\n"
    "v_max3_u32 v159, v159, v34, v35\n"
    "ds_write2_b32 v155, v32, v33 offset0:0 offset1:4\n"
    "ds_write2_b32 v155, v34, v35 offset0:8 offset1:12\n"
    "v_cmp_eq_u32 vcc, -1, v159\n"
    "s_waitcnt lgkmcnt(0)\n"
    "s_sub_u32 s50, s50, 1\n"
    "s_cbranch_scc1 .Loqo_%=\n"
    "s_nop 2\n"
    "s_cbranch_vccz .Loqo_%=\n"
    "s_sleep 2\n"
    "s_branch .Lopo_%=\n"
    ".Loqo_%=:\n"
    "s_barrier\n"
    "v_lshlrev_b32 v159, 2, v152\n"
    "v_add_u32 v159, 63616, v159\n"
    "v_lshlrev_b32 v160, 2, v165\n"
    "s_barrier\n"
    "s_barrier\n"
    "s_cmp_lg_u32 %4, 6\n"
    "s_cbranch_scc1 .Lbe_%=\n"
    "ds_read_b32 v0, v159 offset:0\n"
    "ds_read_b32 v1, v159 offset:256\n"
    "ds_read_b32 v2, v159 offset:512\n"
    "ds_read_b32 v3, v159 offset:768\n"
    "ds_read_b32 v4, v159 offset:1024\n"
    "ds_read_b32 v5, v159 offset:1280\n"
    "ds_read_b32 v6, v159 offset:1536\n"
    "ds_read_b32 v7, v159 offset:1792\n"
    "ds_read_b32 v8, v159 offset:2048\n"
    "s_waitcnt lgkmcnt(0)\n"
    "v_add_f32 v0, v0, v1\n"
    "v_add_f32 v3, v3, v4\n"
    "v_add_f32 v6, v6, v7\n"
    "v_add_f32 v0, v0, v2\n"
    "v_add_f32 v3, v3, v5\n"
    "v_add_f32 v6, v6, v8\n"
    "v_add_f32 v0, v0, v3\n"
    "v_add_f32 v0, v0, v6\n"
    "v_mov_b32 v1, v0\n"
    "s_nop 1\n"
    "v_permlane32_swap_b32 v0, v1\n"
    "v_add_f32 v0, v0, v1\n"
    "buffer_store_dword v0, v160, s[36:39], %17 offen\n"
    ".Lbe_%=:\n"
    ".Le_%=:\n"
        :: "s"((u32)(size_t)ws), "s"((u32)((size_t)ws >> 32)), "s"((u32)(size_t)x), "s"((u32)((size_t)x >> 32)), "s"(w),
           "s"((u32)OFF_KH + head * 65536 + (t0 + qi) * 1024), "s"((u32)OFF_KL + hp * 16384 + t0 * 256 + qi * 1024),
           "s"((u32)OFF_QH + head * 65536 + T * 1024), "s"((u32)OFF_QL + hp * 16384 + T * 256 + hd * 4),
           "s"(T < 8 ? 8 - T : 0u), "s"((u32)OFF_O32 + T * 36864 + hd * 18432 + hp * 1024), "s"((u32)OFF_O32 + T * 36864 + hd * 18432 + 16384 + hp * 128),
           "s"((u32)OFF_O32 + Tp * 36864 + 18432 + (w & 1) * 1024), "s"((u32)OFF_WOT + nu * 147456 + blk * 1024), "s"((32 * Tp * 576 + 144 * nu + (hd ? 128 : 16 * blk)) * 2),
           "s"((u32)OFF_H32 + 36864 * Tp + (hd ? (16 + (nu >> 1)) * 1024 + (nu & 1) * 512 : 4096 * nu + 512 * blk)),
           "s"((u32)OFF_MEAN1 + Tp * 128), "s"((u32)OFF_SSQ + Tp * 512 + nu * 4), "s"(nu), "s"(blk), "s"(0u)
        : "v0", "v1", "v2", "v3", "v4", "v5", "v6", "v7", "v8", "v9", "v10", "v11", "v12", "v13", "v14", "v15", "v16", "v17", "v18", "v19", "v20", "v21", "v22", "v23", "v24", "v25", "v26", "v27", "v28", "v29", "v30", "v31", "v32", "v33", "v34", "v35", "v36", "v37", "v38", "v39", "v40", "v41", "v42", "v43", "v44", "v45", "v46", "v47", "v48", "v49", "v50", "v51", "v52", "v53", "v54", "v55", "v56", "v57", "v58", "v59", "v60", "v61", "v62", "v63", "v64", "v65", "v66", "v67", "v68", "v69", "v70", "v71", "v72", "v73", "v74", "v75", "v76", "v77", "v78", "v79", "v80", "v81", "v82", "v83", "v84", "v85", "v86", "v87", "v88", "v89", "v90", "v91", "v92", "v93", "v94", "v95", "v96", "v97", "v98", "v99", "v100", "v101", "v102", "v103", "v104", "v105", "v106", "v107", "v108", "v109", "v110", "v111", "v112", "v113", "v114", "v115", "v116", "v117", "v118", "v119", "v120", "v121", "v122", "v123", "v124", "v125", "v126", "v127", "v128", "v129", "v130", "v131", "v132", "v133", "v134", "v135", "v136", "v137", "v138", "v139", "v140", "v141", "v142", "v143", "v144", "v145", "v146", "v147", "v148", "v149", "v150", "v151", "v152", "v153", "v154", "v155", "v156", "v157", "v158", "v159", "v160", "v161", "v162", "v163", "v164", "v165", "v166", "v167", "s36", "s37", "s38", "s39", "s40", "s41", "s42", "s43", "s44", "s45", "s46", "s47", "s48", "s49", "s50", "s51", "s52", "s53", "s54", "s55", "s56", "s57", "s58", "s59", "s60", "s61", "s62", "s63", "s64", "s65", "s66", "s67", "s68", "s69", "s70", "s71", "s72", "s73", "s74", "s75", "s76", "s77", "s78", "s79", "s80", "s81", "s82", "s83", "s84", "s85", "s86", "s87", "s88", "s89", "s90", "s91", "s92", "s93", "s94", "s95", "s96", "s97", "s98", "s99", "vcc", "scc", "memory");
    } else {
        const u32 ch0 = 96 + qi * 32 + idx, ch = ch0 < 144 ? ch0 : 0;
        const u64 tch = (u64)(ch & 1 ? wu : wg) + (ch >> 1) * 73728 + (xcd >> 1) * 2304 + (xcd & 1) * 1024;
        asm volatile(
    "s_nop 4\n"
    "s_mov_b32 s36, %0\n"
    "s_and_b32 s37, %1, 0xffff\n"
    "s_mov_b32 s38, 134217728\n"
    "s_mov_b32 s39, 0x20000\n"
    "v_mbcnt_lo_u32_b32 v152, -1, 0\n"
    "v_mbcnt_hi_u32_b32 v152, -1, v152\n"
    "v_lshlrev_b32 v153, 4, v152\n"
    "v_and_b32 v159, 31, v152\n"
    "v_lshrrev_b32 v160, 5, v152\n"
    "s_setprio 3\n"
    "s_lshl_b32 s51, %5, 10\n"
    "s_add_u32 m0, s51, 28672\n"
    "s_add_u32 s44, %6, 0\n"
    "buffer_load_dwordx4 v153, s[36:39], s44 offen lds\n"
    "s_add_u32 m0, s51, 32768\n"
    "s_add_u32 s45, %6, 4096\n"
    "buffer_load_dwordx4 v153, s[36:39], s45 offen lds\n"
    "s_add_u32 m0, s51, 36864\n"
    "s_add_u32 s46, %6, 8192\n"
    "buffer_load_dwordx4 v153, s[36:39], s46 offen lds\n"
    "s_add_u32 m0, s51, 40960\n"
    "s_add_u32 s47, %6, 12288\n"
    "buffer_load_dwordx4 v153, s[36:39], s47 offen lds\n"
    "s_add_u32 m0, s51, 45056\n"
    "s_add_u32 s44, %6, 77824\n"
    "buffer_load_dwordx4 v153, s[36:39], s44 offen lds\n"
    "s_add_u32 m0, s51, 49152\n"
    "s_add_u32 s45, %6, 81920\n"
    "buffer_load_dwordx4 v153, s[36:39], s45 offen lds\n"
    "s_add_u32 m0, s51, 53248\n"
    "s_add_u32 s46, %6, 86016\n"
    "buffer_load_dwordx4 v153, s[36:39], s46 offen lds\n"
    "s_add_u32 m0, s51, 57344\n"
    "s_add_u32 s47, %6, 90112\n"
    "buffer_load_dwordx4 v153, s[36:39], s47 offen lds\n"
    "v_lshlrev_b32 v165, 2, v159\n"
    "s_barrier\n"
    "v_mov_b32 v8, 0xffff\n"
    "v_mov_b32 v9, -1\n"
    "s_mov_b32 vcc_lo, 0xffffffff\n"
    "s_mov_b32 vcc_hi, 0xfffffff0\n"
    "v_cndmask_b32 v0, 0, v8, vcc\n"
    "s_mov_b32 vcc_lo, 0xfffffffe\n"
    "s_mov_b32 vcc_hi, 0xffffffe0\n"
    "v_cndmask_b32 v0, v0, v9, vcc\n"
    "s_mov_b32 vcc_lo, 0xfffffffc\n"
    "s_mov_b32 vcc_hi, 0xffffffc0\n"
    "v_cndmask_b32 v1, 0, v8, vcc\n"
    "s_mov_b32 vcc_lo, 0xfffffff8\n"
    "s_mov_b32 vcc_hi, 0xffffff80\n"
    "v_cndmask_b32 v1, v1, v9, vcc\n"
    "s_mov_b32 vcc_lo, 0xffffff00\n"
    "s_mov_b32 vcc_hi, 0xfffff000\n"
    "v_cndmask_b32 v2, 0, v8, vcc\n"
    "s_mov_b32 vcc_lo, 0xfffffe00\n"
    "s_mov_b32 vcc_hi, 0xffffe000\n"
    "v_cndmask_b32 v2, v2, v9, vcc\n"
    "s_mov_b32 vcc_lo, 0xfffffc00\n"
    "s_mov_b32 vcc_hi, 0xffffc000\n"
    "v_cndmask_b32 v3, 0, v8, vcc\n"
    "s_mov_b32 vcc_lo, 0xfffff800\n"
    "s_mov_b32 vcc_hi, 0xffff8000\n"
    "v_cndmask_b32 v3, v3, v9, vcc\n"
    "s_mov_b32 vcc_lo, 0xffff0000\n"
    "s_mov_b32 vcc_hi, 0xfff00000\n"
    "v_cndmask_b32 v4, 0, v8, vcc\n"
    "s_mov_b32 vcc_lo, 0xfffe0000\n"
    "s_mov_b32 vcc_hi, 0xffe00000\n"
    "v_cndmask_b32 v4, v4, v9, vcc\n"
    "s_mov_b32 vcc_lo, 0xfffc0000\n"
    "s_mov_b32 vcc_hi, 0xffc00000\n"
    "v_cndmask_b32 v5, 0, v8, vcc\n"
    "s_mov_b32 vcc_lo, 0xfff80000\n"
    "s_mov_b32 vcc_hi, 0xff800000\n"
    "v_cndmask_b32 v5, v5, v9, vcc\n"
    "s_mov_b32 vcc_lo, 0xff000000\n"
    "s_mov_b32 vcc_hi, 0xf0000000\n"
    "v_cndmask_b32 v6, 0, v8, vcc\n"
    "s_mov_b32 vcc_lo, 0xfe000000\n"
    "s_mov_b32 vcc_hi, 0xe0000000\n"
    "v_cndmask_b32 v6, v6, v9, vcc\n"
    "s_mov_b32 vcc_lo, 0xfc000000\n"
    "s_mov_b32 vcc_hi, 0xc0000000\n"
    "v_cndmask_b32 v7, 0, v8, vcc\n"
    "s_mov_b32 vcc_lo, 0xf8000000\n"
    "s_mov_b32 vcc_hi, 0x80000000\n"
    "v_cndmask_b32 v7, v7, v9, vcc\n"
    "ds_write_b128 v153, v[0:3] offset:61440\n"
    "ds_write_b128 v153, v[4:7] offset:62464\n"
    "v_mov_b32 v8, 65984\n"
    "v_mov_b32 v9, 0\n"
    "ds_write_b32 v8, v9\n"
    "s_waitcnt vmcnt(0) lgkmcnt(0)\n"
    "s_cmp_ge_u32 %13, 144\n"
    "s_cbranch_scc1 .Ltc_%=\n"
    "v_lshrrev_b32 v161, 3, v152\n"
    "v_lshlrev_b32 v161, 13, v161\n"
    "v_lshl_add_u32 v161, v152, 7, v161\n"
    "global_load_dword v167, v161, %12\n"
    "s_nop 2\n"
    ".Ltc_%=:\n"
    "s_barrier\n"
    "s_add_u32 s44, %7, 0\n"
    "buffer_load_dwordx4 v[36:39], v153, s[36:39], s44 offen\n"
    "s_add_u32 s45, %7, 8192\n"
    "buffer_load_dwordx4 v[40:43], v153, s[36:39], s45 offen\n"
    "s_add_u32 s46, %7, 16384\n"
    "buffer_load_dwordx4 v[44:47], v153, s[36:39], s46 offen\n"
    "s_add_u32 s47, %7, 24576\n"
    "buffer_load_dwordx4 v[48:51], v153, s[36:39], s47 offen\n"
    "s_add_u32 s44, %7, 32768\n"
    "buffer_load_dwordx4 v[52:55], v153, s[36:39], s44 offen\n"
    "s_add_u32 s45, %7, 40960\n"
    "buffer_load_dwordx4 v[56:59], v153, s[36:39], s45 offen\n"
    "s_add_u32 s46, %7, 49152\n"
    "buffer_load_dwordx4 v[60:63], v153, s[36:39], s46 offen\n"
    "s_add_u32 s47, %7, 57344\n"
    "buffer_load_dwordx4 v[64:67], v153, s[36:39], s47 offen\n"
    "s_add_u32 s44, %7, 65536\n"
    "buffer_load_dwordx4 v[68:71], v153, s[36:39], s44 offen\n"
    "s_add_u32 s45, %7, 73728\n"
    "buffer_load_dwordx4 v[72:75], v153, s[36:39], s45 offen\n"
    "s_add_u32 s46, %7, 81920\n"
    "buffer_load_dwordx4 v[76:79], v153, s[36:39], s46 offen\n"
    "s_add_u32 s47, %7, 90112\n"
    "buffer_load_dwordx4 v[80:83], v153, s[36:39], s47 offen\n"
    "s_add_u32 s44, %7, 98304\n"
    "buffer_load_dwordx4 v[84:87], v153, s[36:39], s44 offen\n"
    "s_add_u32 s45, %7, 106496\n"
    "buffer_load_dwordx4 v[88:91], v153, s[36:39], s45 offen\n"
    "s_add_u32 s46, %7, 114688\n"
    "buffer_load_dwordx4 v[92:95], v153, s[36:39], s46 offen\n"
    "s_add_u32 s47, %7, 122880\n"
    "buffer_load_dwordx4 v[96:99], v153, s[36:39], s47 offen\n"
    "s_add_u32 s44, %7, 131072\n"
    "buffer_load_dwordx4 v[100:103], v153, s[36:39], s44 offen\n"
    "s_add_u32 s45, %7, 139264\n"
    "buffer_load_dwordx4 v[104:107], v153, s[36:39], s45 offen\n"
    "s_add_u32 m0, s51, 102912\n"
    "s_add_u32 s46, %16, 0\n"
    "buffer_load_dwordx4 v153, s[36:39], s46 offen lds\n"
    "s_add_u32 m0, s51, 107008\n"
    "s_add_u32 s47, %16, 4096\n"
    "buffer_load_dwordx4 v153, s[36:39], s47 offen lds\n"
    "s_add_u32 m0, s51, 111104\n"
    "s_add_u32 s44, %16, 8192\n"
    "buffer_load_dwordx4 v153, s[36:39], s44 offen lds\n"
    "s_add_u32 m0, s51, 115200\n"
    "s_add_u32 s45, %16, 12288\n"
    "buffer_load_dwordx4 v153, s[36:39], s45 offen lds\n"
    "s_add_u32 m0, s51, 119296\n"
    "s_add_u32 s46, %16, 16384\n"
    "buffer_load_dwordx4 v153, s[36:39], s46 offen lds\n"
    "s_sub_u32 s49, %16, s51\n"
    "s_add_u32 m0, 0, 123392\n"
    "s_add_u32 s47, s49, 20480\n"
    "buffer_load_dwordx4 v153, s[36:39], s47 offen lds\n"
    "s_cmp_gt_u32 %14, ffn_code_end-ffn_code_begin+760\n"
    "s_cbranch_scc1 .Lcp_%=\n"
    "s_getpc_b64 s[56:57]\n"
    ".Lpc_%=:\n"
    "s_add_u32 s56, s56, ffn_code_begin-768-.Lpc_%=\n"
    "s_addc_u32 s57, s57, (ffn_code_begin-768-.Lpc_%=)>>32\n"
    "v_lshl_add_u32 v161, v152, 7, %14\n"
    "v_min_u32 v161, ffn_code_end-ffn_code_begin+760, v161\n"
    "global_load_dword v166, v161, s[56:57]\n"
    "s_nop 2\n"
    ".Lcp_%=:\n"
    "s_setprio 0\n"
    "s_mov_b32 s40, %2\n"
    "s_and_b32 s41, %3, 0xffff\n"
    "s_mov_b32 s42, 2359296\n"
    "s_mov_b32 s43, 0x20000\n"
    "v_and_b32 v159, 31, v152\n"
    "v_lshrrev_b32 v160, 5, v152\n"
    "v_mul_u32_u24 v163, 1152, v159\n"
    "v_lshl_add_u32 v163, v160, 4, v163\n"
    "buffer_load_dwordx4 v[148:151], v163, s[40:43], %8 offen\n"
    "buffer_load_dword v162, v165, s[36:39], %10 offen\n"
    "v_bfe_u32 v164, v152, 4, 1\n"
    "v_mul_u32_u24 v164, 18432, v164\n"
    "v_lshl_add_u32 v164, v160, 8, v164\n"
    "v_and_b32 v161, 15, v152\n"
    "v_lshl_add_u32 v164, v161, 4, v164\n"
    "v_lshrrev_b32 v160, 4, v152\n"
    "v_lshlrev_b32 v156, 9, v160\n"
    "v_lshl_add_u32 v156, v161, 4, v156\n"
    "v_add_u32 v156, 66048, v156\n"
    "s_lshl_b32 s49, %5, 8\n"
    "s_add_u32 s49, s49, 63616\n"
    "v_lshl_add_u32 v157, v152, 2, s49\n"
    "v_add_u32 v158, 63488, v165\n"
    "s_add_u32 s49, s51, 66048\n"
    "s_lshr_b32 s48, s51, 1\n"
    "s_add_u32 s56, s48, 82432\n"
    "v_and_b32 v160, 15, v152\n"
    "v_lshl_add_u32 v155, v160, 5, s56\n"
    "v_lshrrev_b32 v160, 4, v152\n"
    "v_lshl_add_u32 v155, v160, 2, v155\n"
    "v_lshlrev_b32 v161, 3, v152\n"
    "s_sub_u32 s48, %15, s48\n"
    "s_add_u32 s48, s48, 16384\n"
    "v_mov_b32 v0, 65984\n"
    "s_movk_i32 s50, 0x4000\n"
    ".Lw_%=:\n"
    "s_sleep 1\n"
    "ds_read_b32 v1, v0\n"
    "s_waitcnt lgkmcnt(0)\n"
    "v_readfirstlane_b32 s56, v1\n"
    "s_sub_u32 s50, s50, 1\n"
    "s_cbranch_scc1 .Lwd_%=\n"
    "s_cmp_lt_u32 s56, 1\n"
    "s_cbranch_scc1 .Lw_%=\n"
    ".Lwd_%=:\n"
    "s_setprio 3\n"
    "v_add_u32 v154, s49, v153\n"
    "s_movk_i32 s50, 0x2000\n"
    ".Lope_%=:\n"
    "s_add_u32 s44, %15, 0\n"
    "buffer_load_dwordx4 v[0:3], v153, s[36:39], s44 offen sc1\n"
    "s_add_u32 s45, %15, 4096\n"
    "buffer_load_dwordx4 v[4:7], v153, s[36:39], s45 offen sc1\n"
    "s_add_u32 s46, %15, 8192\n"
    "buffer_load_dwordx4 v[8:11], v153, s[36:39], s46 offen sc1\n"
    "s_add_u32 s47, %15, 12288\n"
    "buffer_load_dwordx4 v[12:15], v153, s[36:39], s47 offen sc1\n"
    "buffer_load_dwordx2 v[16:17], v161, s[36:39], s48 offen sc1\n"
    "s_waitcnt vmcnt(4)\n"
    "v_max_u32 v159, v0, v1\n"
    "v_max3_u32 v159, v159, v2, v3\n"
    "ds_write_b128 v154, v[0:3] offset:0\n"
    "s_waitcnt vmcnt(3)\n"
    "v_max3_u32 v159, v159, v4, v5\n"
    "v_max3_u32 v159, v159, v6, v7\n"
    "ds_write_b128 v154, v[4:7] offset:4096\n"
    "s_waitcnt vmcnt(2)\n"
    "v_max3_u32 v159, v159, v8, v9\n"
    "v_max3_u32 v159, v159, v10, v11\n"
    "ds_write_b128 v154, v[8:11] offset:8192\n"
    "s_waitcnt vmcnt(1)\n"
    "v_max3_u32 v159, v159, v12, v13\n"
    "v_max3_u32 v159, v159, v14, v15\n"
    "ds_write_b128 v154, v[12:15] offset:12288\n"
    "s_waitcnt vmcnt(0)\n"
    "v_max3_u32 v159, v159, v16, v17\n"
    "ds_write2_b32 v155, v16, v17 offset0:0 offset1:4\n"
    "v_cmp_eq_u32 vcc, -1, v159\n"
    "s_waitcnt lgkmcnt(0)\n"
    "s_sub_u32 s50, s50, 1\n"
    "s_cbranch_scc1 .Loqe_%=\n"
    "s_nop 2\n"
    "s_cbranch_vccz .Loqe_%=\n"
    "s_sleep 2\n"
    "s_branch .Lope_%=\n"
    ".Loqe_%=:\n"
    "s_setprio 0\n"
    "s_barrier\n"
    "ds_read_b128 v[116:119], v156 offset:0\n"
    "ds_read_b128 v[120:123], v156 offset:256\n"
    "ds_read_b128 v[124:127], v156 offset:2048\n"
    "ds_read_b128 v[128:131], v156 offset:2304\n"
    "ds_read_b128 v[132:135], v156 offset:4096\n"
    "ds_read_b128 v[136:139], v156 offset:4352\n"
    "ds_read_b128 v[140:143], v156 offset:6144\n"
    "ds_read_b128 v[144:147], v156 offset:6400\n"
    "ds_read_b128 v[0:3], v156 offset:8192\n"
    "ds_read_b128 v[4:7], v156 offset:8448\n"
    "ds_read_b128 v[8:11], v156 offset:10240\n"
    "ds_read_b128 v[12:15], v156 offset:10496\n"
    "s_waitcnt lgkmcnt(10)\n"
    "v_mfma_f32_16x16x32_bf16 v[108:111], v[36:39], v[116:119], 0\n"
    "ds_read_b128 v[16:19], v156 offset:12288\n"
    "ds_read_b128 v[20:23], v156 offset:12544\n"
    "v_mfma_f32_16x16x32_bf16 v[112:115], v[36:39], v[120:123], 0\n"
    "s_waitcnt lgkmcnt(10)\n"
    "v_mfma_f32_16x16x32_bf16 v[108:111], v[40:43], v[124:127], v[108:111]\n"
    "ds_read_b128 v[24:27], v156 offset:14336\n"
    "ds_read_b128 v[28:31], v156 offset:14592\n"
    "v_mfma_f32_16x16x32_bf16 v[112:115], v[40:43], v[128:131], v[112:115]\n"
    "s_waitcnt lgkmcnt(10)\n"
    "v_mfma_f32_16x16x32_bf16 v[108:111], v[44:47], v[132:135], v[108:111]\n"
    "ds_read_b128 v[116:119], v156 offset:16384\n"
    "ds_read_b128 v[120:123], v156 offset:16640\n"
    "v_mfma_f32_16x16x32_bf16 v[112:115], v[44:47], v[136:139], v[112:115]\n"
    "s_waitcnt lgkmcnt(10)\n"
    "v_mfma_f32_16x16x32_bf16 v[108:111], v[48:51], v[140:143], v[108:111]\n"
    "v_mfma_f32_16x16x32_bf16 v[112:115], v[48:51], v[144:147], v[112:115]\n"
    "s_waitcnt lgkmcnt(8)\n"
    "v_mfma_f32_16x16x32_bf16 v[108:111], v[52:55], v[0:3], v[108:111]\n"
    "v_mfma_f32_16x16x32_bf16 v[112:115], v[52:55], v[4:7], v[112:115]\n"
    "s_waitcnt lgkmcnt(6)\n"
    "v_mfma_f32_16x16x32_bf16 v[108:111], v[56:59], v[8:11], v[108:111]\n"
    "v_mfma_f32_16x16x32_bf16 v[112:115], v[56:59], v[12:15], v[112:115]\n"
    "s_waitcnt lgkmcnt(4)\n"
    "v_mfma_f32_16x16x32_bf16 v[108:111], v[60:63], v[16:19], v[108:111]\n"
    "v_mfma_f32_16x16x32_bf16 v[112:115], v[60:63], v[20:23], v[112:115]\n"
    "s_waitcnt lgkmcnt(2)\n"
    "v_mfma_f32_16x16x32_bf16 v[108:111], v[64:67], v[24:27], v[108:111]\n"
    "v_mfma_f32_16x16x32_bf16 v[112:115], v[64:67], v[28:31], v[112:115]\n"
    "s_waitcnt lgkmcnt(0)\n"
    "v_mfma_f32_16x16x32_bf16 v[108:111], v[68:71], v[116:119], v[108:111]\n"
    "v_mfma_f32_16x16x32_bf16 v[112:115], v[68:71], v[120:123], v[112:115]\n"
    "s_barrier\n"
    "ds_read_b128 v[124:127], v156 offset:18432\n"
    "ds_read_b128 v[128:131], v156 offset:18688\n"
    "ds_read_b128 v[132:135], v156 offset:20480\n"
    "ds_read_b128 v[136:139], v156 offset:20736\n"
    "ds_read_b128 v[140:143], v156 offset:22528\n"
    "ds_read_b128 v[144:147], v156 offset:22784\n"
    "ds_read_b128 v[0:3], v156 offset:24576\n"
    "ds_read_b128 v[4:7], v156 offset:24832\n"
    "ds_read_b128 v[8:11], v156 offset:26624\n"
    "ds_read_b128 v[12:15], v156 offset:26880\n"
    "ds_read_b128 v[16:19], v156 offset:28672\n"
    "ds_read_b128 v[20:23], v156 offset:28928\n"
    "s_waitcnt lgkmcnt(10)\n"
    "v_mfma_f32_16x16x32_bf16 v[108:111], v[72:75], v[124:127], v[108:111]\n"
    "ds_read_b128 v[24:27], v156 offset:30720\n"
    "ds_read_b128 v[28:31], v156 offset:30976\n"
    "v_mfma_f32_16x16x32_bf16 v[112:115], v[72:75], v[128:131], v[112:115]\n"
    "s_waitcnt lgkmcnt(10)\n"
    "v_mfma_f32_16x16x32_bf16 v[108:111], v[76:79], v[132:135], v[108:111]\n"
    "ds_read_b128 v[116:119], v156 offset:32768\n"
    "ds_read_b128 v[120:123], v156 offset:33024\n"
    "v_mfma_f32_16x16x32_bf16 v[112:115], v[76:79], v[136:139], v[112:115]\n"
    "s_waitcnt lgkmcnt(10)\n"
    "v_mfma_f32_16x16x32_bf16 v[108:111], v[80:83], v[140:143], v[108:111]\n"
    "ds_read_b128 v[124:127], v156 offset:34816\n"
    "ds_read_b128 v[128:131], v156 offset:35072\n"
    "v_mfma_f32_16x16x32_bf16 v[112:115], v[80:83], v[144:147], v[112:115]\n"
    "s_waitcnt lgkmcnt(10)\n"
    "v_mfma_f32_16x16x32_bf16 v[108:111], v[84:87], v[0:3], v[108:111]\n"
    "v_mfma_f32_16x16x32_bf16 v[112:115], v[84:87], v[4:7], v[112:115]\n"
    "s_waitcnt lgkmcnt(8)\n"
    "v_mfma_f32_16x16x32_bf16 v[108:111], v[88:91], v[8:11], v[108:111]\n"
    "v_mfma_f32_16x16x32_bf16 v[112:115], v[88:91], v[12:15], v[112:115]\n"
    "s_waitcnt lgkmcnt(6)\n"
    "v_mfma_f32_16x16x32_bf16 v[108:111], v[92:95], v[16:19], v[108:111]\n"
    "v_mfma_f32_16x16x32_bf16 v[112:115], v[92:95], v[20:23], v[112:115]\n"
    "s_waitcnt lgkmcnt(4)\n"
    "v_mfma_f32_16x16x32_bf16 v[108:111], v[96:99], v[24:27], v[108:111]\n"
    "v_mfma_f32_16x16x32_bf16 v[112:115], v[96:99], v[28:31], v[112:115]\n"
    "s_waitcnt lgkmcnt(2)\n"
    "v_mfma_f32_16x16x32_bf16 v[108:111], v[100:103], v[116:119], v[108:111]\n"
    "v_mfma_f32_16x16x32_bf16 v[112:115], v[100:103], v[120:123], v[112:115]\n"
    "s_waitcnt lgkmcnt(0)\n"
    "v_mfma_f32_16x16x32_bf16 v[108:111], v[104:107], v[124:127], v[108:111]\n"
    "v_mfma_f32_16x16x32_bf16 v[112:115], v[104:107], v[128:131], v[112:115]\n"
    "s_nop 7\n"
    "v_permlane16_swap_b32 v108, v112\n"
    "v_permlane16_swap_b32 v109, v113\n"
    "v_permlane16_swap_b32 v110, v114\n"
    "v_permlane16_swap_b32 v111, v115\n"
    "v_lshlrev_b32 v160, 16, v148\n"
    "v_and_b32 v161, 0xffff0000, v148\n"
    "v_add_f32 v108, v108, v160\n"
    "v_add_f32 v109, v109, v161\n"
    "v_lshlrev_b32 v160, 16, v149\n"
    "v_and_b32 v161, 0xffff0000, v149\n"
    "v_add_f32 v110, v110, v160\n"
    "v_add_f32 v111, v111, v161\n"
    "v_lshlrev_b32 v160, 16, v150\n"
    "v_and_b32 v161, 0xffff0000, v150\n"
    "v_add_f32 v112, v112, v160\n"
    "v_add_f32 v113, v113, v161\n"
    "v_lshlrev_b32 v160, 16, v151\n"
    "v_and_b32 v161, 0xffff0000, v151\n"
    "v_add_f32 v114, v114, v160\n"
    "v_add_f32 v115, v115, v161\n"
    "s_barrier\n"
    "ds_read_b32 v159, v158\n"
    "s_waitcnt lgkmcnt(0)\n"
    "v_add_f32 v162, v162, v159\n"
    "v_sub_f32 v108, v108, v162\n"
    "v_sub_f32 v109, v109, v162\n"
    "v_sub_f32 v110, v110, v162\n"
    "v_sub_f32 v111, v111, v162\n"
    "v_sub_f32 v112, v112, v162\n"
    "v_sub_f32 v113, v113, v162\n"
    "v_sub_f32 v114, v114, v162\n"
    "v_sub_f32 v115, v115, v162\n"
    "v_cvt_pk_bf16_f32 v148, v108, v109\n"
    "v_cvt_pk_bf16_f32 v149, v110, v111\n"
    "v_cvt_pk_bf16_f32 v150, v112, v113\n"
    "v_cvt_pk_bf16_f32 v151, v114, v115\n"
    "buffer_store_dwordx4 v[148:151], v164, s[36:39], %9 offen\n"
    "v_dot2_f32_bf16 v160, v148, v148, 0\n"
    "v_dot2_f32_bf16 v160, v149, v149, v160\n"
    "v_dot2_f32_bf16 v160, v150, v150, v160\n"
    "v_dot2_f32_bf16 v160, v151, v151, v160\n"
    "s_nop 2\n"
    "ds_write_b32 v157, v160\n"
    "s_waitcnt lgkmcnt(0)\n"
    "s_barrier\n"
    "att_code_end:\n"
        :: "s"((u32)(size_t)ws), "s"((u32)((size_t)ws >> 32)), "s"((u32)(size_t)x), "s"((u32)((size_t)x >> 32)), "s"(w), "s"(qi),
           "s"((u32)OFF_VT + hp * 155648 + t0 * 1216 + qi * 1024),
           "s"((u32)OFF_WOT + nu * 147456 + qi * 1024), "s"((32 * Tp * 576 + 144 * nu + 16 * qi) * 2), "s"((u32)OFF_H32 + 36864 * Tp + 4096 * nu + 512 * qi),
           "s"((u32)OFF_MEAN1 + Tp * 128), "s"(0u),
           "s"(rfl64(tch)), "s"(ch0),
           "s"((4 * idx + qi) * 8192), "s"((u32)OFF_O32 + Tp * 36864 + qi * 1024), "s"((u32)OFF_WB + nu * 21504 + qi * 1024)
        : "v0", "v1", "v2", "v3", "v4", "v5", "v6", "v7", "v8", "v9", "v10", "v11", "v12", "v13", "v14", "v15", "v16", "v17", "v18", "v19", "v20", "v21", "v22", "v23", "v24", "v25", "v26", "v27", "v28", "v29", "v30", "v31", "v32", "v33", "v34", "v35", "v36", "v37", "v38", "v39", "v40", "v41", "v42", "v43", "v44", "v45", "v46", "v47", "v48", "v49", "v50", "v51", "v52", "v53", "v54", "v55", "v56", "v57", "v58", "v59", "v60", "v61", "v62", "v63", "v64", "v65", "v66", "v67", "v68", "v69", "v70", "v71", "v72", "v73", "v74", "v75", "v76", "v77", "v78", "v79", "v80", "v81", "v82", "v83", "v84", "v85", "v86", "v87", "v88", "v89", "v90", "v91", "v92", "v93", "v94", "v95", "v96", "v97", "v98", "v99", "v100", "v101", "v102", "v103", "v104", "v105", "v106", "v107", "v108", "v109", "v110", "v111", "v112", "v113", "v114", "v115", "v116", "v117", "v118", "v119", "v120", "v121", "v122", "v123", "v124", "v125", "v126", "v127", "v128", "v129", "v130", "v131", "v132", "v133", "v134", "v135", "v136", "v137", "v138", "v139", "v140", "v141", "v142", "v143", "v144", "v145", "v146", "v147", "v148", "v149", "v150", "v151", "v152", "v153", "v154", "v155", "v156", "v157", "v158", "v159", "v160", "v161", "v162", "v163", "v164", "v165", "v166", "v167", "s36", "s37", "s38", "s39", "s40", "s41", "s42", "s43", "s44", "s45", "s46", "s47", "s48", "s49", "s50", "s51", "s52", "s53", "s54", "s55", "s56", "s57", "s58", "s59", "s60", "s61", "s62", "s63", "s64", "s65", "s66", "s67", "s68", "s69", "s70", "s71", "s72", "s73", "s74", "s75", "s76", "s77", "s78", "s79", "s80", "s81", "s82", "s83", "s84", "s85", "s86", "s87", "s88", "s89", "s90", "s91", "s92", "s93", "s94", "s95", "s96", "s97", "s98", "s99", "vcc", "scc", "memory");
    }
}

__global__ __launch_bounds__(256, 1) void ffn_kernel(const u16* __restrict__ wg, const u16* __restrict__ wu, char* ws, u16* out) {
    const u32 w = __builtin_amdgcn_readfirstlane(threadIdx.x >> 6);
    const u32 b = blockIdx.x, xcd = b & 7, idx = b >> 3, j = xcd >> 1, rt = idx & 7, c = 4 * (xcd & 1) + (idx >> 3), ct = 8 * j + c;
    const u32 mu = 2 * rt + (c >> 2), nu = c & 3, mh = w & 1, kh = w >> 1, rb0 = 16 * rt + 4 * w, rbd = 8 * mu + 4 * mh, mg = 2 * mh + kh;
    const u32 rbk = rbd + 2 * kh, rbs = rbd + 2 - 2 * kh;
    const u32 grp = (mu * 4 + nu) * 4;
    u32 po[3];
#pragma unroll
    for (u32 p = 0; p < 3; ++p) po[p] = (u32)OFF_PART + ((grp + ((j + 1 + p) & 3)) * 3 + p) * 8192 + w * 2048;
    const u32 row0 = 16 * rbk;
    asm volatile(
    "ffn_code_begin:\n"
    "s_nop 4\n"
    "s_mov_b64 s[36:37], %0\n"
    "s_and_b32 s37, s37, 0xffff\n"
    "s_mov_b32 s38, 134217728\n"
    "s_mov_b32 s39, 0x20000\n"
    "s_mov_b64 s[92:93], %2\n"
    "s_and_b32 s93, s93, 0xffff\n"
    "s_mov_b32 s94, 5308416\n"
    "s_mov_b32 s95, 0x20000\n"
    "s_mov_b64 s[96:97], %3\n"
    "s_and_b32 s97, s97, 0xffff\n"
    "s_mov_b32 s98, 5308416\n"
    "s_mov_b32 s99, 0x20000\n"
    "v_mbcnt_lo_u32_b32 v248, -1, 0\n"
    "v_mbcnt_hi_u32_b32 v248, -1, v248\n"
    "v_lshlrev_b32 v232, 4, v248\n"
    "s_lshl_b32 s50, %7, 10\n"
    "s_mul_i32 s56, %7, 1056\n"
    "v_bfe_u32 v249, v248, 3, 1\n"
    "v_bfe_u32 v245, v248, 4, 1\n"
    "v_lshl_add_u32 v249, v249, 1, v245\n"
    "v_lshrrev_b32 v245, 5, v248\n"
    "v_lshl_add_u32 v249, v245, 2, v249\n"
    "v_mul_u32_u24 v249, 36864, v249\n"
    "v_and_b32 v219, 0x70, v232\n"
    "v_add_u32 v219, v219, v249\n"
    "v_mul_u32_u24 v245, 1179648, v245\n"
    "v_add_u32 v220, v219, v245\n"
    "s_add_u32 m0, s56, 0\n"
    "s_add_u32 s44, %4, 0\n"
    "buffer_load_dwordx4 v219, s[92:95], s44 offen lds\n"
    "s_add_u32 m0, s56, 4224\n"
    "s_add_u32 s45, %4, 294912\n"
    "buffer_load_dwordx4 v219, s[92:95], s45 offen lds\n"
    "s_add_u32 m0, s56, 8448\n"
    "s_add_u32 s46, %4, 589824\n"
    "buffer_load_dwordx4 v219, s[92:95], s46 offen lds\n"
    "s_add_u32 s47, %8, 0\n"
    "buffer_load_dwordx4 a[0:3], v232, s[36:39], s47 offen offset:0\n"
    "buffer_load_dwordx4 a[16:19], v232, s[36:39], s47 offen offset:1024\n"
    "buffer_load_dwordx4 a[32:35], v232, s[36:39], s47 offen offset:2048\n"
    "s_add_u32 s48, %8, 18432\n"
    "buffer_load_dwordx4 a[4:7], v232, s[36:39], s48 offen offset:0\n"
    "buffer_load_dwordx4 a[20:23], v232, s[36:39], s48 offen offset:1024\n"
    "buffer_load_dwordx4 a[36:39], v232, s[36:39], s48 offen offset:2048\n"
    "s_add_u32 s49, %8, 36864\n"
    "buffer_load_dwordx4 a[8:11], v232, s[36:39], s49 offen offset:0\n"
    "buffer_load_dwordx4 a[24:27], v232, s[36:39], s49 offen offset:1024\n"
    "buffer_load_dwordx4 a[40:43], v232, s[36:39], s49 offen offset:2048\n"
    "s_add_u32 s44, %8, 55296\n"
    "buffer_load_dwordx4 a[12:15], v232, s[36:39], s44 offen offset:0\n"
    "buffer_load_dwordx4 a[28:31], v232, s[36:39], s44 offen offset:1024\n"
    "buffer_load_dwordx4 a[44:47], v232, s[36:39], s44 offen offset:2048\n"
    "s_add_u32 m0, s56, 12672\n"
    "s_add_u32 s45, %4, 0\n"
    "buffer_load_dwordx4 v219, s[96:99], s45 offen lds\n"
    "s_add_u32 m0, s56, 16896\n"
    "s_add_u32 s46, %4, 294912\n"
    "buffer_load_dwordx4 v219, s[96:99], s46 offen lds\n"
    "s_add_u32 m0, s56, 21120\n"
    "s_add_u32 s47, %4, 589824\n"
    "buffer_load_dwordx4 v219, s[96:99], s47 offen lds\n"
    "v_and_b32 v249, 0xf0, v232\n"
    "buffer_load_dwordx4 v[160:163], v249, s[36:39], %9 offen offset:0\n"
    "buffer_load_dwordx4 v[164:167], v249, s[36:39], %9 offen offset:256\n"
    "buffer_load_dwordx4 v[168:171], v249, s[36:39], %9 offen offset:512\n"
    "buffer_load_dwordx4 v[172:175], v249, s[36:39], %9 offen offset:768\n"
    "s_mov_b64 s[40:41], %1\n"
    "s_and_b32 s41, s41, 0xffff\n"
    "s_mov_b32 s42, 2359296\n"
    "s_mov_b32 s43, 0x20000\n"
    "v_bfe_u32 v216, v248, 2, 2\n"
    "v_mul_u32_u24 v216, 1056, v216\n"
    "v_bfe_u32 v217, v248, 4, 1\n"
    "v_lshl_add_u32 v216, v217, 7, v216\n"
    "v_lshrrev_b32 v217, 5, v248\n"
    "v_lshl_add_u32 v216, v217, 9, v216\n"
    "v_and_b32 v217, 3, v248\n"
    "v_lshl_add_u32 v216, v217, 3, v216\n"
    "v_add_u32 v217, 0x10000, v216\n"
    "v_add_u32 v218, 0x20000, v216\n"
    "v_mov_b32 v233, v232\n"
    "v_add_u32 v234, 0x10000, v232\n"
    "v_add_u32 v235, 0x20000, v232\n"
    "v_lshrrev_b32 v244, 1, v232\n"
    "v_lshlrev_b32 v250, 3, v232\n"
    "v_bfe_u32 v245, v248, 4, 1\n"
    "s_mov_b32 s51, 147456\n"
    "v_mul_lo_u32 v245, s51, v245\n"
    "v_add_u32 v251, v249, v245\n"
    "v_and_b32 v245, 32, v248\n"
    "v_lshl_add_u32 v251, v245, 3, v251\n"
    "s_lshl_b32 s51, %7, 11\n"
    "s_xor_b32 s52, s51, 0x1000\n"
    "s_add_u32 s51, s51, 76032\n"
    "s_add_u32 s52, s52, 76032\n"
    "v_add_u32 v246, s51, v232\n"
    "v_add_u32 v247, s52, v232\n"
    "s_waitcnt vmcnt(19)\n"
    "s_barrier\n"
    "ds_read_b64_tr_b16 v[192:193], v216 offset:0\n"
    "ds_read_b64_tr_b16 v[194:195], v216 offset:256\n"
    "ds_read_b64_tr_b16 v[196:197], v216 offset:4224\n"
    "ds_read_b64_tr_b16 v[198:199], v216 offset:4480\n"
    "ds_read_b64_tr_b16 v[200:201], v216 offset:8448\n"
    "ds_read_b64_tr_b16 v[202:203], v216 offset:8704\n"
    "s_waitcnt vmcnt(18) lgkmcnt(2)\n"
    "v_mfma_f32_16x16x32_bf16 v[32:35], v[192:195], a[0:3], 0\n"
    "s_waitcnt vmcnt(17)\n"
    "s_add_u32 m0, s56, 25344\n"
    "ds_read_b64_tr_b16 v[204:205], v216 offset:32\n"
    "v_mfma_f32_16x16x32_bf16 v[32:35], v[196:199], a[16:19], v[32:35]\n"
    "s_waitcnt vmcnt(16) lgkmcnt(1)\n"
    "s_add_u32 s48, %4, 884736\n"
    "ds_read_b64_tr_b16 v[206:207], v216 offset:288\n"
    "v_mfma_f32_16x16x32_bf16 v[32:35], v[200:203], a[32:35], v[32:35]\n"
    "s_waitcnt vmcnt(13)\n"
    "ds_read_b64_tr_b16 v[208:209], v216 offset:4256\n"
    "buffer_load_dwordx4 v219, s[92:95], s48 offen lds\n"
    "v_mfma_f32_16x16x32_bf16 v[36:39], v[200:203], a[36:39], 0\n"
    "ds_read_b64_tr_b16 v[210:211], v216 offset:4512\n"
    "v_mfma_f32_16x16x32_bf16 v[36:39], v[196:199], a[20:23], v[36:39]\n"
    "s_add_u32 m0, s56, 29568\n"
    "s_add_u32 s49, %4, 1327104\n"
    "ds_read_b64_tr_b16 v[212:213], v216 offset:8480\n"
    "v_mfma_f32_16x16x32_bf16 v[36:39], v[192:195], a[4:7], v[36:39]\n"
    "s_waitcnt vmcnt(13)\n"
    "ds_read_b64_tr_b16 v[214:215], v216 offset:8736\n"
    "buffer_load_dwordx4 v219, s[92:95], s49 offen lds\n"
    "v_mfma_f32_16x16x32_bf16 v[40:43], v[192:195], a[8:11], 0\n"
    "s_waitcnt vmcnt(13)\n"
    "v_mfma_f32_16x16x32_bf16 v[40:43], v[196:199], a[24:27], v[40:43]\n"
    "s_waitcnt vmcnt(12)\n"
    "v_mfma_f32_16x16x32_bf16 v[40:43], v[200:203], a[40:43], v[40:43]\n"
    "s_waitcnt vmcnt(9)\n"
    "v_mfma_f32_16x16x32_bf16 v[44:47], v[200:203], a[44:47], 0\n"
    "s_add_u32 m0, s56, 33792\n"
    "s_add_u32 s44, %4, 1622016\n"
    "v_mfma_f32_16x16x32_bf16 v[44:47], v[196:199], a[28:31], v[44:47]\n"
    "buffer_load_dwordx4 v219, s[92:95], s44 offen lds\n"
    "v_mfma_f32_16x16x32_bf16 v[44:47], v[192:195], a[12:15], v[44:47]\n"
    "s_waitcnt lgkmcnt(2)\n"
    "v_mfma_f32_16x16x32_bf16 v[64:67], v[204:207], a[0:3], 0\n"
    "ds_read_b64_tr_b16 v[192:193], v216 offset:64\n"
    "v_mfma_f32_16x16x32_bf16 v[64:67], v[208:211], a[16:19], v[64:67]\n"
    "s_waitcnt lgkmcnt(1)\n"
    "ds_read_b64_tr_b16 v[194:195], v216 offset:320\n"
    "v_mfma_f32_16x16x32_bf16 v[64:67], v[212:215], a[32:35], v[64:67]\n"
    "s_add_u32 s45, %8, 3072\n"
    "ds_read_b64_tr_b16 v[196:197], v216 offset:4288\n"
    "v_mfma_f32_16x16x32_bf16 v[68:71], v[212:215], a[36:39], 0\n"
    "ds_read_b64_tr_b16 v[198:199], v216 offset:4544\n"
    "buffer_load_dwordx4 a[48:51], v232, s[36:39], s45 offen offset:0\n"
    "v_mfma_f32_16x16x32_bf16 v[68:71], v[208:211], a[20:23], v[68:71]\n"
    "ds_read_b64_tr_b16 v[200:201], v216 offset:8512\n"
    "v_mfma_f32_16x16x32_bf16 v[68:71], v[204:207], a[4:7], v[68:71]\n"
    "ds_read_b64_tr_b16 v[202:203], v216 offset:8768\n"
    "v_mfma_f32_16x16x32_bf16 v[72:75], v[204:207], a[8:11], 0\n"
    "v_mfma_f32_16x16x32_bf16 v[72:75], v[208:211], a[24:27], v[72:75]\n"
    "v_mfma_f32_16x16x32_bf16 v[72:75], v[212:215], a[40:43], v[72:75]\n"
    "buffer_load_dwordx4 a[64:67], v232, s[36:39], s45 offen offset:1024\n"
    "v_mfma_f32_16x16x32_bf16 v[76:79], v[212:215], a[44:47], 0\n"
    "v_mfma_f32_16x16x32_bf16 v[76:79], v[208:211], a[28:31], v[76:79]\n"
    "v_mfma_f32_16x16x32_bf16 v[76:79], v[204:207], a[12:15], v[76:79]\n"
    "s_waitcnt lgkmcnt(2)\n"
    "v_mfma_f32_16x16x32_bf16 v[96:99], v[192:195], a[0:3], 0\n"
    "ds_read_b64_tr_b16 v[204:205], v216 offset:96\n"
    "v_mfma_f32_16x16x32_bf16 v[96:99], v[196:199], a[16:19], v[96:99]\n"
    "s_waitcnt lgkmcnt(1)\n"
    "ds_read_b64_tr_b16 v[206:207], v216 offset:352\n"
    "buffer_load_dwordx4 a[80:83], v232, s[36:39], s45 offen offset:2048\n"
    "v_mfma_f32_16x16x32_bf16 v[96:99], v[200:203], a[32:35], v[96:99]\n"
    "ds_read_b64_tr_b16 v[208:209], v216 offset:4320\n"
    "v_mfma_f32_16x16x32_bf16 v[100:103], v[200:203], a[36:39], 0\n"
    "ds_read_b64_tr_b16 v[210:211], v216 offset:4576\n"
    "v_mfma_f32_16x16x32_bf16 v[100:103], v[196:199], a[20:23], v[100:103]\n"
    "ds_read_b64_tr_b16 v[212:213], v216 offset:8544\n"
    "v_mfma_f32_16x16x32_bf16 v[100:103], v[192:195], a[4:7], v[100:103]\n"
    "s_add_u32 s46, %8, 21504\n"
    "ds_read_b64_tr_b16 v[214:215], v216 offset:8800\n"
    "v_mfma_f32_16x16x32_bf16 v[104:107], v[192:195], a[8:11], 0\n"
    "buffer_load_dwordx4 a[52:55], v232, s[36:39], s46 offen offset:0\n"
    "v_mfma_f32_16x16x32_bf16 v[104:107], v[196:199], a[24:27], v[104:107]\n"
    "v_mfma_f32_16x16x32_bf16 v[104:107], v[200:203], a[40:43], v[104:107]\n"
    "v_mfma_f32_16x16x32_bf16 v[108:111], v[200:203], a[44:47], 0\n"
    "v_mfma_f32_16x16x32_bf16 v[108:111], v[196:199], a[28:31], v[108:111]\n"
    "v_mfma_f32_16x16x32_bf16 v[108:111], v[192:195], a[12:15], v[108:111]\n"
    "buffer_load_dwordx4 a[68:71], v232, s[36:39], s46 offen offset:1024\n"
    "buffer_load_dwordx4 a[84:87], v232, s[36:39], s46 offen offset:2048\n"
    "s_waitcnt vmcnt(13)\n"
    "s_barrier\n"
    "s_waitcnt lgkmcnt(2)\n"
    "v_mfma_f32_16x16x32_bf16 v[128:131], v[204:207], a[0:3], 0\n"
    "ds_read_b64_tr_b16 v[192:193], v216 offset:12672\n"
    "v_mfma_f32_16x16x32_bf16 v[128:131], v[208:211], a[16:19], v[128:131]\n"
    "s_waitcnt lgkmcnt(1)\n"
    "ds_read_b64_tr_b16 v[194:195], v216 offset:12928\n"
    "v_mfma_f32_16x16x32_bf16 v[128:131], v[212:215], a[32:35], v[128:131]\n"
    "ds_read_b64_tr_b16 v[196:197], v216 offset:16896\n"
    "v_mfma_f32_16x16x32_bf16 v[132:135], v[212:215], a[36:39], 0\n"
    "s_add_u32 s47, %8, 39936\n"
    "ds_read_b64_tr_b16 v[198:199], v216 offset:17152\n"
    "v_mfma_f32_16x16x32_bf16 v[132:135], v[208:211], a[20:23], v[132:135]\n"
    "ds_read_b64_tr_b16 v[200:201], v216 offset:21120\n"
    "buffer_load_dwordx4 a[56:59], v232, s[36:39], s47 offen offset:0\n"
    "v_mfma_f32_16x16x32_bf16 v[132:135], v[204:207], a[4:7], v[132:135]\n"
    "ds_read_b64_tr_b16 v[202:203], v216 offset:21376\n"
    "v_mfma_f32_16x16x32_bf16 v[136:139], v[204:207], a[8:11], 0\n"
    "v_mfma_f32_16x16x32_bf16 v[136:139], v[208:211], a[24:27], v[136:139]\n"
    "v_mfma_f32_16x16x32_bf16 v[136:139], v[212:215], a[40:43], v[136:139]\n"
    "v_mfma_f32_16x16x32_bf16 v[140:143], v[212:215], a[44:47], 0\n"
    "buffer_load_dwordx4 a[72:75], v232, s[36:39], s47 offen offset:1024\n"
    "v_mfma_f32_16x16x32_bf16 v[140:143], v[208:211], a[28:31], v[140:143]\n"
    "v_mfma_f32_16x16x32_bf16 v[140:143], v[204:207], a[12:15], v[140:143]\n"
    "s_waitcnt lgkmcnt(2)\n"
    "v_mfma_f32_16x16x32_bf16 v[48:51], v[192:195], a[0:3], 0\n"
    "ds_read_b64_tr_b16 v[204:205], v216 offset:12704\n"
    "v_mfma_f32_16x16x32_bf16 v[48:51], v[196:199], a[16:19], v[48:51]\n"
    "s_waitcnt lgkmcnt(1)\n"
    "ds_read_b64_tr_b16 v[206:207], v216 offset:12960\n"
    "v_mfma_f32_16x16x32_bf16 v[48:51], v[200:203], a[32:35], v[48:51]\n"
    "ds_read_b64_tr_b16 v[208:209], v216 offset:16928\n"
    "buffer_load_dwordx4 a[88:91], v232, s[36:39], s47 offen offset:2048\n"
    "v_mfma_f32_16x16x32_bf16 v[52:55], v[200:203], a[36:39], 0\n"
    "ds_read_b64_tr_b16 v[210:211], v216 offset:17184\n"
    "v_mfma_f32_16x16x32_bf16 v[52:55], v[196:199], a[20:23], v[52:55]\n"
    "ds_read_b64_tr_b16 v[212:213], v216 offset:21152\n"
    "v_mfma_f32_16x16x32_bf16 v[52:55], v[192:195], a[4:7], v[52:55]\n"
    "ds_read_b64_tr_b16 v[214:215], v216 offset:21408\n"
    "v_mfma_f32_16x16x32_bf16 v[56:59], v[192:195], a[8:11], 0\n"
    "s_add_u32 s48, %8, 58368\n"
    "v_mfma_f32_16x16x32_bf16 v[56:59], v[196:199], a[24:27], v[56:59]\n"
    "buffer_load_dwordx4 a[60:63], v232, s[36:39], s48 offen offset:0\n"
    "v_mfma_f32_16x16x32_bf16 v[56:59], v[200:203], a[40:43], v[56:59]\n"
    "v_mfma_f32_16x16x32_bf16 v[60:63], v[200:203], a[44:47], 0\n"
    "v_mfma_f32_16x16x32_bf16 v[60:63], v[196:199], a[28:31], v[60:63]\n"
    "v_mfma_f32_16x16x32_bf16 v[60:63], v[192:195], a[12:15], v[60:63]\n"
    "s_waitcnt lgkmcnt(2)\n"
    "v_mfma_f32_16x16x32_bf16 v[80:83], v[204:207], a[0:3], 0\n"
    "ds_read_b64_tr_b16 v[192:193], v216 offset:12736\n"
    "buffer_load_dwordx4 a[76:79], v232, s[36:39], s48 offen offset:1024\n"
    "v_mfma_f32_16x16x32_bf16 v[80:83], v[208:211], a[16:19], v[80:83]\n"
    "s_waitcnt lgkmcnt(1)\n"
    "ds_read_b64_tr_b16 v[194:195], v216 offset:12992\n"
    "v_mfma_f32_16x16x32_bf16 v[80:83], v[212:215], a[32:35], v[80:83]\n"
    "ds_read_b64_tr_b16 v[196:197], v216 offset:16960\n"
    "v_mfma_f32_16x16x32_bf16 v[84:87], v[212:215], a[36:39], 0\n"
    "ds_read_b64_tr_b16 v[198:199], v216 offset:17216\n"
    "v_mfma_f32_16x16x32_bf16 v[84:87], v[208:211], a[20:23], v[84:87]\n"
    "ds_read_b64_tr_b16 v[200:201], v216 offset:21184\n"
    "v_mfma_f32_16x16x32_bf16 v[84:87], v[204:207], a[4:7], v[84:87]\n"
    "ds_read_b64_tr_b16 v[202:203], v216 offset:21440\n"
    "buffer_load_dwordx4 a[92:95], v232, s[36:39], s48 offen offset:2048\n"
    "v_mfma_f32_16x16x32_bf16 v[88:91], v[204:207], a[8:11], 0\n"
    "v_mfma_f32_16x16x32_bf16 v[88:91], v[208:211], a[24:27], v[88:91]\n"
    "v_mfma_f32_16x16x32_bf16 v[88:91], v[212:215], a[40:43], v[88:91]\n"
    "v_mfma_f32_16x16x32_bf16 v[92:95], v[212:215], a[44:47], 0\n"
    "s_add_u32 m0, s56, 38016\n"
    "s_add_u32 s49, %4, 884736\n"
    "v_mfma_f32_16x16x32_bf16 v[92:95], v[208:211], a[28:31], v[92:95]\n"
    "buffer_load_dwordx4 v219, s[96:99], s49 offen lds\n"
    "v_mfma_f32_16x16x32_bf16 v[92:95], v[204:207], a[12:15], v[92:95]\n"
    "s_waitcnt lgkmcnt(2)\n"
    "v_mfma_f32_16x16x32_bf16 v[112:115], v[192:195], a[0:3], 0\n"
    "ds_read_b64_tr_b16 v[204:205], v216 offset:12768\n"
    "v_mfma_f32_16x16x32_bf16 v[112:115], v[196:199], a[16:19], v[112:115]\n"
    "s_waitcnt lgkmcnt(1)\n"
    "ds_read_b64_tr_b16 v[206:207], v216 offset:13024\n"
    "v_mfma_f32_16x16x32_bf16 v[112:115], v[200:203], a[32:35], v[112:115]\n"
    "s_add_u32 m0, s56, 42240\n"
    "s_add_u32 s44, %4, 1327104\n"
    "ds_read_b64_tr_b16 v[208:209], v216 offset:16992\n"
    "v_mfma_f32_16x16x32_bf16 v[116:119], v[200:203], a[36:39], 0\n"
    "ds_read_b64_tr_b16 v[210:211], v216 offset:17248\n"
    "buffer_load_dwordx4 v219, s[96:99], s44 offen lds\n"
    "v_mfma_f32_16x16x32_bf16 v[116:119], v[196:199], a[20:23], v[116:119]\n"
    "ds_read_b64_tr_b16 v[212:213], v216 offset:21216\n"
    "v_mfma_f32_16x16x32_bf16 v[116:119], v[192:195], a[4:7], v[116:119]\n"
    "ds_read_b64_tr_b16 v[214:215], v216 offset:21472\n"
    "v_mfma_f32_16x16x32_bf16 v[120:123], v[192:195], a[8:11], 0\n"
    "v_mfma_f32_16x16x32_bf16 v[120:123], v[196:199], a[24:27], v[120:123]\n"
    "s_add_u32 m0, s56, 46464\n"
    "s_add_u32 s45, %4, 1622016\n"
    "v_mfma_f32_16x16x32_bf16 v[120:123], v[200:203], a[40:43], v[120:123]\n"
    "buffer_load_dwordx4 v219, s[96:99], s45 offen lds\n"
    "v_mfma_f32_16x16x32_bf16 v[124:127], v[200:203], a[44:47], 0\n"
    "v_mfma_f32_16x16x32_bf16 v[124:127], v[196:199], a[28:31], v[124:127]\n"
    "v_mfma_f32_16x16x32_bf16 v[124:127], v[192:195], a[12:15], v[124:127]\n"
    "s_waitcnt vmcnt(15)\n"
    "s_barrier\n"
    "s_waitcnt lgkmcnt(2)\n"
    "v_mfma_f32_16x16x32_bf16 v[144:147], v[204:207], a[0:3], 0\n"
    "s_add_u32 m0, s56, 50688\n"
    "s_add_u32 s46, %4, 1916928\n"
    "ds_read_b64_tr_b16 v[192:193], v216 offset:25344\n"
    "v_mfma_f32_16x16x32_bf16 v[144:147], v[208:211], a[16:19], v[144:147]\n"
    "s_waitcnt lgkmcnt(1)\n"
    "ds_read_b64_tr_b16 v[194:195], v216 offset:25600\n"
    "buffer_load_dwordx4 v219, s[92:95], s46 offen lds\n"
    "v_mfma_f32_16x16x32_bf16 v[144:147], v[212:215], a[32:35], v[144:147]\n"
    "ds_read_b64_tr_b16 v[196:197], v216 offset:29568\n"
    "v_mfma_f32_16x16x32_bf16 v[148:151], v[212:215], a[36:39], 0\n"
    "ds_read_b64_tr_b16 v[198:199], v216 offset:29824\n"
    "v_mfma_f32_16x16x32_bf16 v[148:151], v[208:211], a[20:23], v[148:151]\n"
    "ds_read_b64_tr_b16 v[200:201], v216 offset:33792\n"
    "v_mfma_f32_16x16x32_bf16 v[148:151], v[204:207], a[4:7], v[148:151]\n"
    "s_add_u32 m0, s56, 54912\n"
    "s_add_u32 s47, %4, 2211840\n"
    "ds_read_b64_tr_b16 v[202:203], v216 offset:34048\n"
    "v_mfma_f32_16x16x32_bf16 v[152:155], v[204:207], a[8:11], 0\n"
    "buffer_load_dwordx4 v219, s[92:95], s47 offen lds\n"
    "v_mfma_f32_16x16x32_bf16 v[152:155], v[208:211], a[24:27], v[152:155]\n"
    "v_mfma_f32_16x16x32_bf16 v[152:155], v[212:215], a[40:43], v[152:155]\n"
    "v_mfma_f32_16x16x32_bf16 v[156:159], v[212:215], a[44:47], 0\n"
    "v_mfma_f32_16x16x32_bf16 v[156:159], v[208:211], a[28:31], v[156:159]\n"
    "s_add_u32 m0, s56, 59136\n"
    "s_add_u32 s48, %4, 2654208\n"
    "v_mfma_f32_16x16x32_bf16 v[156:159], v[204:207], a[12:15], v[156:159]\n"
    "s_waitcnt vmcnt(16) lgkmcnt(2)\n"
    "buffer_load_dwordx4 v219, s[92:95], s48 offen lds\n"
    "v_mfma_f32_16x16x32_bf16 v[32:35], v[192:195], a[48:51], v[32:35]\n"
    "s_waitcnt vmcnt(16)\n"
    "ds_read_b64_tr_b16 v[204:205], v216 offset:25376\n"
    "v_mfma_f32_16x16x32_bf16 v[32:35], v[196:199], a[64:67], v[32:35]\n"
    "s_waitcnt vmcnt(15) lgkmcnt(1)\n"
    "ds_read_b64_tr_b16 v[206:207], v216 offset:25632\n"
    "v_mfma_f32_16x16x32_bf16 v[32:35], v[200:203], a[80:83], v[32:35]\n"
    "s_waitcnt vmcnt(12)\n"
    "ds_read_b64_tr_b16 v[208:209], v216 offset:29600\n"
    "v_mfma_f32_16x16x32_bf16 v[36:39], v[200:203], a[84:87], v[36:39]\n"
    "s_add_u32 m0, s56, 63360\n"
    "s_add_u32 s49, %4, 1916928\n"
    "ds_read_b64_tr_b16 v[210:211], v216 offset:29856\n"
    "v_mfma_f32_16x16x32_bf16 v[36:39], v[196:199], a[68:71], v[36:39]\n"
    "ds_read_b64_tr_b16 v[212:213], v216 offset:33824\n"
    "buffer_load_dwordx4 v219, s[96:99], s49 offen lds\n"
    "v_mfma_f32_16x16x32_bf16 v[36:39], v[192:195], a[52:55], v[36:39]\n"
    "s_waitcnt vmcnt(12)\n"
    "ds_read_b64_tr_b16 v[214:215], v216 offset:34080\n"
    "v_mfma_f32_16x16x32_bf16 v[40:43], v[192:195], a[56:59], v[40:43]\n"
    "s_waitcnt vmcnt(11)\n"
    "v_mfma_f32_16x16x32_bf16 v[40:43], v[196:199], a[72:75], v[40:43]\n"
    "s_waitcnt vmcnt(10)\n"
    "v_mfma_f32_16x16x32_bf16 v[40:43], v[200:203], a[88:91], v[40:43]\n"
    "s_waitcnt vmcnt(7)\n"
    "s_add_u32 m0, s56, 67584\n"
    "s_add_u32 s44, %4, 2211840\n"
    "v_mfma_f32_16x16x32_bf16 v[44:47], v[200:203], a[92:95], v[44:47]\n"
    "buffer_load_dwordx4 v219, s[96:99], s44 offen lds\n"
    "v_mfma_f32_16x16x32_bf16 v[44:47], v[196:199], a[76:79], v[44:47]\n"
    "v_mfma_f32_16x16x32_bf16 v[44:47], v[192:195], a[60:63], v[44:47]\n"
    "s_waitcnt lgkmcnt(2)\n"
    "v_mfma_f32_16x16x32_bf16 v[64:67], v[204:207], a[48:51], v[64:67]\n"
    "ds_read_b64_tr_b16 v[192:193], v216 offset:25408\n"
    "v_mfma_f32_16x16x32_bf16 v[64:67], v[208:211], a[64:67], v[64:67]\n"
    "s_waitcnt lgkmcnt(1)\n"
    "s_add_u32 m0, s56, 71808\n"
    "ds_read_b64_tr_b16 v[194:195], v216 offset:25664\n"
    "v_mfma_f32_16x16x32_bf16 v[64:67], v[212:215], a[80:83], v[64:67]\n"
    "s_add_u32 s45, %4, 2654208\n"
    "ds_read_b64_tr_b16 v[196:197], v216 offset:29632\n"
    "buffer_load_dwordx4 v219, s[96:99], s45 offen lds\n"
    "v_mfma_f32_16x16x32_bf16 v[68:71], v[212:215], a[84:87], v[68:71]\n"
    "ds_read_b64_tr_b16 v[198:199], v216 offset:29888\n"
    "v_mfma_f32_16x16x32_bf16 v[68:71], v[208:211], a[68:71], v[68:71]\n"
    "ds_read_b64_tr_b16 v[200:201], v216 offset:33856\n"
    "v_mfma_f32_16x16x32_bf16 v[68:71], v[204:207], a[52:55], v[68:71]\n"
    "ds_read_b64_tr_b16 v[202:203], v216 offset:34112\n"
    "v_mfma_f32_16x16x32_bf16 v[72:75], v[204:207], a[56:59], v[72:75]\n"
    "s_add_u32 s46, %8, 6144\n"
    "v_mfma_f32_16x16x32_bf16 v[72:75], v[208:211], a[72:75], v[72:75]\n"
    "buffer_load_dwordx4 a[96:99], v232, s[36:39], s46 offen offset:0\n"
    "v_mfma_f32_16x16x32_bf16 v[72:75], v[212:215], a[88:91], v[72:75]\n"
    "v_mfma_f32_16x16x32_bf16 v[76:79], v[212:215], a[92:95], v[76:79]\n"
    "v_mfma_f32_16x16x32_bf16 v[76:79], v[208:211], a[76:79], v[76:79]\n"
    "v_mfma_f32_16x16x32_bf16 v[76:79], v[204:207], a[60:63], v[76:79]\n"
    "s_waitcnt lgkmcnt(2)\n"
    "v_mfma_f32_16x16x32_bf16 v[96:99], v[192:195], a[48:51], v[96:99]\n"
    "ds_read_b64_tr_b16 v[204:205], v216 offset:25440\n"
    "buffer_load_dwordx4 a[112:115], v232, s[36:39], s46 offen offset:1024\n"
    "v_mfma_f32_16x16x32_bf16 v[96:99], v[196:199], a[64:67], v[96:99]\n"
    "s_waitcnt lgkmcnt(1)\n"
    "ds_read_b64_tr_b16 v[206:207], v216 offset:25696\n"
    "v_mfma_f32_16x16x32_bf16 v[96:99], v[200:203], a[80:83], v[96:99]\n"
    "ds_read_b64_tr_b16 v[208:209], v216 offset:29664\n"
    "v_mfma_f32_16x16x32_bf16 v[100:103], v[200:203], a[84:87], v[100:103]\n"
    "ds_read_b64_tr_b16 v[210:211], v216 offset:29920\n"
    "v_mfma_f32_16x16x32_bf16 v[100:103], v[196:199], a[68:71], v[100:103]\n"
    "ds_read_b64_tr_b16 v[212:213], v216 offset:33888\n"
    "v_mfma_f32_16x16x32_bf16 v[100:103], v[192:195], a[52:55], v[100:103]\n"
    "ds_read_b64_tr_b16 v[214:215], v216 offset:34144\n"
    "buffer_load_dwordx4 a[128:131], v232, s[36:39], s46 offen offset:2048\n"
    "v_mfma_f32_16x16x32_bf16 v[104:107], v[192:195], a[56:59], v[104:107]\n"
    "v_mfma_f32_16x16x32_bf16 v[104:107], v[196:199], a[72:75], v[104:107]\n"
    "v_mfma_f32_16x16x32_bf16 v[104:107], v[200:203], a[88:91], v[104:107]\n"
    "v_mfma_f32_16x16x32_bf16 v[108:111], v[200:203], a[92:95], v[108:111]\n"
    "s_add_u32 s47, %8, 24576\n"
    "v_mfma_f32_16x16x32_bf16 v[108:111], v[196:199], a[76:79], v[108:111]\n"
    "buffer_load_dwordx4 a[100:103], v232, s[36:39], s47 offen offset:0\n"
    "v_mfma_f32_16x16x32_bf16 v[108:111], v[192:195], a[60:63], v[108:111]\n"
    "buffer_load_dwordx4 a[116:119], v232, s[36:39], s47 offen offset:1024\n"
    "buffer_load_dwordx4 a[132:135], v232, s[36:39], s47 offen offset:2048\n"
    "s_waitcnt vmcnt(12)\n"
    "s_barrier\n"
    "s_waitcnt lgkmcnt(2)\n"
    "v_mfma_f32_16x16x32_bf16 v[128:131], v[204:207], a[48:51], v[128:131]\n"
    "ds_read_b64_tr_b16 v[192:193], v216 offset:38016\n"
    "v_mfma_f32_16x16x32_bf16 v[128:131], v[208:211], a[64:67], v[128:131]\n"
    "s_waitcnt lgkmcnt(1)\n"
    "ds_read_b64_tr_b16 v[194:195], v216 offset:38272\n"
    "v_mfma_f32_16x16x32_bf16 v[128:131], v[212:215], a[80:83], v[128:131]\n"
    "s_add_u32 s48, %8, 43008\n"
    "ds_read_b64_tr_b16 v[196:197], v216 offset:42240\n"
    "v_mfma_f32_16x16x32_bf16 v[132:135], v[212:215], a[84:87], v[132:135]\n"
    "ds_read_b64_tr_b16 v[198:199], v216 offset:42496\n"
    "buffer_load_dwordx4 a[104:107], v232, s[36:39], s48 offen offset:0\n"
    "v_mfma_f32_16x16x32_bf16 v[132:135], v[208:211], a[68:71], v[132:135]\n"
    "ds_read_b64_tr_b16 v[200:201], v216 offset:46464\n"
    "v_mfma_f32_16x16x32_bf16 v[132:135], v[204:207], a[52:55], v[132:135]\n"
    "ds_read_b64_tr_b16 v[202:203], v216 offset:46720\n"
    "v_mfma_f32_16x16x32_bf16 v[136:139], v[204:207], a[56:59], v[136:139]\n"
    "v_mfma_f32_16x16x32_bf16 v[136:139], v[208:211], a[72:75], v[136:139]\n"
    "v_mfma_f32_16x16x32_bf16 v[136:139], v[212:215], a[88:91], v[136:139]\n"
    "buffer_load_dwordx4 a[120:123], v232, s[36:39], s48 offen offset:1024\n"
    "v_mfma_f32_16x16x32_bf16 v[140:143], v[212:215], a[92:95], v[140:143]\n"
    "v_mfma_f32_16x16x32_bf16 v[140:143], v[208:211], a[76:79], v[140:143]\n"
    "v_mfma_f32_16x16x32_bf16 v[140:143], v[204:207], a[60:63], v[140:143]\n"
    "s_waitcnt lgkmcnt(2)\n"
    "v_mfma_f32_16x16x32_bf16 v[48:51], v[192:195], a[48:51], v[48:51]\n"
    "ds_read_b64_tr_b16 v[204:205], v216 offset:38048\n"
    "v_mfma_f32_16x16x32_bf16 v[48:51], v[196:199], a[64:67], v[48:51]\n"
    "s_waitcnt lgkmcnt(1)\n"
    "ds_read_b64_tr_b16 v[206:207], v216 offset:38304\n"
    "buffer_load_dwordx4 a[136:139], v232, s[36:39], s48 offen offset:2048\n"
    "v_mfma_f32_16x16x32_bf16 v[48:51], v[200:203], a[80:83], v[48:51]\n"
    "ds_read_b64_tr_b16 v[208:209], v216 offset:42272\n"
    "v_mfma_f32_16x16x32_bf16 v[52:55], v[200:203], a[84:87], v[52:55]\n"
    "ds_read_b64_tr_b16 v[210:211], v216 offset:42528\n"
    "v_mfma_f32_16x16x32_bf16 v[52:55], v[196:199], a[68:71], v[52:55]\n"
    "ds_read_b64_tr_b16 v[212:213], v216 offset:46496\n"
    "v_mfma_f32_16x16x32_bf16 v[52:55], v[192:195], a[52:55], v[52:55]\n"
    "s_add_u32 s49, %8, 61440\n"
    "ds_read_b64_tr_b16 v[214:215], v216 offset:46752\n"
    "v_mfma_f32_16x16x32_bf16 v[56:59], v[192:195], a[56:59], v[56:59]\n"
    "buffer_load_dwordx4 a[108:111], v232, s[36:39], s49 offen offset:0\n"
    "v_mfma_f32_16x16x32_bf16 v[56:59], v[196:199], a[72:75], v[56:59]\n"
    "v_mfma_f32_16x16x32_bf16 v[56:59], v[200:203], a[88:91], v[56:59]\n"
    "v_mfma_f32_16x16x32_bf16 v[60:63], v[200:203], a[92:95], v[60:63]\n"
    "v_mfma_f32_16x16x32_bf16 v[60:63], v[196:199], a[76:79], v[60:63]\n"
    "v_mfma_f32_16x16x32_bf16 v[60:63], v[192:195], a[60:63], v[60:63]\n"
    "s_waitcnt lgkmcnt(2)\n"
    "buffer_load_dwordx4 a[124:127], v232, s[36:39], s49 offen offset:1024\n"
    "v_mfma_f32_16x16x32_bf16 v[80:83], v[204:207], a[48:51], v[80:83]\n"
    "ds_read_b64_tr_b16 v[192:193], v216 offset:38080\n"
    "v_mfma_f32_16x16x32_bf16 v[80:83], v[208:211], a[64:67], v[80:83]\n"
    "s_waitcnt lgkmcnt(1)\n"
    "ds_read_b64_tr_b16 v[194:195], v216 offset:38336\n"
    "v_mfma_f32_16x16x32_bf16 v[80:83], v[212:215], a[80:83], v[80:83]\n"
    "ds_read_b64_tr_b16 v[196:197], v216 offset:42304\n"
    "v_mfma_f32_16x16x32_bf16 v[84:87], v[212:215], a[84:87], v[84:87]\n"
    "ds_read_b64_tr_b16 v[198:199], v216 offset:42560\n"
    "v_mfma_f32_16x16x32_bf16 v[84:87], v[208:211], a[68:71], v[84:87]\n"
    "ds_read_b64_tr_b16 v[200:201], v216 offset:46528\n"
    "buffer_load_dwordx4 a[140:143], v232, s[36:39], s49 offen offset:2048\n"
    "v_mfma_f32_16x16x32_bf16 v[84:87], v[204:207], a[52:55], v[84:87]\n"
    "ds_read_b64_tr_b16 v[202:203], v216 offset:46784\n"
    "v_mfma_f32_16x16x32_bf16 v[88:91], v[204:207], a[56:59], v[88:91]\n"
    "v_mfma_f32_16x16x32_bf16 v[88:91], v[208:211], a[72:75], v[88:91]\n"
    "v_mfma_f32_16x16x32_bf16 v[88:91], v[212:215], a[88:91], v[88:91]\n"
    "s_add_u32 m0, s56, 76032\n"
    "s_add_u32 s44, %4, 2949120\n"
    "v_mfma_f32_16x16x32_bf16 v[92:95], v[212:215], a[92:95], v[92:95]\n"
    "buffer_load_dwordx4 v219, s[92:95], s44 offen lds\n"
    "v_mfma_f32_16x16x32_bf16 v[92:95], v[208:211], a[76:79], v[92:95]\n"
    "v_mfma_f32_16x16x32_bf16 v[92:95], v[204:207], a[60:63], v[92:95]\n"
    "s_waitcnt lgkmcnt(2)\n"
    "v_mfma_f32_16x16x32_bf16 v[112:115], v[192:195], a[48:51], v[112:115]\n"
    "ds_read_b64_tr_b16 v[204:205], v216 offset:38112\n"
    "v_mfma_f32_16x16x32_bf16 v[112:115], v[196:199], a[64:67], v[112:115]\n"
    "s_waitcnt lgkmcnt(1)\n"
    "s_add_u32 m0, s56, 80256\n"
    "ds_read_b64_tr_b16 v[206:207], v216 offset:38368\n"
    "v_mfma_f32_16x16x32_bf16 v[112:115], v[200:203], a[80:83], v[112:115]\n"
    "s_add_u32 s45, %4, 3244032\n"
    "ds_read_b64_tr_b16 v[208:209], v216 offset:42336\n"
    "buffer_load_dwordx4 v219, s[92:95], s45 offen lds\n"
    "v_mfma_f32_16x16x32_bf16 v[116:119], v[200:203], a[84:87], v[116:119]\n"
    "ds_read_b64_tr_b16 v[210:211], v216 offset:42592\n"
    "v_mfma_f32_16x16x32_bf16 v[116:119], v[196:199], a[68:71], v[116:119]\n"
    "ds_read_b64_tr_b16 v[212:213], v216 offset:46560\n"
    "v_mfma_f32_16x16x32_bf16 v[116:119], v[192:195], a[52:55], v[116:119]\n"
    "ds_read_b64_tr_b16 v[214:215], v216 offset:46816\n"
    "v_mfma_f32_16x16x32_bf16 v[120:123], v[192:195], a[56:59], v[120:123]\n"
    "s_add_u32 m0, s56, 84480\n"
    "s_add_u32 s46, %4, 3538944\n"
    "v_mfma_f32_16x16x32_bf16 v[120:123], v[196:199], a[72:75], v[120:123]\n"
    "buffer_load_dwordx4 v219, s[92:95], s46 offen lds\n"
    "v_mfma_f32_16x16x32_bf16 v[120:123], v[200:203], a[88:91], v[120:123]\n"
    "v_mfma_f32_16x16x32_bf16 v[124:127], v[200:203], a[92:95], v[124:127]\n"
    "v_mfma_f32_16x16x32_bf16 v[124:127], v[196:199], a[76:79], v[124:127]\n"
    "v_mfma_f32_16x16x32_bf16 v[124:127], v[192:195], a[60:63], v[124:127]\n"
    "s_add_u32 m0, s56, 88704\n"
    "s_add_u32 s47, %4, 2949120\n"
    "buffer_load_dwordx4 v219, s[96:99], s47 offen lds\n"
    "s_waitcnt vmcnt(16)\n"
    "s_barrier\n"
    "s_waitcnt lgkmcnt(2)\n"
    "v_mfma_f32_16x16x32_bf16 v[144:147], v[204:207], a[48:51], v[144:147]\n"
    "s_add_u32 m0, s56, 92928\n"
    "s_add_u32 s48, %4, 3244032\n"
    "ds_read_b64_tr_b16 v[192:193], v216 offset:50688\n"
    "v_mfma_f32_16x16x32_bf16 v[144:147], v[208:211], a[64:67], v[144:147]\n"
    "s_waitcnt lgkmcnt(1)\n"
    "ds_read_b64_tr_b16 v[194:195], v216 offset:50944\n"
    "buffer_load_dwordx4 v219, s[96:99], s48 offen lds\n"
    "v_mfma_f32_16x16x32_bf16 v[144:147], v[212:215], a[80:83], v[144:147]\n"
    "ds_read_b64_tr_b16 v[196:197], v216 offset:54912\n"
    "v_mfma_f32_16x16x32_bf16 v[148:151], v[212:215], a[84:87], v[148:151]\n"
    "ds_read_b64_tr_b16 v[198:199], v216 offset:55168\n"
    "v_mfma_f32_16x16x32_bf16 v[148:151], v[208:211], a[68:71], v[148:151]\n"
    "s_add_u32 m0, s56, 97152\n"
    "s_add_u32 s49, %4, 3538944\n"
    "ds_read_b64_tr_b16 v[200:201], v216 offset:59136\n"
    "v_mfma_f32_16x16x32_bf16 v[148:151], v[204:207], a[52:55], v[148:151]\n"
    "ds_read_b64_tr_b16 v[202:203], v216 offset:59392\n"
    "buffer_load_dwordx4 v219, s[96:99], s49 offen lds\n"
    "v_mfma_f32_16x16x32_bf16 v[152:155], v[204:207], a[56:59], v[152:155]\n"
    "v_mfma_f32_16x16x32_bf16 v[152:155], v[208:211], a[72:75], v[152:155]\n"
    "v_mfma_f32_16x16x32_bf16 v[152:155], v[212:215], a[88:91], v[152:155]\n"
    "v_mfma_f32_16x16x32_bf16 v[156:159], v[212:215], a[92:95], v[156:159]\n"
    "s_add_u32 s44, %8, 9216\n"
    "v_mfma_f32_16x16x32_bf16 v[156:159], v[208:211], a[76:79], v[156:159]\n"
    "buffer_load_dwordx4 a[144:147], v232, s[36:39], s44 offen offset:0\n"
    "v_mfma_f32_16x16x32_bf16 v[156:159], v[204:207], a[60:63], v[156:159]\n"
    "s_waitcnt vmcnt(18) lgkmcnt(2)\n"
    "v_mfma_f32_16x16x32_bf16 v[32:35], v[192:195], a[96:99], v[32:35]\n"
    "s_waitcnt vmcnt(17)\n"
    "ds_read_b64_tr_b16 v[204:205], v216 offset:50720\n"
    "v_mov_b32 v252, -1\n"
    "v_mfma_f32_16x16x32_bf16 v[32:35], v[196:199], a[112:115], v[32:35]\n"
    "s_waitcnt vmcnt(16) lgkmcnt(1)\n"
    "ds_read_b64_tr_b16 v[206:207], v216 offset:50976\n"
    "v_mfma_f32_16x16x32_bf16 v[32:35], v[200:203], a[128:131], v[32:35]\n"
    "s_waitcnt vmcnt(13)\n"
    "ds_read_b64_tr_b16 v[208:209], v216 offset:54944\n"
    "v_mfma_f32_16x16x32_bf16 v[36:39], v[200:203], a[132:135], v[36:39]\n"
    "ds_read_b64_tr_b16 v[210:211], v216 offset:55200\n"
    "buffer_load_dwordx4 a[160:163], v232, s[36:39], s44 offen offset:1024\n"
    "v_mfma_f32_16x16x32_bf16 v[36:39], v[196:199], a[116:119], v[36:39]\n"
    "ds_read_b64_tr_b16 v[212:213], v216 offset:59168\n"
    "v_mfma_f32_16x16x32_bf16 v[36:39], v[192:195], a[100:103], v[36:39]\n"
    "s_waitcnt vmcnt(13)\n"
    "ds_read_b64_tr_b16 v[214:215], v216 offset:59424\n"
    "v_mfma_f32_16x16x32_bf16 v[40:43], v[192:195], a[104:107], v[40:43]\n"
    "s_waitcnt vmcnt(12)\n"
    "v_mov_b32 v253, -1\n"
    "v_mfma_f32_16x16x32_bf16 v[40:43], v[196:199], a[120:123], v[40:43]\n"
    "s_waitcnt vmcnt(11)\n"
    "v_mfma_f32_16x16x32_bf16 v[40:43], v[200:203], a[136:139], v[40:43]\n"
    "s_waitcnt vmcnt(8)\n"
    "buffer_load_dwordx4 a[176:179], v232, s[36:39], s44 offen offset:2048\n"
    "v_mfma_f32_16x16x32_bf16 v[44:47], v[200:203], a[140:143], v[44:47]\n"
    "v_mfma_f32_16x16x32_bf16 v[44:47], v[196:199], a[124:127], v[44:47]\n"
    "v_mfma_f32_16x16x32_bf16 v[44:47], v[192:195], a[108:111], v[44:47]\n"
    "s_waitcnt lgkmcnt(2)\n"
    "v_mfma_f32_16x16x32_bf16 v[64:67], v[204:207], a[96:99], v[64:67]\n"
    "s_add_u32 s45, %8, 27648\n"
    "ds_read_b64_tr_b16 v[192:193], v216 offset:50752\n"
    "v_mov_b32 v254, -1\n"
    "v_mfma_f32_16x16x32_bf16 v[64:67], v[208:211], a[112:115], v[64:67]\n"
    "s_waitcnt lgkmcnt(1)\n"
    "ds_read_b64_tr_b16 v[194:195], v216 offset:51008\n"
    "buffer_load_dwordx4 a[148:151], v232, s[36:39], s45 offen offset:0\n"
    "v_mfma_f32_16x16x32_bf16 v[64:67], v[212:215], a[128:131], v[64:67]\n"
    "ds_read_b64_tr_b16 v[196:197], v216 offset:54976\n"
    "v_mfma_f32_16x16x32_bf16 v[68:71], v[212:215], a[132:135], v[68:71]\n"
    "ds_read_b64_tr_b16 v[198:199], v216 offset:55232\n"
    "v_mfma_f32_16x16x32_bf16 v[68:71], v[208:211], a[116:119], v[68:71]\n"
    "ds_read_b64_tr_b16 v[200:201], v216 offset:59200\n"
    "v_mfma_f32_16x16x32_bf16 v[68:71], v[204:207], a[100:103], v[68:71]\n"
    "ds_read_b64_tr_b16 v[202:203], v216 offset:59456\n"
    "v_mfma_f32_16x16x32_bf16 v[72:75], v[204:207], a[104:107], v[72:75]\n"
    "buffer_load_dwordx4 a[164:167], v232, s[36:39], s45 offen offset:1024\n"
    "v_mov_b32 v255, -1\n"
    "v_mfma_f32_16x16x32_bf16 v[72:75], v[208:211], a[120:123], v[72:75]\n"
    "v_mfma_f32_16x16x32_bf16 v[72:75], v[212:215], a[136:139], v[72:75]\n"
    "v_mfma_f32_16x16x32_bf16 v[76:79], v[212:215], a[140:143], v[76:79]\n"
    "v_mfma_f32_16x16x32_bf16 v[76:79], v[208:211], a[124:127], v[76:79]\n"
    "v_mfma_f32_16x16x32_bf16 v[76:79], v[204:207], a[108:111], v[76:79]\n"
    "s_waitcnt lgkmcnt(2)\n"
    "buffer_load_dwordx4 a[180:183], v232, s[36:39], s45 offen offset:2048\n"
    "v_mfma_f32_16x16x32_bf16 v[96:99], v[192:195], a[96:99], v[96:99]\n"
    "ds_read_b64_tr_b16 v[204:205], v216 offset:50784\n"
    "v_mfma_f32_16x16x32_bf16 v[96:99], v[196:199], a[112:115], v[96:99]\n"
    "s_waitcnt lgkmcnt(1)\n"
    "ds_read_b64_tr_b16 v[206:207], v216 offset:51040\n"
    "v_mfma_f32_16x16x32_bf16 v[96:99], v[200:203], a[128:131], v[96:99]\n"
    "ds_read_b64_tr_b16 v[208:209], v216 offset:55008\n"
    "v_mfma_f32_16x16x32_bf16 v[100:103], v[200:203], a[132:135], v[100:103]\n"
    "s_add_u32 s46, %8, 46080\n"
    "ds_read_b64_tr_b16 v[210:211], v216 offset:55264\n"
    "v_mfma_f32_16x16x32_bf16 v[100:103], v[196:199], a[116:119], v[100:103]\n"
    "ds_read_b64_tr_b16 v[212:213], v216 offset:59232\n"
    "buffer_load_dwordx4 a[152:155], v232, s[36:39], s46 offen offset:0\n"
    "v_mfma_f32_16x16x32_bf16 v[100:103], v[192:195], a[100:103], v[100:103]\n"
    "ds_read_b64_tr_b16 v[214:215], v216 offset:59488\n"
    "v_mfma_f32_16x16x32_bf16 v[104:107], v[192:195], a[104:107], v[104:107]\n"
    "v_mfma_f32_16x16x32_bf16 v[104:107], v[196:199], a[120:123], v[104:107]\n"
    "v_mfma_f32_16x16x32_bf16 v[104:107], v[200:203], a[136:139], v[104:107]\n"
    "v_mfma_f32_16x16x32_bf16 v[108:111], v[200:203], a[140:143], v[108:111]\n"
    "buffer_load_dwordx4 a[168:171], v232, s[36:39], s46 offen offset:1024\n"
    "v_mfma_f32_16x16x32_bf16 v[108:111], v[196:199], a[124:127], v[108:111]\n"
    "v_mfma_f32_16x16x32_bf16 v[108:111], v[192:195], a[108:111], v[108:111]\n"
    "s_waitcnt lgkmcnt(2)\n"
    "v_mfma_f32_16x16x32_bf16 v[128:131], v[204:207], a[96:99], v[128:131]\n"
    "ds_read_b64_tr_b16 v[192:193], v216 offset:63360\n"
    "v_mfma_f32_16x16x32_bf16 v[128:131], v[208:211], a[112:115], v[128:131]\n"
    "s_waitcnt lgkmcnt(1)\n"
    "ds_read_b64_tr_b16 v[194:195], v216 offset:63616\n"
    "v_mfma_f32_16x16x32_bf16 v[128:131], v[212:215], a[128:131], v[128:131]\n"
    "ds_read_b64_tr_b16 v[196:197], v217 offset:2048\n"
    "buffer_load_dwordx4 a[184:187], v232, s[36:39], s46 offen offset:2048\n"
    "v_mfma_f32_16x16x32_bf16 v[132:135], v[212:215], a[132:135], v[132:135]\n"
    "ds_read_b64_tr_b16 v[198:199], v217 offset:2304\n"
    "v_mfma_f32_16x16x32_bf16 v[132:135], v[208:211], a[116:119], v[132:135]\n"
    "ds_read_b64_tr_b16 v[200:201], v217 offset:6272\n"
    "v_mfma_f32_16x16x32_bf16 v[132:135], v[204:207], a[100:103], v[132:135]\n"
    "ds_read_b64_tr_b16 v[202:203], v217 offset:6528\n"
    "v_mfma_f32_16x16x32_bf16 v[136:139], v[204:207], a[104:107], v[136:139]\n"
    "s_add_u32 s47, %8, 64512\n"
    "v_mfma_f32_16x16x32_bf16 v[136:139], v[208:211], a[120:123], v[136:139]\n"
    "buffer_load_dwordx4 a[156:159], v232, s[36:39], s47 offen offset:0\n"
    "v_mfma_f32_16x16x32_bf16 v[136:139], v[212:215], a[136:139], v[136:139]\n"
    "v_mfma_f32_16x16x32_bf16 v[140:143], v[212:215], a[140:143], v[140:143]\n"
    "v_mfma_f32_16x16x32_bf16 v[140:143], v[208:211], a[124:127], v[140:143]\n"
    "v_mfma_f32_16x16x32_bf16 v[140:143], v[204:207], a[108:111], v[140:143]\n"
    "s_waitcnt lgkmcnt(2)\n"
    "v_mfma_f32_16x16x32_bf16 v[48:51], v[192:195], a[96:99], v[48:51]\n"
    "ds_read_b64_tr_b16 v[204:205], v216 offset:63392\n"
    "buffer_load_dwordx4 a[172:175], v232, s[36:39], s47 offen offset:1024\n"
    "v_mfma_f32_16x16x32_bf16 v[48:51], v[196:199], a[112:115], v[48:51]\n"
    "s_waitcnt lgkmcnt(1)\n"
    "ds_read_b64_tr_b16 v[206:207], v216 offset:63648\n"
    "v_mfma_f32_16x16x32_bf16 v[48:51], v[200:203], a[128:131], v[48:51]\n"
    "ds_read_b64_tr_b16 v[208:209], v217 offset:2080\n"
    "v_mfma_f32_16x16x32_bf16 v[52:55], v[200:203], a[132:135], v[52:55]\n"
    "ds_read_b64_tr_b16 v[210:211], v217 offset:2336\n"
    "v_mfma_f32_16x16x32_bf16 v[52:55], v[196:199], a[116:119], v[52:55]\n"
    "ds_read_b64_tr_b16 v[212:213], v217 offset:6304\n"
    "v_mfma_f32_16x16x32_bf16 v[52:55], v[192:195], a[100:103], v[52:55]\n"
    "ds_read_b64_tr_b16 v[214:215], v217 offset:6560\n"
    "buffer_load_dwordx4 a[188:191], v232, s[36:39], s47 offen offset:2048\n"
    "v_mfma_f32_16x16x32_bf16 v[56:59], v[192:195], a[104:107], v[56:59]\n"
    "v_mfma_f32_16x16x32_bf16 v[56:59], v[196:199], a[120:123], v[56:59]\n"
    "v_mfma_f32_16x16x32_bf16 v[56:59], v[200:203], a[136:139], v[56:59]\n"
    "v_mfma_f32_16x16x32_bf16 v[60:63], v[200:203], a[140:143], v[60:63]\n"
    "s_add_u32 m0, s56, 101376\n"
    "s_add_u32 s48, %4, 3981312\n"
    "v_mfma_f32_16x16x32_bf16 v[60:63], v[196:199], a[124:127], v[60:63]\n"
    "buffer_load_dwordx4 v219, s[92:95], s48 offen lds\n"
    "v_mfma_f32_16x16x32_bf16 v[60:63], v[192:195], a[108:111], v[60:63]\n"
    "s_waitcnt lgkmcnt(2)\n"
    "v_mfma_f32_16x16x32_bf16 v[80:83], v[204:207], a[96:99], v[80:83]\n"
    "ds_read_b64_tr_b16 v[192:193], v216 offset:63424\n"
    "v_mfma_f32_16x16x32_bf16 v[80:83], v[208:211], a[112:115], v[80:83]\n"
    "s_waitcnt lgkmcnt(1)\n"
    "ds_read_b64_tr_b16 v[194:195], v216 offset:63680\n"
    "v_mfma_f32_16x16x32_bf16 v[80:83], v[212:215], a[128:131], v[80:83]\n"
    "s_add_u32 m0, s56, 105600\n"
    "s_add_u32 s49, %4, 4276224\n"
    "ds_read_b64_tr_b16 v[196:197], v217 offset:2112\n"
    "v_mfma_f32_16x16x32_bf16 v[84:87], v[212:215], a[132:135], v[84:87]\n"
    "ds_read_b64_tr_b16 v[198:199], v217 offset:2368\n"
    "buffer_load_dwordx4 v219, s[92:95], s49 offen lds\n"
    "v_mfma_f32_16x16x32_bf16 v[84:87], v[208:211], a[116:119], v[84:87]\n"
    "ds_read_b64_tr_b16 v[200:201], v217 offset:6336\n"
    "v_mfma_f32_16x16x32_bf16 v[84:87], v[204:207], a[100:103], v[84:87]\n"
    "ds_read_b64_tr_b16 v[202:203], v217 offset:6592\n"
    "v_mfma_f32_16x16x32_bf16 v[88:91], v[204:207], a[104:107], v[88:91]\n"
    "v_mfma_f32_16x16x32_bf16 v[88:91], v[208:211], a[120:123], v[88:91]\n"
    "s_add_u32 m0, s56, 109824\n"
    "s_add_u32 s44, %4, 4571136\n"
    "v_mfma_f32_16x16x32_bf16 v[88:91], v[212:215], a[136:139], v[88:91]\n"
    "buffer_load_dwordx4 v219, s[92:95], s44 offen lds\n"
    "v_mfma_f32_16x16x32_bf16 v[92:95], v[212:215], a[140:143], v[92:95]\n"
    "v_mfma_f32_16x16x32_bf16 v[92:95], v[208:211], a[124:127], v[92:95]\n"
    "v_mfma_f32_16x16x32_bf16 v[92:95], v[204:207], a[108:111], v[92:95]\n"
    "s_waitcnt lgkmcnt(2)\n"
    "v_mfma_f32_16x16x32_bf16 v[112:115], v[192:195], a[96:99], v[112:115]\n"
    "s_add_u32 m0, s56, 114048\n"
    "s_add_u32 s45, %4, 3981312\n"
    "ds_read_b64_tr_b16 v[204:205], v216 offset:63456\n"
    "v_mfma_f32_16x16x32_bf16 v[112:115], v[196:199], a[112:115], v[112:115]\n"
    "s_waitcnt lgkmcnt(1)\n"
    "ds_read_b64_tr_b16 v[206:207], v216 offset:63712\n"
    "buffer_load_dwordx4 v219, s[96:99], s45 offen lds\n"
    "v_mfma_f32_16x16x32_bf16 v[112:115], v[200:203], a[128:131], v[112:115]\n"
    "ds_read_b64_tr_b16 v[208:209], v217 offset:2144\n"
    "v_mfma_f32_16x16x32_bf16 v[116:119], v[200:203], a[132:135], v[116:119]\n"
    "ds_read_b64_tr_b16 v[210:211], v217 offset:2400\n"
    "v_mfma_f32_16x16x32_bf16 v[116:119], v[196:199], a[116:119], v[116:119]\n"
    "ds_read_b64_tr_b16 v[212:213], v217 offset:6368\n"
    "v_mfma_f32_16x16x32_bf16 v[116:119], v[192:195], a[100:103], v[116:119]\n"
    "s_add_u32 m0, s56, 118272\n"
    "s_add_u32 s46, %4, 4276224\n"
    "ds_read_b64_tr_b16 v[214:215], v217 offset:6624\n"
    "v_mfma_f32_16x16x32_bf16 v[120:123], v[192:195], a[104:107], v[120:123]\n"
    "buffer_load_dwordx4 v219, s[96:99], s46 offen lds\n"
    "v_mfma_f32_16x16x32_bf16 v[120:123], v[196:199], a[120:123], v[120:123]\n"
    "v_mfma_f32_16x16x32_bf16 v[120:123], v[200:203], a[136:139], v[120:123]\n"
    "v_mfma_f32_16x16x32_bf16 v[124:127], v[200:203], a[140:143], v[124:127]\n"
    "v_mfma_f32_16x16x32_bf16 v[124:127], v[196:199], a[124:127], v[124:127]\n"
    "s_add_u32 m0, s56, 122496\n"
    "s_add_u32 s47, %4, 4571136\n"
    "v_mfma_f32_16x16x32_bf16 v[124:127], v[192:195], a[108:111], v[124:127]\n"
    "buffer_load_dwordx4 v219, s[96:99], s47 offen lds\n"
    "s_waitcnt vmcnt(18)\n"
    "s_barrier\n"
    "s_waitcnt lgkmcnt(2)\n"
    "v_mfma_f32_16x16x32_bf16 v[144:147], v[204:207], a[96:99], v[144:147]\n"
    "ds_read_b64_tr_b16 v[192:193], v217 offset:10496\n"
    "v_mfma_f32_16x16x32_bf16 v[144:147], v[208:211], a[112:115], v[144:147]\n"
    "s_waitcnt lgkmcnt(1)\n"
    "ds_read_b64_tr_b16 v[194:195], v217 offset:10752\n"
    "v_mfma_f32_16x16x32_bf16 v[144:147], v[212:215], a[128:131], v[144:147]\n"
    "ds_read_b64_tr_b16 v[196:197], v217 offset:14720\n"
    "v_mfma_f32_16x16x32_bf16 v[148:151], v[212:215], a[132:135], v[148:151]\n"
    "s_add_u32 s48, %8, 12288\n"
    "ds_read_b64_tr_b16 v[198:199], v217 offset:14976\n"
    "v_mfma_f32_16x16x32_bf16 v[148:151], v[208:211], a[116:119], v[148:151]\n"
    "ds_read_b64_tr_b16 v[200:201], v217 offset:18944\n"
    "buffer_load_dwordx4 a[192:195], v232, s[36:39], s48 offen offset:0\n"
    "v_mfma_f32_16x16x32_bf16 v[148:151], v[204:207], a[100:103], v[148:151]\n"
    "ds_read_b64_tr_b16 v[202:203], v217 offset:19200\n"
    "v_mfma_f32_16x16x32_bf16 v[152:155], v[204:207], a[104:107], v[152:155]\n"
    "v_mfma_f32_16x16x32_bf16 v[152:155], v[208:211], a[120:123], v[152:155]\n"
    "v_mfma_f32_16x16x32_bf16 v[152:155], v[212:215], a[136:139], v[152:155]\n"
    "v_mfma_f32_16x16x32_bf16 v[156:159], v[212:215], a[140:143], v[156:159]\n"
    "buffer_load_dwordx4 a[208:211], v232, s[36:39], s48 offen offset:1024\n"
    "v_mfma_f32_16x16x32_bf16 v[156:159], v[208:211], a[124:127], v[156:159]\n"
    "v_mfma_f32_16x16x32_bf16 v[156:159], v[204:207], a[108:111], v[156:159]\n"
    "s_waitcnt vmcnt(19) lgkmcnt(2)\n"
    "v_mfma_f32_16x16x32_bf16 v[32:35], v[192:195], a[144:147], v[32:35]\n"
    "s_waitcnt vmcnt(18)\n"
    "ds_read_b64_tr_b16 v[204:205], v217 offset:10528\n"
    "v_mfma_f32_16x16x32_bf16 v[32:35], v[196:199], a[160:163], v[32:35]\n"
    "s_waitcnt vmcnt(17) lgkmcnt(1)\n"
    "ds_read_b64_tr_b16 v[206:207], v217 offset:10784\n"
    "v_mfma_f32_16x16x32_bf16 v[32:35], v[200:203], a[176:179], v[32:35]\n"
    "s_waitcnt vmcnt(14)\n"
    "ds_read_b64_tr_b16 v[208:209], v217 offset:14752\n"
    "buffer_load_dwordx4 a[224:227], v232, s[36:39], s48 offen offset:2048\n"
    "v_mfma_f32_16x16x32_bf16 v[36:39], v[200:203], a[180:183], v[36:39]\n"
    "ds_read_b64_tr_b16 v[210:211], v217 offset:15008\n"
    "v_mfma_f32_16x16x32_bf16 v[36:39], v[196:199], a[164:167], v[36:39]\n"
    "ds_read_b64_tr_b16 v[212:213], v217 offset:18976\n"
    "v_mfma_f32_16x16x32_bf16 v[36:39], v[192:195], a[148:151], v[36:39]\n"
    "s_waitcnt vmcnt(14)\n"
    "ds_read_b64_tr_b16 v[214:215], v217 offset:19232\n"
    "v_mfma_f32_16x16x32_bf16 v[40:43], v[192:195], a[152:155], v[40:43]\n"
    "s_waitcnt vmcnt(13)\n"
    "s_add_u32 s49, %8, 30720\n"
    "v_mfma_f32_16x16x32_bf16 v[40:43], v[196:199], a[168:171], v[40:43]\n"
    "s_waitcnt vmcnt(12)\n"
    "buffer_load_dwordx4 a[196:199], v232, s[36:39], s49 offen offset:0\n"
    "v_mfma_f32_16x16x32_bf16 v[40:43], v[200:203], a[184:187], v[40:43]\n"
    "s_waitcnt vmcnt(10)\n"
    "v_mfma_f32_16x16x32_bf16 v[44:47], v[200:203], a[188:191], v[44:47]\n"
    "v_mfma_f32_16x16x32_bf16 v[44:47], v[196:199], a[172:175], v[44:47]\n"
    "v_mfma_f32_16x16x32_bf16 v[44:47], v[192:195], a[156:159], v[44:47]\n"
    "s_waitcnt lgkmcnt(2)\n"
    "v_mfma_f32_16x16x32_bf16 v[64:67], v[204:207], a[144:147], v[64:67]\n"
    "ds_read_b64_tr_b16 v[192:193], v217 offset:10560\n"
    "buffer_load_dwordx4 a[212:215], v232, s[36:39], s49 offen offset:1024\n"
    "v_mfma_f32_16x16x32_bf16 v[64:67], v[208:211], a[160:163], v[64:67]\n"
    "s_waitcnt lgkmcnt(1)\n"
    "ds_read_b64_tr_b16 v[194:195], v217 offset:10816\n"
    "v_mfma_f32_16x16x32_bf16 v[64:67], v[212:215], a[176:179], v[64:67]\n"
    "ds_read_b64_tr_b16 v[196:197], v217 offset:14784\n"
    "v_mfma_f32_16x16x32_bf16 v[68:71], v[212:215], a[180:183], v[68:71]\n"
    "ds_read_b64_tr_b16 v[198:199], v217 offset:15040\n"
    "v_mfma_f32_16x16x32_bf16 v[68:71], v[208:211], a[164:167], v[68:71]\n"
    "ds_read_b64_tr_b16 v[200:201], v217 offset:19008\n"
    "v_mfma_f32_16x16x32_bf16 v[68:71], v[204:207], a[148:151], v[68:71]\n"
    "ds_read_b64_tr_b16 v[202:203], v217 offset:19264\n"
    "buffer_load_dwordx4 a[228:231], v232, s[36:39], s49 offen offset:2048\n"
    "v_mfma_f32_16x16x32_bf16 v[72:75], v[204:207], a[152:155], v[72:75]\n"
    "v_mfma_f32_16x16x32_bf16 v[72:75], v[208:211], a[168:171], v[72:75]\n"
    "v_mfma_f32_16x16x32_bf16 v[72:75], v[212:215], a[184:187], v[72:75]\n"
    "v_mfma_f32_16x16x32_bf16 v[76:79], v[212:215], a[188:191], v[76:79]\n"
    "s_add_u32 s44, %8, 49152\n"
    "v_mfma_f32_16x16x32_bf16 v[76:79], v[208:211], a[172:175], v[76:79]\n"
    "buffer_load_dwordx4 a[200:203], v232, s[36:39], s44 offen offset:0\n"
    "v_mfma_f32_16x16x32_bf16 v[76:79], v[204:207], a[156:159], v[76:79]\n"
    "s_waitcnt lgkmcnt(2)\n"
    "v_mfma_f32_16x16x32_bf16 v[96:99], v[192:195], a[144:147], v[96:99]\n"
    "ds_read_b64_tr_b16 v[204:205], v217 offset:10592\n"
    "v_mfma_f32_16x16x32_bf16 v[96:99], v[196:199], a[160:163], v[96:99]\n"
    "s_waitcnt lgkmcnt(1)\n"
    "ds_read_b64_tr_b16 v[206:207], v217 offset:10848\n"
    "v_mfma_f32_16x16x32_bf16 v[96:99], v[200:203], a[176:179], v[96:99]\n"
    "ds_read_b64_tr_b16 v[208:209], v217 offset:14816\n"
    "v_add_f32 v160, v160, v161\n"
    "v_mfma_f32_16x16x32_bf16 v[100:103], v[200:203], a[180:183], v[100:103]\n"
    "ds_read_b64_tr_b16 v[210:211], v217 offset:15072\n"
    "buffer_load_dwordx4 a[216:219], v232, s[36:39], s44 offen offset:1024\n"
    "v_mfma_f32_16x16x32_bf16 v[100:103], v[196:199], a[164:167], v[100:103]\n"
    "ds_read_b64_tr_b16 v[212:213], v217 offset:19040\n"
    "v_mfma_f32_16x16x32_bf16 v[100:103], v[192:195], a[148:151], v[100:103]\n"
    "ds_read_b64_tr_b16 v[214:215], v217 offset:19296\n"
    "v_add_f32 v162, v162, v163\n"
    "v_mfma_f32_16x16x32_bf16 v[104:107], v[192:195], a[152:155], v[104:107]\n"
    "v_mfma_f32_16x16x32_bf16 v[104:107], v[196:199], a[168:171], v[104:107]\n"
    "v_mfma_f32_16x16x32_bf16 v[104:107], v[200:203], a[184:187], v[104:107]\n"
    "buffer_load_dwordx4 a[232:235], v232, s[36:39], s44 offen offset:2048\n"
    "v_add_f32 v160, v160, v162\n"
    "v_mfma_f32_16x16x32_bf16 v[108:111], v[200:203], a[188:191], v[108:111]\n"
    "v_mfma_f32_16x16x32_bf16 v[108:111], v[196:199], a[172:175], v[108:111]\n"
    "v_mov_b32 v161, 0x3727c5ac\n"
    "v_mfma_f32_16x16x32_bf16 v[108:111], v[192:195], a[156:159], v[108:111]\n"
    "s_waitcnt lgkmcnt(2)\n"
    "v_mfma_f32_16x16x32_bf16 v[128:131], v[204:207], a[144:147], v[128:131]\n"
    "s_add_u32 s45, %8, 67584\n"
    "ds_read_b64_tr_b16 v[192:193], v217 offset:23168\n"
    "v_mfma_f32_16x16x32_bf16 v[128:131], v[208:211], a[160:163], v[128:131]\n"
    "s_waitcnt lgkmcnt(1)\n"
    "ds_read_b64_tr_b16 v[194:195], v217 offset:23424\n"
    "buffer_load_dwordx4 a[204:207], v232, s[36:39], s45 offen offset:0\n"
    "v_mfma_f32_16x16x32_bf16 v[128:131], v[212:215], a[176:179], v[128:131]\n"
    "ds_read_b64_tr_b16 v[196:197], v217 offset:27392\n"
    "v_fmac_f32 v161, 0x3ae38e39, v160\n"
    "v_mfma_f32_16x16x32_bf16 v[132:135], v[212:215], a[180:183], v[132:135]\n"
    "ds_read_b64_tr_b16 v[198:199], v217 offset:27648\n"
    "v_mfma_f32_16x16x32_bf16 v[132:135], v[208:211], a[164:167], v[132:135]\n"
    "ds_read_b64_tr_b16 v[200:201], v217 offset:31616\n"
    "v_mfma_f32_16x16x32_bf16 v[132:135], v[204:207], a[148:151], v[132:135]\n"
    "ds_read_b64_tr_b16 v[202:203], v217 offset:31872\n"
    "v_mfma_f32_16x16x32_bf16 v[136:139], v[204:207], a[152:155], v[136:139]\n"
    "buffer_load_dwordx4 a[220:223], v232, s[36:39], s45 offen offset:1024\n"
    "v_mfma_f32_16x16x32_bf16 v[136:139], v[208:211], a[168:171], v[136:139]\n"
    "v_sqrt_f32 v240, v161\n"
    "v_mfma_f32_16x16x32_bf16 v[136:139], v[212:215], a[184:187], v[136:139]\n"
    "v_mfma_f32_16x16x32_bf16 v[140:143], v[212:215], a[188:191], v[140:143]\n"
    "v_rsq_f32 v236, v161\n"
    "v_mfma_f32_16x16x32_bf16 v[140:143], v[208:211], a[172:175], v[140:143]\n"
    "v_mfma_f32_16x16x32_bf16 v[140:143], v[204:207], a[156:159], v[140:143]\n"
    "s_waitcnt lgkmcnt(2)\n"
    "buffer_load_dwordx4 a[236:239], v232, s[36:39], s45 offen offset:2048\n"
    "v_mfma_f32_16x16x32_bf16 v[48:51], v[192:195], a[144:147], v[48:51]\n"
    "ds_read_b64_tr_b16 v[204:205], v217 offset:23200\n"
    "v_mfma_f32_16x16x32_bf16 v[48:51], v[196:199], a[160:163], v[48:51]\n"
    "s_waitcnt lgkmcnt(1)\n"
    "ds_read_b64_tr_b16 v[206:207], v217 offset:23456\n"
    "v_mfma_f32_16x16x32_bf16 v[48:51], v[200:203], a[176:179], v[48:51]\n"
    "ds_read_b64_tr_b16 v[208:209], v217 offset:27424\n"
    "v_mfma_f32_16x16x32_bf16 v[52:55], v[200:203], a[180:183], v[52:55]\n"
    "s_add_u32 m0, s56, 126720\n"
    "s_add_u32 s46, %4, 4866048\n"
    "ds_read_b64_tr_b16 v[210:211], v217 offset:27680\n"
    "v_mfma_f32_16x16x32_bf16 v[52:55], v[196:199], a[164:167], v[52:55]\n"
    "ds_read_b64_tr_b16 v[212:213], v217 offset:31648\n"
    "buffer_load_dwordx4 v219, s[92:95], s46 offen lds\n"
    "v_mfma_f32_16x16x32_bf16 v[52:55], v[192:195], a[148:151], v[52:55]\n"
    "ds_read_b64_tr_b16 v[214:215], v217 offset:31904\n"
    "v_mul_f32 v236, 0xbfb8aa3b, v236\n"
    "v_mfma_f32_16x16x32_bf16 v[56:59], v[192:195], a[152:155], v[56:59]\n"
    "v_mfma_f32_16x16x32_bf16 v[56:59], v[196:199], a[168:171], v[56:59]\n"
    "v_mfma_f32_16x16x32_bf16 v[56:59], v[200:203], a[184:187], v[56:59]\n"
    "s_add_u32 m0, s56, 130944\n"
    "s_add_u32 s47, %4, 1179648\n"
    "v_add_f32 v164, v164, v165\n"
    "v_mfma_f32_16x16x32_bf16 v[60:63], v[200:203], a[188:191], v[60:63]\n"
    "buffer_load_dwordx4 v220, s[92:95], s47 offen lds\n"
    "v_mfma_f32_16x16x32_bf16 v[60:63], v[196:199], a[172:175], v[60:63]\n"
    "v_mfma_f32_16x16x32_bf16 v[60:63], v[192:195], a[156:159], v[60:63]\n"
    "s_waitcnt lgkmcnt(2)\n"
    "v_add_f32 v166, v166, v167\n"
    "v_mfma_f32_16x16x32_bf16 v[80:83], v[204:207], a[144:147], v[80:83]\n"
    "ds_read_b64_tr_b16 v[192:193], v217 offset:23232\n"
    "v_mfma_f32_16x16x32_bf16 v[80:83], v[208:211], a[160:163], v[80:83]\n"
    "s_waitcnt lgkmcnt(1)\n"
    "s_add_u32 m0, s56, 135168\n"
    "ds_read_b64_tr_b16 v[194:195], v217 offset:23488\n"
    "v_mfma_f32_16x16x32_bf16 v[80:83], v[212:215], a[176:179], v[80:83]\n"
    "s_add_u32 s48, %4, 3833856\n"
    "ds_read_b64_tr_b16 v[196:197], v217 offset:27456\n"
    "buffer_load_dwordx4 v220, s[92:95], s48 offen lds\n"
    "v_mfma_f32_16x16x32_bf16 v[84:87], v[212:215], a[180:183], v[84:87]\n"
    "ds_read_b64_tr_b16 v[198:199], v217 offset:27712\n"
    "v_add_f32 v164, v164, v166\n"
    "v_mfma_f32_16x16x32_bf16 v[84:87], v[208:211], a[164:167], v[84:87]\n"
    "ds_read_b64_tr_b16 v[200:201], v217 offset:31680\n"
    "v_mov_b32 v165, 0x3727c5ac\n"
    "v_mfma_f32_16x16x32_bf16 v[84:87], v[204:207], a[148:151], v[84:87]\n"
    "ds_read_b64_tr_b16 v[202:203], v217 offset:31936\n"
    "v_mfma_f32_16x16x32_bf16 v[88:91], v[204:207], a[152:155], v[88:91]\n"
    "s_add_u32 m0, s56, 139392\n"
    "s_add_u32 s49, %4, 4866048\n"
    "v_mfma_f32_16x16x32_bf16 v[88:91], v[208:211], a[168:171], v[88:91]\n"
    "buffer_load_dwordx4 v219, s[96:99], s49 offen lds\n"
    "v_fmac_f32 v165, 0x3ae38e39, v164\n"
    "v_mfma_f32_16x16x32_bf16 v[88:91], v[212:215], a[184:187], v[88:91]\n"
    "v_mfma_f32_16x16x32_bf16 v[92:95], v[212:215], a[188:191], v[92:95]\n"
    "v_mfma_f32_16x16x32_bf16 v[92:95], v[208:211], a[172:175], v[92:95]\n"
    "v_sqrt_f32 v241, v165\n"
    "v_mfma_f32_16x16x32_bf16 v[92:95], v[204:207], a[156:159], v[92:95]\n"
    "s_waitcnt lgkmcnt(2)\n"
    "s_add_u32 m0, s56, 143616\n"
    "s_add_u32 s44, %4, 1179648\n"
    "v_mfma_f32_16x16x32_bf16 v[112:115], v[192:195], a[144:147], v[112:115]\n"
    "ds_read_b64_tr_b16 v[204:205], v217 offset:23264\n"
    "buffer_load_dwordx4 v220, s[96:99], s44 offen lds\n"
    "v_mfma_f32_16x16x32_bf16 v[112:115], v[196:199], a[160:163], v[112:115]\n"
    "s_waitcnt lgkmcnt(1)\n"
    "ds_read_b64_tr_b16 v[206:207], v217 offset:23520\n"
    "v_mfma_f32_16x16x32_bf16 v[112:115], v[200:203], a[176:179], v[112:115]\n"
    "ds_read_b64_tr_b16 v[208:209], v217 offset:27488\n"
    "v_mfma_f32_16x16x32_bf16 v[116:119], v[200:203], a[180:183], v[116:119]\n"
    "ds_read_b64_tr_b16 v[210:211], v217 offset:27744\n"
    "v_mfma_f32_16x16x32_bf16 v[116:119], v[196:199], a[164:167], v[116:119]\n"
    "s_add_u32 m0, s56, 147840\n"
    "s_add_u32 s45, %4, 3833856\n"
    "ds_read_b64_tr_b16 v[212:213], v217 offset:31712\n"
    "v_mfma_f32_16x16x32_bf16 v[116:119], v[192:195], a[148:151], v[116:119]\n"
    "ds_read_b64_tr_b16 v[214:215], v217 offset:31968\n"
    "buffer_load_dwordx4 v220, s[96:99], s45 offen lds\n"
    "v_mfma_f32_16x16x32_bf16 v[120:123], v[192:195], a[152:155], v[120:123]\n"
    "v_rsq_f32 v237, v165\n"
    "v_mfma_f32_16x16x32_bf16 v[120:123], v[196:199], a[168:171], v[120:123]\n"
    "v_mfma_f32_16x16x32_bf16 v[120:123], v[200:203], a[184:187], v[120:123]\n"
    "v_mfma_f32_16x16x32_bf16 v[124:127], v[200:203], a[188:191], v[124:127]\n"
    "s_add_u32 m0, s56, 0\n"
    "s_add_u32 s46, %5, 0\n"
    "v_mul_f32 v237, 0xbfb8aa3b, v237\n"
    "v_mfma_f32_16x16x32_bf16 v[124:127], v[196:199], a[172:175], v[124:127]\n"
    "buffer_load_dwordx4 v232, s[36:39], s46 offen lds\n"
    "v_mfma_f32_16x16x32_bf16 v[124:127], v[192:195], a[156:159], v[124:127]\n"
    "s_waitcnt vmcnt(19)\n"
    "s_barrier\n"
    "s_waitcnt lgkmcnt(2)\n"
    "v_mfma_f32_16x16x32_bf16 v[144:147], v[204:207], a[144:147], v[144:147]\n"
    "ds_read_b64_tr_b16 v[192:193], v217 offset:35840\n"
    "v_mfma_f32_16x16x32_bf16 v[144:147], v[208:211], a[160:163], v[144:147]\n"
    "s_waitcnt lgkmcnt(1)\n"
    "ds_read_b64_tr_b16 v[194:195], v217 offset:36096\n"
    "v_mfma_f32_16x16x32_bf16 v[144:147], v[212:215], a[176:179], v[144:147]\n"
    "s_add_u32 s47, %8, 15360\n"
    "ds_read_b64_tr_b16 v[196:197], v217 offset:40064\n"
    "v_add_f32 v168, v168, v169\n"
    "v_mfma_f32_16x16x32_bf16 v[148:151], v[212:215], a[180:183], v[148:151]\n"
    "ds_read_b64_tr_b16 v[198:199], v217 offset:40320\n"
    "buffer_load_dwordx4 a[240:243], v232, s[36:39], s47 offen offset:0\n"
    "v_mfma_f32_16x16x32_bf16 v[148:151], v[208:211], a[164:167], v[148:151]\n"
    "ds_read_b64_tr_b16 v[200:201], v217 offset:44288\n"
    "v_mfma_f32_16x16x32_bf16 v[148:151], v[204:207], a[148:151], v[148:151]\n"
    "ds_read_b64_tr_b16 v[202:203], v217 offset:44544\n"
    "v_add_f32 v170, v170, v171\n"
    "v_mfma_f32_16x16x32_bf16 v[152:155], v[204:207], a[152:155], v[152:155]\n"
    "v_mfma_f32_16x16x32_bf16 v[152:155], v[208:211], a[168:171], v[152:155]\n"
    "v_mfma_f32_16x16x32_bf16 v[152:155], v[212:215], a[184:187], v[152:155]\n"
    "buffer_load_dwordx4 v[0:3], v232, s[36:39], s47 offen offset:1024\n"
    "v_add_f32 v168, v168, v170\n"
    "v_mfma_f32_16x16x32_bf16 v[156:159], v[212:215], a[188:191], v[156:159]\n"
    "v_mfma_f32_16x16x32_bf16 v[156:159], v[208:211], a[172:175], v[156:159]\n"
    "v_mov_b32 v169, 0x3727c5ac\n"
    "v_mfma_f32_16x16x32_bf16 v[156:159], v[204:207], a[156:159], v[156:159]\n"
    "s_waitcnt vmcnt(20) lgkmcnt(2)\n"
    "v_mfma_f32_16x16x32_bf16 v[32:35], v[192:195], a[192:195], v[32:35]\n"
    "s_waitcnt vmcnt(19)\n"
    "ds_read_b64_tr_b16 v[204:205], v217 offset:35872\n"
    "v_mfma_f32_16x16x32_bf16 v[32:35], v[196:199], a[208:211], v[32:35]\n"
    "s_waitcnt vmcnt(18) lgkmcnt(1)\n"
    "ds_read_b64_tr_b16 v[206:207], v217 offset:36128\n"
    "buffer_load_dwordx4 v[16:19], v232, s[36:39], s47 offen offset:2048\n"
    "v_mfma_f32_16x16x32_bf16 v[32:35], v[200:203], a[224:227], v[32:35]\n"
    "s_waitcnt vmcnt(16)\n"
    "ds_read_b64_tr_b16 v[208:209], v217 offset:40096\n"
    "v_fmac_f32 v169, 0x3ae38e39, v168\n"
    "v_mfma_f32_16x16x32_bf16 v[36:39], v[200:203], a[228:231], v[36:39]\n"
    "ds_read_b64_tr_b16 v[210:211], v217 offset:40352\n"
    "v_mfma_f32_16x16x32_bf16 v[36:39], v[196:199], a[212:215], v[36:39]\n"
    "ds_read_b64_tr_b16 v[212:213], v217 offset:44320\n"
    "v_mfma_f32_16x16x32_bf16 v[36:39], v[192:195], a[196:199], v[36:39]\n"
    "s_waitcnt vmcnt(15)\n"
    "s_add_u32 s48, %8, 33792\n"
    "ds_read_b64_tr_b16 v[214:215], v217 offset:44576\n"
    "v_mfma_f32_16x16x32_bf16 v[40:43], v[192:195], a[200:203], v[40:43]\n"
    "s_waitcnt vmcnt(14)\n"
    "buffer_load_dwordx4 a[244:247], v232, s[36:39], s48 offen offset:0\n"
    "v_mfma_f32_16x16x32_bf16 v[40:43], v[196:199], a[216:219], v[40:43]\n"
    "s_waitcnt vmcnt(14)\n"
    "v_sqrt_f32 v242, v169\n"
    "v_mfma_f32_16x16x32_bf16 v[40:43], v[200:203], a[232:235], v[40:43]\n"
    "s_waitcnt vmcnt(11)\n"
    "v_mfma_f32_16x16x32_bf16 v[44:47], v[200:203], a[236:239], v[44:47]\n"
    "v_rsq_f32 v238, v169\n"
    "v_mfma_f32_16x16x32_bf16 v[44:47], v[196:199], a[220:223], v[44:47]\n"
    "v_mfma_f32_16x16x32_bf16 v[44:47], v[192:195], a[204:207], v[44:47]\n"
    "s_waitcnt lgkmcnt(2)\n"
    "buffer_load_dwordx4 v[4:7], v232, s[36:39], s48 offen offset:1024\n"
    "v_mfma_f32_16x16x32_bf16 v[64:67], v[204:207], a[192:195], v[64:67]\n"
    "ds_read_b64_tr_b16 v[192:193], v217 offset:35904\n"
    "v_mfma_f32_16x16x32_bf16 v[64:67], v[208:211], a[208:211], v[64:67]\n"
    "s_waitcnt lgkmcnt(1)\n"
    "ds_read_b64_tr_b16 v[194:195], v217 offset:36160\n"
    "v_mfma_f32_16x16x32_bf16 v[64:67], v[212:215], a[224:227], v[64:67]\n"
    "ds_read_b64_tr_b16 v[196:197], v217 offset:40128\n"
    "v_mfma_f32_16x16x32_bf16 v[68:71], v[212:215], a[228:231], v[68:71]\n"
    "ds_read_b64_tr_b16 v[198:199], v217 offset:40384\n"
    "v_mul_f32 v238, 0xbfb8aa3b, v238\n"
    "v_mfma_f32_16x16x32_bf16 v[68:71], v[208:211], a[212:215], v[68:71]\n"
    "ds_read_b64_tr_b16 v[200:201], v217 offset:44352\n"
    "buffer_load_dwordx4 v[20:23], v232, s[36:39], s48 offen offset:2048\n"
    "v_mfma_f32_16x16x32_bf16 v[68:71], v[204:207], a[196:199], v[68:71]\n"
    "ds_read_b64_tr_b16 v[202:203], v217 offset:44608\n"
    "v_mfma_f32_16x16x32_bf16 v[72:75], v[204:207], a[200:203], v[72:75]\n"
    "v_mfma_f32_16x16x32_bf16 v[72:75], v[208:211], a[216:219], v[72:75]\n"
    "v_mfma_f32_16x16x32_bf16 v[72:75], v[212:215], a[232:235], v[72:75]\n"
    "s_add_u32 s49, %8, 52224\n"
    "v_add_f32 v172, v172, v173\n"
    "v_mfma_f32_16x16x32_bf16 v[76:79], v[212:215], a[236:239], v[76:79]\n"
    "buffer_load_dwordx4 a[248:251], v232, s[36:39], s49 offen offset:0\n"
    "v_mfma_f32_16x16x32_bf16 v[76:79], v[208:211], a[220:223], v[76:79]\n"
    "v_mfma_f32_16x16x32_bf16 v[76:79], v[204:207], a[204:207], v[76:79]\n"
    "s_waitcnt lgkmcnt(2)\n"
    "v_add_f32 v174, v174, v175\n"
    "v_mfma_f32_16x16x32_bf16 v[96:99], v[192:195], a[192:195], v[96:99]\n"
    "ds_read_b64_tr_b16 v[204:205], v217 offset:35936\n"
    "v_mfma_f32_16x16x32_bf16 v[96:99], v[196:199], a[208:211], v[96:99]\n"
    "s_waitcnt lgkmcnt(1)\n"
    "ds_read_b64_tr_b16 v[206:207], v217 offset:36192\n"
    "v_mfma_f32_16x16x32_bf16 v[96:99], v[200:203], a[224:227], v[96:99]\n"
    "ds_read_b64_tr_b16 v[208:209], v217 offset:40160\n"
    "buffer_load_dwordx4 v[8:11], v232, s[36:39], s49 offen offset:1024\n"
    "v_mfma_f32_16x16x32_bf16 v[100:103], v[200:203], a[228:231], v[100:103]\n"
    "ds_read_b64_tr_b16 v[210:211], v217 offset:40416\n"
    "v_add_f32 v172, v172, v174\n"
    "v_mfma_f32_16x16x32_bf16 v[100:103], v[196:199], a[212:215], v[100:103]\n"
    "ds_read_b64_tr_b16 v[212:213], v217 offset:44384\n"
    "v_mov_b32 v173, 0x3727c5ac\n"
    "v_mfma_f32_16x16x32_bf16 v[100:103], v[192:195], a[196:199], v[100:103]\n"
    "ds_read_b64_tr_b16 v[214:215], v217 offset:44640\n"
    "v_mfma_f32_16x16x32_bf16 v[104:107], v[192:195], a[200:203], v[104:107]\n"
    "v_mfma_f32_16x16x32_bf16 v[104:107], v[196:199], a[216:219], v[104:107]\n"
    "buffer_load_dwordx4 v[24:27], v232, s[36:39], s49 offen offset:2048\n"
    "v_fmac_f32 v173, 0x3ae38e39, v172\n"
    "v_mfma_f32_16x16x32_bf16 v[104:107], v[200:203], a[232:235], v[104:107]\n"
    "v_mfma_f32_16x16x32_bf16 v[108:111], v[200:203], a[236:239], v[108:111]\n"
    "v_mfma_f32_16x16x32_bf16 v[108:111], v[196:199], a[220:223], v[108:111]\n"
    "v_sqrt_f32 v243, v173\n"
    "v_mfma_f32_16x16x32_bf16 v[108:111], v[192:195], a[204:207], v[108:111]\n"
    "s_waitcnt lgkmcnt(2)\n"
    "s_add_u32 s44, %8, 70656\n"
    "v_mfma_f32_16x16x32_bf16 v[128:131], v[204:207], a[192:195], v[128:131]\n"
    "ds_read_b64_tr_b16 v[192:193], v217 offset:48512\n"
    "buffer_load_dwordx4 a[252:255], v232, s[36:39], s44 offen offset:0\n"
    "v_mfma_f32_16x16x32_bf16 v[128:131], v[208:211], a[208:211], v[128:131]\n"
    "s_waitcnt lgkmcnt(1)\n"
    "ds_read_b64_tr_b16 v[194:195], v217 offset:48768\n"
    "v_mfma_f32_16x16x32_bf16 v[128:131], v[212:215], a[224:227], v[128:131]\n"
    "ds_read_b64_tr_b16 v[196:197], v217 offset:52736\n"
    "v_mfma_f32_16x16x32_bf16 v[132:135], v[212:215], a[228:231], v[132:135]\n"
    "ds_read_b64_tr_b16 v[198:199], v217 offset:52992\n"
    "v_mfma_f32_16x16x32_bf16 v[132:135], v[208:211], a[212:215], v[132:135]\n"
    "ds_read_b64_tr_b16 v[200:201], v217 offset:56960\n"
    "v_mfma_f32_16x16x32_bf16 v[132:135], v[204:207], a[196:199], v[132:135]\n"
    "ds_read_b64_tr_b16 v[202:203], v217 offset:57216\n"
    "buffer_load_dwordx4 v[12:15], v232, s[36:39], s44 offen offset:1024\n"
    "v_mfma_f32_16x16x32_bf16 v[136:139], v[204:207], a[200:203], v[136:139]\n"
    "v_rsq_f32 v239, v173\n"
    "v_mfma_f32_16x16x32_bf16 v[136:139], v[208:211], a[216:219], v[136:139]\n"
    "v_mfma_f32_16x16x32_bf16 v[136:139], v[212:215], a[232:235], v[136:139]\n"
    "v_mfma_f32_16x16x32_bf16 v[140:143], v[212:215], a[236:239], v[140:143]\n"
    "v_mul_f32 v239, 0xbfb8aa3b, v239\n"
    "v_mfma_f32_16x16x32_bf16 v[140:143], v[208:211], a[220:223], v[140:143]\n"
    "buffer_load_dwordx4 v[28:31], v232, s[36:39], s44 offen offset:2048\n"
    "v_mfma_f32_16x16x32_bf16 v[140:143], v[204:207], a[204:207], v[140:143]\n"
    "s_waitcnt lgkmcnt(2)\n"
    "v_mfma_f32_16x16x32_bf16 v[48:51], v[192:195], a[192:195], v[48:51]\n"
    "ds_read_b64_tr_b16 v[204:205], v217 offset:48544\n"
    "v_mfma_f32_16x16x32_bf16 v[48:51], v[196:199], a[208:211], v[48:51]\n"
    "s_waitcnt lgkmcnt(1)\n"
    "ds_read_b64_tr_b16 v[206:207], v217 offset:48800\n"
    "v_mfma_f32_16x16x32_bf16 v[48:51], v[200:203], a[224:227], v[48:51]\n"
    "s_add_u32 m0, s56, 4224\n"
    "s_add_u32 s45, %5, 4096\n"
    "ds_read_b64_tr_b16 v[208:209], v217 offset:52768\n"
    "v_mfma_f32_16x16x32_bf16 v[52:55], v[200:203], a[228:231], v[52:55]\n"
    "ds_read_b64_tr_b16 v[210:211], v217 offset:53024\n"
    "buffer_load_dwordx4 v232, s[36:39], s45 offen lds\n"
    "v_mfma_f32_16x16x32_bf16 v[52:55], v[196:199], a[212:215], v[52:55]\n"
    "ds_read_b64_tr_b16 v[212:213], v217 offset:56992\n"
    "v_mfma_f32_16x16x32_bf16 v[52:55], v[192:195], a[196:199], v[52:55]\n"
    "ds_read_b64_tr_b16 v[214:215], v217 offset:57248\n"
    "v_mfma_f32_16x16x32_bf16 v[56:59], v[192:195], a[200:203], v[56:59]\n"
    "v_mfma_f32_16x16x32_bf16 v[56:59], v[196:199], a[216:219], v[56:59]\n"
    "s_add_u32 m0, s56, 8448\n"
    "s_add_u32 s46, %5, 8192\n"
    "v_mfma_f32_16x16x32_bf16 v[56:59], v[200:203], a[232:235], v[56:59]\n"
    "buffer_load_dwordx4 v232, s[36:39], s46 offen lds\n"
    "v_mfma_f32_16x16x32_bf16 v[60:63], v[200:203], a[236:239], v[60:63]\n"
    "v_mfma_f32_16x16x32_bf16 v[60:63], v[196:199], a[220:223], v[60:63]\n"
    "v_mfma_f32_16x16x32_bf16 v[60:63], v[192:195], a[204:207], v[60:63]\n"
    "s_waitcnt lgkmcnt(2)\n"
    "v_mfma_f32_16x16x32_bf16 v[80:83], v[204:207], a[192:195], v[80:83]\n"
    "s_add_u32 m0, s56, 12672\n"
    "s_add_u32 s47, %5, 12288\n"
    "ds_read_b64_tr_b16 v[192:193], v217 offset:48576\n"
    "v_mfma_f32_16x16x32_bf16 v[80:83], v[208:211], a[208:211], v[80:83]\n"
    "s_waitcnt lgkmcnt(1)\n"
    "ds_read_b64_tr_b16 v[194:195], v217 offset:48832\n"
    "buffer_load_dwordx4 v232, s[36:39], s47 offen lds\n"
    "v_mfma_f32_16x16x32_bf16 v[80:83], v[212:215], a[224:227], v[80:83]\n"
    "ds_read_b64_tr_b16 v[196:197], v217 offset:52800\n"
    "v_mfma_f32_16x16x32_bf16 v[84:87], v[212:215], a[228:231], v[84:87]\n"
    "ds_read_b64_tr_b16 v[198:199], v217 offset:53056\n"
    "v_mfma_f32_16x16x32_bf16 v[84:87], v[208:211], a[212:215], v[84:87]\n"
    "ds_read_b64_tr_b16 v[200:201], v217 offset:57024\n"
    "v_mfma_f32_16x16x32_bf16 v[84:87], v[204:207], a[196:199], v[84:87]\n"
    "s_add_u32 m0, s56, 16896\n"
    "s_add_u32 s48, %5, 16384\n"
    "ds_read_b64_tr_b16 v[202:203], v217 offset:57280\n"
    "v_mfma_f32_16x16x32_bf16 v[88:91], v[204:207], a[200:203], v[88:91]\n"
    "buffer_load_dwordx4 v232, s[36:39], s48 offen lds\n"
    "v_mfma_f32_16x16x32_bf16 v[88:91], v[208:211], a[216:219], v[88:91]\n"
    "v_mfma_f32_16x16x32_bf16 v[88:91], v[212:215], a[232:235], v[88:91]\n"
    "v_mfma_f32_16x16x32_bf16 v[92:95], v[212:215], a[236:239], v[92:95]\n"
    "v_mfma_f32_16x16x32_bf16 v[92:95], v[208:211], a[220:223], v[92:95]\n"
    "s_add_u32 m0, s56, 21120\n"
    "s_add_u32 s49, %5, 20480\n"
    "v_mfma_f32_16x16x32_bf16 v[92:95], v[204:207], a[204:207], v[92:95]\n"
    "s_waitcnt lgkmcnt(2)\n"
    "buffer_load_dwordx4 v232, s[36:39], s49 offen lds\n"
    "v_mfma_f32_16x16x32_bf16 v[112:115], v[192:195], a[192:195], v[112:115]\n"
    "ds_read_b64_tr_b16 v[204:205], v217 offset:48608\n"
    "v_mfma_f32_16x16x32_bf16 v[112:115], v[196:199], a[208:211], v[112:115]\n"
    "s_waitcnt lgkmcnt(1)\n"
    "ds_read_b64_tr_b16 v[206:207], v217 offset:48864\n"
    "v_mfma_f32_16x16x32_bf16 v[112:115], v[200:203], a[224:227], v[112:115]\n"
    "ds_read_b64_tr_b16 v[208:209], v217 offset:52832\n"
    "v_mfma_f32_16x16x32_bf16 v[116:119], v[200:203], a[228:231], v[116:119]\n"
    "s_add_u32 s53, %19, 0\n"
    "ds_read_b64_tr_b16 v[210:211], v217 offset:53088\n"
    "v_mfma_f32_16x16x32_bf16 v[116:119], v[196:199], a[212:215], v[116:119]\n"
    "ds_read_b64_tr_b16 v[212:213], v217 offset:57056\n"
    "buffer_store_dwordx4 v[252:255], v232, s[36:39], s53 offen offset:0 sc1\n"
    "v_mfma_f32_16x16x32_bf16 v[116:119], v[192:195], a[196:199], v[116:119]\n"
    "ds_read_b64_tr_b16 v[214:215], v217 offset:57312\n"
    "v_mfma_f32_16x16x32_bf16 v[120:123], v[192:195], a[200:203], v[120:123]\n"
    "v_mfma_f32_16x16x32_bf16 v[120:123], v[196:199], a[216:219], v[120:123]\n"
    "v_mfma_f32_16x16x32_bf16 v[120:123], v[200:203], a[232:235], v[120:123]\n"
    "s_add_u32 m0, s56, 25344\n"
    "s_add_u32 s44, %5, 24576\n"
    "v_mfma_f32_16x16x32_bf16 v[124:127], v[200:203], a[236:239], v[124:127]\n"
    "buffer_load_dwordx4 v232, s[36:39], s44 offen lds\n"
    "v_mfma_f32_16x16x32_bf16 v[124:127], v[196:199], a[220:223], v[124:127]\n"
    "v_mfma_f32_16x16x32_bf16 v[124:127], v[192:195], a[204:207], v[124:127]\n"
    "s_waitcnt vmcnt(19)\n"
    "s_barrier\n"
    "s_waitcnt lgkmcnt(2)\n"
    "v_mfma_f32_16x16x32_bf16 v[144:147], v[204:207], a[192:195], v[144:147]\n"
    "ds_read_b64_tr_b16 v[192:193], v217 offset:61184\n"
    "v_mfma_f32_16x16x32_bf16 v[144:147], v[208:211], a[208:211], v[144:147]\n"
    "s_waitcnt lgkmcnt(1)\n"
    "s_add_u32 m0, s56, 29568\n"
    "ds_read_b64_tr_b16 v[194:195], v217 offset:61440\n"
    "v_mfma_f32_16x16x32_bf16 v[144:147], v[212:215], a[224:227], v[144:147]\n"
    "s_add_u32 s45, %5, 28672\n"
    "ds_read_b64_tr_b16 v[196:197], v217 offset:65408\n"
    "buffer_load_dwordx4 v232, s[36:39], s45 offen lds\n"
    "v_mfma_f32_16x16x32_bf16 v[148:151], v[212:215], a[228:231], v[148:151]\n"
    "ds_read_b64_tr_b16 v[198:199], v218 offset:128\n"
    "v_mfma_f32_16x16x32_bf16 v[148:151], v[208:211], a[212:215], v[148:151]\n"
    "ds_read_b64_tr_b16 v[200:201], v218 offset:4096\n"
    "v_mfma_f32_16x16x32_bf16 v[148:151], v[204:207], a[196:199], v[148:151]\n"
    "ds_read_b64_tr_b16 v[202:203], v218 offset:4352\n"
    "v_mfma_f32_16x16x32_bf16 v[152:155], v[204:207], a[200:203], v[152:155]\n"
    "s_add_u32 m0, s56, 33792\n"
    "s_add_u32 s46, %5, 32768\n"
    "v_mfma_f32_16x16x32_bf16 v[152:155], v[208:211], a[216:219], v[152:155]\n"
    "buffer_load_dwordx4 v232, s[36:39], s46 offen lds\n"
    "v_mfma_f32_16x16x32_bf16 v[152:155], v[212:215], a[232:235], v[152:155]\n"
    "v_mfma_f32_16x16x32_bf16 v[156:159], v[212:215], a[236:239], v[156:159]\n"
    "v_mfma_f32_16x16x32_bf16 v[156:159], v[208:211], a[220:223], v[156:159]\n"
    "v_mfma_f32_16x16x32_bf16 v[156:159], v[204:207], a[204:207], v[156:159]\n"
    "s_waitcnt vmcnt(20) lgkmcnt(2)\n"
    "s_add_u32 m0, s56, 38016\n"
    "s_add_u32 s47, %4, 128\n"
    "v_mfma_f32_16x16x32_bf16 v[32:35], v[192:195], a[240:243], v[32:35]\n"
    "s_waitcnt vmcnt(19)\n"
    "ds_read_b64_tr_b16 v[204:205], v218 offset:8320\n"
    "buffer_load_dwordx4 v219, s[92:95], s47 offen lds\n"
    "v_mfma_f32_16x16x32_bf16 v[32:35], v[196:199], v[0:3], v[32:35]\n"
    "s_waitcnt vmcnt(19) lgkmcnt(1)\n"
    "ds_read_b64_tr_b16 v[206:207], v218 offset:8576\n"
    "v_mfma_f32_16x16x32_bf16 v[32:35], v[200:203], v[16:19], v[32:35]\n"
    "s_waitcnt vmcnt(16)\n"
    "ds_read_b64_tr_b16 v[208:209], v218 offset:12544\n"
    "v_mfma_f32_16x16x32_bf16 v[36:39], v[200:203], v[20:23], v[36:39]\n"
    "ds_read_b64_tr_b16 v[210:211], v218 offset:12800\n"
    "v_mfma_f32_16x16x32_bf16 v[36:39], v[196:199], v[4:7], v[36:39]\n"
    "s_add_u32 m0, s56, 42240\n"
    "s_add_u32 s48, %4, 295040\n"
    "ds_read_b64_tr_b16 v[212:213], v218 offset:16768\n"
    "v_mfma_f32_16x16x32_bf16 v[36:39], v[192:195], a[244:247], v[36:39]\n"
    "s_waitcnt vmcnt(15)\n"
    "ds_read_b64_tr_b16 v[214:215], v218 offset:17024\n"
    "buffer_load_dwordx4 v219, s[92:95], s48 offen lds\n"
    "v_mfma_f32_16x16x32_bf16 v[40:43], v[192:195], a[248:251], v[40:43]\n"
    "s_waitcnt vmcnt(15)\n"
    "v_mfma_f32_16x16x32_bf16 v[40:43], v[196:199], v[8:11], v[40:43]\n"
    "s_waitcnt vmcnt(14)\n"
    "v_mfma_f32_16x16x32_bf16 v[40:43], v[200:203], v[24:27], v[40:43]\n"
    "s_waitcnt vmcnt(11)\n"
    "v_mfma_f32_16x16x32_bf16 v[44:47], v[200:203], v[28:31], v[44:47]\n"
    "s_add_u32 m0, s56, 46464\n"
    "s_add_u32 s49, %4, 589952\n"
    "v_mfma_f32_16x16x32_bf16 v[44:47], v[196:199], v[12:15], v[44:47]\n"
    "buffer_load_dwordx4 v219, s[92:95], s49 offen lds\n"
    "v_mfma_f32_16x16x32_bf16 v[44:47], v[192:195], a[252:255], v[44:47]\n"
    "s_waitcnt lgkmcnt(2)\n"
    "v_mfma_f32_16x16x32_bf16 v[48:51], v[204:207], a[240:243], v[48:51]\n"
    "ds_read_b64_tr_b16 v[192:193], v217 offset:61216\n"
    "v_mfma_f32_16x16x32_bf16 v[48:51], v[208:211], v[0:3], v[48:51]\n"
    "s_waitcnt lgkmcnt(1)\n"
    "ds_read_b64_tr_b16 v[194:195], v217 offset:61472\n"
    "v_mfma_f32_16x16x32_bf16 v[48:51], v[212:215], v[16:19], v[48:51]\n"
    "s_add_u32 m0, s56, 50688\n"
    "s_add_u32 s44, %4, 128\n"
    "ds_read_b64_tr_b16 v[196:197], v217 offset:65440\n"
    "v_mfma_f32_16x16x32_bf16 v[52:55], v[212:215], v[20:23], v[52:55]\n"
    "ds_read_b64_tr_b16 v[198:199], v218 offset:160\n"
    "buffer_load_dwordx4 v219, s[96:99], s44 offen lds\n"
    "v_mfma_f32_16x16x32_bf16 v[52:55], v[208:211], v[4:7], v[52:55]\n"
    "ds_read_b64_tr_b16 v[200:201], v218 offset:4128\n"
    "v_mfma_f32_16x16x32_bf16 v[52:55], v[204:207], a[244:247], v[52:55]\n"
    "ds_read_b64_tr_b16 v[202:203], v218 offset:4384\n"
    "v_mfma_f32_16x16x32_bf16 v[56:59], v[204:207], a[248:251], v[56:59]\n"
    "v_mfma_f32_16x16x32_bf16 v[56:59], v[208:211], v[8:11], v[56:59]\n"
    "s_add_u32 m0, s56, 54912\n"
    "s_add_u32 s45, %4, 295040\n"
    "v_mfma_f32_16x16x32_bf16 v[56:59], v[212:215], v[24:27], v[56:59]\n"
    "buffer_load_dwordx4 v219, s[96:99], s45 offen lds\n"
    "v_mfma_f32_16x16x32_bf16 v[60:63], v[212:215], v[28:31], v[60:63]\n"
    "v_mfma_f32_16x16x32_bf16 v[60:63], v[208:211], v[12:15], v[60:63]\n"
    "v_mfma_f32_16x16x32_bf16 v[60:63], v[204:207], a[252:255], v[60:63]\n"
    "s_waitcnt lgkmcnt(2)\n"
    "v_mfma_f32_16x16x32_bf16 v[64:67], v[192:195], a[240:243], v[64:67]\n"
    "s_add_u32 m0, s56, 59136\n"
    "s_add_u32 s46, %4, 589952\n"
    "ds_read_b64_tr_b16 v[204:205], v218 offset:8352\n"
    "v_mfma_f32_16x16x32_bf16 v[64:67], v[196:199], v[0:3], v[64:67]\n"
    "s_waitcnt lgkmcnt(1)\n"
    "ds_read_b64_tr_b16 v[206:207], v218 offset:8608\n"
    "buffer_load_dwordx4 v219, s[96:99], s46 offen lds\n"
    "v_mfma_f32_16x16x32_bf16 v[64:67], v[200:203], v[16:19], v[64:67]\n"
    "ds_read_b64_tr_b16 v[208:209], v218 offset:12576\n"
    "v_mfma_f32_16x16x32_bf16 v[68:71], v[200:203], v[20:23], v[68:71]\n"
    "ds_read_b64_tr_b16 v[210:211], v218 offset:12832\n"
    "v_mul_f32 v32, v32, v236\n"
    "v_mfma_f32_16x16x32_bf16 v[68:71], v[196:199], v[4:7], v[68:71]\n"
    "ds_read_b64_tr_b16 v[212:213], v218 offset:16800\n"
    "v_mul_f32 v33, v33, v236\n"
    "v_mfma_f32_16x16x32_bf16 v[68:71], v[192:195], a[244:247], v[68:71]\n"
    "s_add_u32 m0, s56, 63360\n"
    "s_add_u32 s47, %4, 884864\n"
    "ds_read_b64_tr_b16 v[214:215], v218 offset:17056\n"
    "v_mfma_f32_16x16x32_bf16 v[72:75], v[192:195], a[248:251], v[72:75]\n"
    "buffer_load_dwordx4 v219, s[92:95], s47 offen lds\n"
    "v_mul_f32 v34, v34, v236\n"
    "v_mfma_f32_16x16x32_bf16 v[72:75], v[196:199], v[8:11], v[72:75]\n"
    "v_mul_f32 v35, v35, v236\n"
    "v_mul_f32 v36, v36, v237\n"
    "v_mfma_f32_16x16x32_bf16 v[72:75], v[200:203], v[24:27], v[72:75]\n"
    "v_mul_f32 v37, v37, v237\n"
    "v_mul_f32 v38, v38, v237\n"
    "v_mfma_f32_16x16x32_bf16 v[76:79], v[200:203], v[28:31], v[76:79]\n"
    "v_mul_f32 v39, v39, v237\n"
    "v_mfma_f32_16x16x32_bf16 v[76:79], v[196:199], v[12:15], v[76:79]\n"
    "s_add_u32 m0, s56, 67584\n"
    "s_add_u32 s48, %4, 1327232\n"
    "v_mfma_f32_16x16x32_bf16 v[76:79], v[192:195], a[252:255], v[76:79]\n"
    "s_waitcnt lgkmcnt(2)\n"
    "buffer_load_dwordx4 v219, s[92:95], s48 offen lds\n"
    "v_mfma_f32_16x16x32_bf16 v[80:83], v[204:207], a[240:243], v[80:83]\n"
    "ds_read_b64_tr_b16 v[192:193], v217 offset:61248\n"
    "v_mfma_f32_16x16x32_bf16 v[80:83], v[208:211], v[0:3], v[80:83]\n"
    "s_waitcnt lgkmcnt(1)\n"
    "ds_read_b64_tr_b16 v[194:195], v217 offset:61504\n"
    "v_mfma_f32_16x16x32_bf16 v[80:83], v[212:215], v[16:19], v[80:83]\n"
    "ds_read_b64_tr_b16 v[196:197], v217 offset:65472\n"
    "v_mfma_f32_16x16x32_bf16 v[84:87], v[212:215], v[20:23], v[84:87]\n"
    "s_add_u32 m0, s56, 71808\n"
    "s_add_u32 s49, %4, 1622144\n"
    "ds_read_b64_tr_b16 v[198:199], v218 offset:192\n"
    "v_mfma_f32_16x16x32_bf16 v[84:87], v[208:211], v[4:7], v[84:87]\n"
    "ds_read_b64_tr_b16 v[200:201], v218 offset:4160\n"
    "buffer_load_dwordx4 v219, s[92:95], s49 offen lds\n"
    "v_mfma_f32_16x16x32_bf16 v[84:87], v[204:207], a[244:247], v[84:87]\n"
    "ds_read_b64_tr_b16 v[202:203], v218 offset:4416\n"
    "v_mfma_f32_16x16x32_bf16 v[88:91], v[204:207], a[248:251], v[88:91]\n"
    "v_exp_f32 v32, v32\n"
    "v_mfma_f32_16x16x32_bf16 v[88:91], v[208:211], v[8:11], v[88:91]\n"
    "v_exp_f32 v33, v33\n"
    "v_mfma_f32_16x16x32_bf16 v[88:91], v[212:215], v[24:27], v[88:91]\n"
    "s_add_u32 m0, s56, 76032\n"
    "s_add_u32 s44, %4, 884864\n"
    "v_fma_f32 v32, v32, v240, v240\n"
    "v_mfma_f32_16x16x32_bf16 v[92:95], v[212:215], v[28:31], v[92:95]\n"
    "buffer_load_dwordx4 v219, s[96:99], s44 offen lds\n"
    "v_fma_f32 v33, v33, v240, v240\n"
    "v_mfma_f32_16x16x32_bf16 v[92:95], v[208:211], v[12:15], v[92:95]\n"
    "v_exp_f32 v34, v34\n"
    "v_mfma_f32_16x16x32_bf16 v[92:95], v[204:207], a[252:255], v[92:95]\n"
    "s_waitcnt lgkmcnt(2)\n"
    "v_exp_f32 v35, v35\n"
    "v_mfma_f32_16x16x32_bf16 v[96:99], v[192:195], a[240:243], v[96:99]\n"
    "ds_read_b64_tr_b16 v[204:205], v218 offset:8384\n"
    "v_fma_f32 v34, v34, v240, v240\n"
    "v_mfma_f32_16x16x32_bf16 v[96:99], v[196:199], v[0:3], v[96:99]\n"
    "s_waitcnt lgkmcnt(1)\n"
    "ds_read_b64_tr_b16 v[206:207], v218 offset:8640\n"
    "v_fma_f32 v35, v35, v240, v240\n"
    "v_mfma_f32_16x16x32_bf16 v[96:99], v[200:203], v[16:19], v[96:99]\n"
    "ds_read_b64_tr_b16 v[208:209], v218 offset:12608\n"
    "buffer_store_dwordx4 v[252:255], v232, s[36:39], s53 offen offset:1024 sc1\n"
    "v_mfma_f32_16x16x32_bf16 v[100:103], v[200:203], v[20:23], v[100:103]\n"
    "ds_read_b64_tr_b16 v[210:211], v218 offset:12864\n"
    "v_mfma_f32_16x16x32_bf16 v[100:103], v[196:199], v[4:7], v[100:103]\n"
    "ds_read_b64_tr_b16 v[212:213], v218 offset:16832\n"
    "v_mfma_f32_16x16x32_bf16 v[100:103], v[192:195], a[244:247], v[100:103]\n"
    "ds_read_b64_tr_b16 v[214:215], v218 offset:17088\n"
    "v_mfma_f32_16x16x32_bf16 v[104:107], v[192:195], a[248:251], v[104:107]\n"
    "s_add_u32 m0, s56, 80256\n"
    "s_add_u32 s45, %4, 1327232\n"
    "v_mfma_f32_16x16x32_bf16 v[104:107], v[196:199], v[8:11], v[104:107]\n"
    "buffer_load_dwordx4 v219, s[96:99], s45 offen lds\n"
    "v_mfma_f32_16x16x32_bf16 v[104:107], v[200:203], v[24:27], v[104:107]\n"
    "v_exp_f32 v36, v36\n"
    "v_mfma_f32_16x16x32_bf16 v[108:111], v[200:203], v[28:31], v[108:111]\n"
    "v_exp_f32 v37, v37\n"
    "v_mfma_f32_16x16x32_bf16 v[108:111], v[196:199], v[12:15], v[108:111]\n"
    "v_exp_f32 v38, v38\n"
    "v_mfma_f32_16x16x32_bf16 v[108:111], v[192:195], a[252:255], v[108:111]\n"
    "s_waitcnt lgkmcnt(2)\n"
    "s_add_u32 m0, s56, 84480\n"
    "s_add_u32 s46, %4, 1622144\n"
    "v_mfma_f32_16x16x32_bf16 v[112:115], v[204:207], a[240:243], v[112:115]\n"
    "ds_read_b64_tr_b16 v[192:193], v217 offset:61280\n"
    "buffer_load_dwordx4 v219, s[96:99], s46 offen lds\n"
    "v_mfma_f32_16x16x32_bf16 v[112:115], v[208:211], v[0:3], v[112:115]\n"
    "s_waitcnt lgkmcnt(1)\n"
    "ds_read_b64_tr_b16 v[194:195], v217 offset:61536\n"
    "v_fma_f32 v36, v36, v241, v241\n"
    "v_mfma_f32_16x16x32_bf16 v[112:115], v[212:215], v[16:19], v[112:115]\n"
    "ds_read_b64_tr_b16 v[196:197], v217 offset:65504\n"
    "v_fma_f32 v37, v37, v241, v241\n"
    "v_mfma_f32_16x16x32_bf16 v[116:119], v[212:215], v[20:23], v[116:119]\n"
    "ds_read_b64_tr_b16 v[198:199], v218 offset:224\n"
    "v_fma_f32 v38, v38, v241, v241\n"
    "v_mfma_f32_16x16x32_bf16 v[116:119], v[208:211], v[4:7], v[116:119]\n"
    "s_add_u32 m0, s56, 88704\n"
    "s_add_u32 s47, %4, 1917056\n"
    "ds_read_b64_tr_b16 v[200:201], v218 offset:4192\n"
    "v_mfma_f32_16x16x32_bf16 v[116:119], v[204:207], a[244:247], v[116:119]\n"
    "s_add_u32 s57, %10, 0\n"
    "ds_read_b64_tr_b16 v[202:203], v218 offset:4448\n"
    "buffer_load_dwordx4 v219, s[92:95], s47 offen lds\n"
    "v_mfma_f32_16x16x32_bf16 v[120:123], v[204:207], a[248:251], v[120:123]\n"
    "v_exp_f32 v39, v39\n"
    "v_mfma_f32_16x16x32_bf16 v[120:123], v[208:211], v[8:11], v[120:123]\n"
    "v_fma_f32 v39, v39, v241, v241\n"
    "v_mfma_f32_16x16x32_bf16 v[120:123], v[212:215], v[24:27], v[120:123]\n"
    "v_rcp_f32 v32, v32\n"
    "v_mfma_f32_16x16x32_bf16 v[124:127], v[212:215], v[28:31], v[124:127]\n"
    "s_add_u32 m0, s56, 92928\n"
    "s_add_u32 s48, %4, 2211968\n"
    "v_mul_f32 v32, v32, v48\n"
    "v_mfma_f32_16x16x32_bf16 v[124:127], v[208:211], v[12:15], v[124:127]\n"
    "buffer_load_dwordx4 v219, s[92:95], s48 offen lds\n"
    "v_mfma_f32_16x16x32_bf16 v[124:127], v[204:207], a[252:255], v[124:127]\n"
    "s_waitcnt lgkmcnt(2)\n"
    "v_rcp_f32 v33, v33\n"
    "v_mfma_f32_16x16x32_bf16 v[128:131], v[192:195], a[240:243], v[128:131]\n"
    "ds_read_b64_tr_b16 v[204:205], v218 offset:8416\n"
    "v_mul_f32 v33, v33, v49\n"
    "v_mfma_f32_16x16x32_bf16 v[128:131], v[196:199], v[0:3], v[128:131]\n"
    "s_waitcnt lgkmcnt(1)\n"
    "ds_read_b64_tr_b16 v[206:207], v218 offset:8672\n"
    "v_cvt_pk_bf16_f32 v48, v32, v33\n"
    "v_mfma_f32_16x16x32_bf16 v[128:131], v[200:203], v[16:19], v[128:131]\n"
    "s_add_u32 m0, s56, 97152\n"
    "s_add_u32 s49, %4, 2654336\n"
    "ds_read_b64_tr_b16 v[208:209], v218 offset:12640\n"
    "v_mfma_f32_16x16x32_bf16 v[132:135], v[200:203], v[20:23], v[132:135]\n"
    "ds_read_b64_tr_b16 v[210:211], v218 offset:12896\n"
    "buffer_load_dwordx4 v219, s[92:95], s49 offen lds\n"
    "v_mfma_f32_16x16x32_bf16 v[132:135], v[196:199], v[4:7], v[132:135]\n"
    "ds_read_b64_tr_b16 v[212:213], v218 offset:16864\n"
    "v_mul_f32 v40, v40, v238\n"
    "v_mfma_f32_16x16x32_bf16 v[132:135], v[192:195], a[244:247], v[132:135]\n"
    "ds_read_b64_tr_b16 v[214:215], v218 offset:17120\n"
    "v_mul_f32 v41, v41, v238\n"
    "v_mfma_f32_16x16x32_bf16 v[136:139], v[192:195], a[248:251], v[136:139]\n"
    "v_rcp_f32 v34, v34\n"
    "v_mfma_f32_16x16x32_bf16 v[136:139], v[196:199], v[8:11], v[136:139]\n"
    "s_add_u32 m0, s56, 101376\n"
    "s_add_u32 s44, %4, 1917056\n"
    "v_mul_f32 v34, v34, v50\n"
    "v_mfma_f32_16x16x32_bf16 v[136:139], v[200:203], v[24:27], v[136:139]\n"
    "buffer_load_dwordx4 v219, s[96:99], s44 offen lds\n"
    "v_mul_f32 v42, v42, v238\n"
    "v_mfma_f32_16x16x32_bf16 v[140:143], v[200:203], v[28:31], v[140:143]\n"
    "v_rcp_f32 v35, v35\n"
    "v_mfma_f32_16x16x32_bf16 v[140:143], v[196:199], v[12:15], v[140:143]\n"
    "v_rcp_f32 v36, v36\n"
    "v_mfma_f32_16x16x32_bf16 v[140:143], v[192:195], a[252:255], v[140:143]\n"
    "s_waitcnt lgkmcnt(2)\n"
    "v_rcp_f32 v37, v37\n"
    "v_mfma_f32_16x16x32_bf16 v[144:147], v[204:207], a[240:243], v[144:147]\n"
    "s_add_u32 m0, s56, 105600\n"
    "s_add_u32 s45, %4, 2211968\n"
    "ds_read_b128 v[192:195], v233 offset:0\n"
    "v_mfma_f32_16x16x32_bf16 v[144:147], v[208:211], v[0:3], v[144:147]\n"
    "s_waitcnt lgkmcnt(1)\n"
    "ds_read_b128 v[196:199], v233 offset:1056\n"
    "buffer_load_dwordx4 v219, s[96:99], s45 offen lds\n"
    "v_mfma_f32_16x16x32_bf16 v[144:147], v[212:215], v[16:19], v[144:147]\n"
    "ds_read_b128 v[200:203], v233 offset:2112\n"
    "v_mul_f32 v35, v35, v51\n"
    "v_mfma_f32_16x16x32_bf16 v[148:151], v[212:215], v[20:23], v[148:151]\n"
    "v_rcp_f32 v38, v38\n"
    "v_mfma_f32_16x16x32_bf16 v[148:151], v[208:211], v[4:7], v[148:151]\n"
    "v_rcp_f32 v39, v39\n"
    "v_mfma_f32_16x16x32_bf16 v[148:151], v[204:207], a[244:247], v[148:151]\n"
    "s_add_u32 m0, s56, 109824\n"
    "s_add_u32 s46, %4, 2654336\n"
    "v_mul_f32 v36, v36, v52\n"
    "v_mfma_f32_16x16x32_bf16 v[152:155], v[204:207], a[248:251], v[152:155]\n"
    "buffer_load_dwordx4 v219, s[96:99], s46 offen lds\n"
    "v_mul_f32 v37, v37, v53\n"
    "v_mfma_f32_16x16x32_bf16 v[152:155], v[208:211], v[8:11], v[152:155]\n"
    "v_mul_f32 v38, v38, v54\n"
    "v_mul_f32 v39, v39, v55\n"
    "v_mfma_f32_16x16x32_bf16 v[152:155], v[212:215], v[24:27], v[152:155]\n"
    "v_cvt_pk_bf16_f32 v49, v34, v35\n"
    "v_cvt_pk_bf16_f32 v50, v36, v37\n"
    "v_mfma_f32_16x16x32_bf16 v[156:159], v[212:215], v[28:31], v[156:159]\n"
    "v_cvt_pk_bf16_f32 v51, v38, v39\n"
    "v_permlane16_swap_b32 v48, v50\n"
    "v_mfma_f32_16x16x32_bf16 v[156:159], v[208:211], v[12:15], v[156:159]\n"
    "v_permlane16_swap_b32 v49, v51\n"
    "v_mul_f32 v43, v43, v238\n"
    "v_mfma_f32_16x16x32_bf16 v[156:159], v[204:207], a[252:255], v[156:159]\n"
    "buffer_store_dwordx4 v[48:51], v251, s[36:39], s57 offen sc1\n"
    "v_mul_f32 v44, v44, v239\n"
    "s_waitcnt vmcnt(17)\n"
    "s_barrier\n"
    "s_waitcnt lgkmcnt(0)\n"
    "v_mfma_f32_16x16x32_bf16 v[160:163], v[192:195], a[0:3], 0\n"
    "s_add_u32 m0, s56, 114048\n"
    "s_add_u32 s47, %4, 2949248\n"
    "ds_read_b128 v[204:207], v233 offset:3168\n"
    "v_mfma_f32_16x16x32_bf16 v[160:163], v[196:199], a[16:19], v[160:163]\n"
    "ds_read_b128 v[208:211], v233 offset:4224\n"
    "buffer_load_dwordx4 v219, s[92:95], s47 offen lds\n"
    "v_mfma_f32_16x16x32_bf16 v[160:163], v[200:203], a[32:35], v[160:163]\n"
    "ds_read_b128 v[212:215], v233 offset:5280\n"
    "v_mul_f32 v45, v45, v239\n"
    "v_mfma_f32_16x16x32_bf16 v[164:167], v[200:203], a[36:39], 0\n"
    "v_mul_f32 v46, v46, v239\n"
    "v_mul_f32 v47, v47, v239\n"
    "v_mfma_f32_16x16x32_bf16 v[164:167], v[196:199], a[20:23], v[164:167]\n"
    "v_exp_f32 v40, v40\n"
    "v_mfma_f32_16x16x32_bf16 v[164:167], v[192:195], a[4:7], v[164:167]\n"
    "v_exp_f32 v41, v41\n"
    "v_mfma_f32_16x16x32_bf16 v[168:171], v[192:195], a[8:11], 0\n"
    "s_add_u32 m0, s56, 118272\n"
    "s_add_u32 s48, %4, 3244160\n"
    "v_fma_f32 v40, v40, v242, v242\n"
    "v_mfma_f32_16x16x32_bf16 v[168:171], v[196:199], a[24:27], v[168:171]\n"
    "buffer_load_dwordx4 v219, s[92:95], s48 offen lds\n"
    "v_fma_f32 v41, v41, v242, v242\n"
    "v_mfma_f32_16x16x32_bf16 v[168:171], v[200:203], a[40:43], v[168:171]\n"
    "v_exp_f32 v42, v42\n"
    "v_mfma_f32_16x16x32_bf16 v[172:175], v[200:203], a[44:47], 0\n"
    "v_exp_f32 v43, v43\n"
    "v_mfma_f32_16x16x32_bf16 v[172:175], v[196:199], a[28:31], v[172:175]\n"
    "v_exp_f32 v44, v44\n"
    "v_mfma_f32_16x16x32_bf16 v[172:175], v[192:195], a[12:15], v[172:175]\n"
    "s_waitcnt lgkmcnt(0)\n"
    "v_exp_f32 v45, v45\n"
    "v_mfma_f32_16x16x32_bf16 v[176:179], v[204:207], a[0:3], 0\n"
    "ds_read_b128 v[192:195], v233 offset:6336\n"
    "v_fma_f32 v42, v42, v242, v242\n"
    "v_mfma_f32_16x16x32_bf16 v[176:179], v[208:211], a[16:19], v[176:179]\n"
    "ds_read_b128 v[196:199], v233 offset:7392\n"
    "v_fma_f32 v43, v43, v242, v242\n"
    "v_mfma_f32_16x16x32_bf16 v[176:179], v[212:215], a[32:35], v[176:179]\n"
    "s_add_u32 m0, s56, 122496\n"
    "s_add_u32 s49, %4, 3539072\n"
    "ds_read_b128 v[200:203], v233 offset:8448\n"
    "v_mfma_f32_16x16x32_bf16 v[180:183], v[212:215], a[36:39], 0\n"
    "buffer_load_dwordx4 v219, s[92:95], s49 offen lds\n"
    "v_fma_f32 v44, v44, v243, v243\n"
    "v_mfma_f32_16x16x32_bf16 v[180:183], v[208:211], a[20:23], v[180:183]\n"
    "v_exp_f32 v46, v46\n"
    "v_mfma_f32_16x16x32_bf16 v[180:183], v[204:207], a[4:7], v[180:183]\n"
    "v_exp_f32 v47, v47\n"
    "v_mfma_f32_16x16x32_bf16 v[184:187], v[204:207], a[8:11], 0\n"
    "v_fma_f32 v45, v45, v243, v243\n"
    "v_fma_f32 v46, v46, v243, v243\n"
    "v_mfma_f32_16x16x32_bf16 v[184:187], v[208:211], a[24:27], v[184:187]\n"
    "v_fma_f32 v47, v47, v243, v243\n"
    "v_mul_f32 v64, v64, v236\n"
    "v_mfma_f32_16x16x32_bf16 v[184:187], v[212:215], a[40:43], v[184:187]\n"
    "v_rcp_f32 v40, v40\n"
    "v_mfma_f32_16x16x32_bf16 v[188:191], v[212:215], a[44:47], 0\n"
    "v_rcp_f32 v41, v41\n"
    "v_mfma_f32_16x16x32_bf16 v[188:191], v[208:211], a[28:31], v[188:191]\n"
    "s_add_u32 m0, s56, 126720\n"
    "s_add_u32 s44, %4, 2949248\n"
    "v_mul_f32 v40, v40, v56\n"
    "v_mfma_f32_16x16x32_bf16 v[188:191], v[204:207], a[12:15], v[188:191]\n"
    "s_waitcnt lgkmcnt(0)\n"
    "buffer_load_dwordx4 v219, s[96:99], s44 offen lds\n"
    "v_mul_f32 v41, v41, v57\n"
    "v_mfma_f32_16x16x32_bf16 v[160:163], v[192:195], a[48:51], v[160:163]\n"
    "ds_read_b128 v[204:207], v233 offset:9504\n"
    "v_cvt_pk_bf16_f32 v56, v40, v41\n"
    "v_mfma_f32_16x16x32_bf16 v[160:163], v[196:199], a[64:67], v[160:163]\n"
    "s_add_u32 s58, %10, 294912\n"
    "ds_read_b128 v[208:211], v233 offset:10560\n"
    "v_mul_f32 v65, v65, v236\n"
    "v_mfma_f32_16x16x32_bf16 v[160:163], v[200:203], a[80:83], v[160:163]\n"
    "ds_read_b128 v[212:215], v233 offset:11616\n"
    "v_mul_f32 v66, v66, v236\n"
    "v_mfma_f32_16x16x32_bf16 v[164:167], v[200:203], a[84:87], v[164:167]\n"
    "v_rcp_f32 v42, v42\n"
    "v_mfma_f32_16x16x32_bf16 v[164:167], v[196:199], a[68:71], v[164:167]\n"
    "v_rcp_f32 v43, v43\n"
    "v_mfma_f32_16x16x32_bf16 v[164:167], v[192:195], a[52:55], v[164:167]\n"
    "v_rcp_f32 v44, v44\n"
    "v_mfma_f32_16x16x32_bf16 v[168:171], v[192:195], a[56:59], v[168:171]\n"
    "s_add_u32 s54, %19, 8192\n"
    "v_rcp_f32 v45, v45\n"
    "v_mfma_f32_16x16x32_bf16 v[168:171], v[196:199], a[72:75], v[168:171]\n"
    "buffer_store_dwordx4 v[252:255], v232, s[36:39], s54 offen offset:0 sc1\n"
    "v_mul_f32 v42, v42, v58\n"
    "v_mfma_f32_16x16x32_bf16 v[168:171], v[200:203], a[88:91], v[168:171]\n"
    "v_rcp_f32 v46, v46\n"
    "v_mfma_f32_16x16x32_bf16 v[172:175], v[200:203], a[92:95], v[172:175]\n"
    "v_rcp_f32 v47, v47\n"
    "v_mfma_f32_16x16x32_bf16 v[172:175], v[196:199], a[76:79], v[172:175]\n"
    "v_mul_f32 v43, v43, v59\n"
    "v_mul_f32 v44, v44, v60\n"
    "v_mfma_f32_16x16x32_bf16 v[172:175], v[192:195], a[60:63], v[172:175]\n"
    "s_waitcnt lgkmcnt(0)\n"
    "v_mul_f32 v45, v45, v61\n"
    "v_mul_f32 v46, v46, v62\n"
    "v_mfma_f32_16x16x32_bf16 v[176:179], v[204:207], a[48:51], v[176:179]\n"
    "ds_read_b128 v[192:195], v233 offset:12672\n"
    "v_mul_f32 v47, v47, v63\n"
    "v_mfma_f32_16x16x32_bf16 v[176:179], v[208:211], a[64:67], v[176:179]\n"
    "ds_read_b128 v[196:199], v233 offset:13728\n"
    "v_cvt_pk_bf16_f32 v57, v42, v43\n"
    "v_mfma_f32_16x16x32_bf16 v[176:179], v[212:215], a[80:83], v[176:179]\n"
    "s_add_u32 m0, s56, 130944\n"
    "s_add_u32 s45, %4, 3244160\n"
    "ds_read_b128 v[200:203], v233 offset:14784\n"
    "v_mfma_f32_16x16x32_bf16 v[180:183], v[212:215], a[84:87], v[180:183]\n"
    "buffer_load_dwordx4 v219, s[96:99], s45 offen lds\n"
    "v_cvt_pk_bf16_f32 v58, v44, v45\n"
    "v_mfma_f32_16x16x32_bf16 v[180:183], v[208:211], a[68:71], v[180:183]\n"
    "v_cvt_pk_bf16_f32 v59, v46, v47\n"
    "v_permlane16_swap_b32 v56, v58\n"
    "v_mfma_f32_16x16x32_bf16 v[180:183], v[204:207], a[52:55], v[180:183]\n"
    "v_permlane16_swap_b32 v57, v59\n"
    "v_mul_f32 v67, v67, v236\n"
    "v_mfma_f32_16x16x32_bf16 v[184:187], v[204:207], a[56:59], v[184:187]\n"
    "buffer_store_dwordx4 v[56:59], v251, s[36:39], s58 offen sc1\n"
    "v_mul_f32 v68, v68, v237\n"
    "v_mfma_f32_16x16x32_bf16 v[184:187], v[208:211], a[72:75], v[184:187]\n"
    "v_mul_f32 v69, v69, v237\n"
    "v_mul_f32 v70, v70, v237\n"
    "v_mfma_f32_16x16x32_bf16 v[184:187], v[212:215], a[88:91], v[184:187]\n"
    "v_mul_f32 v71, v71, v237\n"
    "v_mfma_f32_16x16x32_bf16 v[188:191], v[212:215], a[92:95], v[188:191]\n"
    "v_exp_f32 v64, v64\n"
    "v_mfma_f32_16x16x32_bf16 v[188:191], v[208:211], a[76:79], v[188:191]\n"
    "s_add_u32 m0, s56, 135168\n"
    "s_add_u32 s46, %4, 3539072\n"
    "v_fma_f32 v64, v64, v240, v240\n"
    "v_mfma_f32_16x16x32_bf16 v[188:191], v[204:207], a[60:63], v[188:191]\n"
    "s_waitcnt lgkmcnt(0)\n"
    "buffer_load_dwordx4 v219, s[96:99], s46 offen lds\n"
    "v_mfma_f32_16x16x32_bf16 v[160:163], v[192:195], a[96:99], v[160:163]\n"
    "s_add_u32 s59, %10, 1024\n"
    "ds_read_b128 v[204:207], v233 offset:15840\n"
    "v_mfma_f32_16x16x32_bf16 v[160:163], v[196:199], a[112:115], v[160:163]\n"
    "ds_read_b128 v[208:211], v233 offset:16896\n"
    "v_mfma_f32_16x16x32_bf16 v[160:163], v[200:203], a[128:131], v[160:163]\n"
    "ds_read_b128 v[212:215], v233 offset:17952\n"
    "v_mul_f32 v72, v72, v238\n"
    "v_mfma_f32_16x16x32_bf16 v[164:167], v[200:203], a[132:135], v[164:167]\n"
    "v_exp_f32 v65, v65\n"
    "v_mfma_f32_16x16x32_bf16 v[164:167], v[196:199], a[116:119], v[164:167]\n"
    "v_exp_f32 v66, v66\n"
    "v_mfma_f32_16x16x32_bf16 v[164:167], v[192:195], a[100:103], v[164:167]\n"
    "v_exp_f32 v67, v67\n"
    "v_mfma_f32_16x16x32_bf16 v[168:171], v[192:195], a[104:107], v[168:171]\n"
    "s_add_u32 m0, s56, 139392\n"
    "s_add_u32 s47, %4, 3981440\n"
    "v_fma_f32 v65, v65, v240, v240\n"
    "v_mfma_f32_16x16x32_bf16 v[168:171], v[196:199], a[120:123], v[168:171]\n"
    "buffer_load_dwordx4 v219, s[92:95], s47 offen lds\n"
    "v_fma_f32 v66, v66, v240, v240\n"
    "v_mfma_f32_16x16x32_bf16 v[168:171], v[200:203], a[136:139], v[168:171]\n"
    "v_exp_f32 v68, v68\n"
    "v_mfma_f32_16x16x32_bf16 v[172:175], v[200:203], a[140:143], v[172:175]\n"
    "v_exp_f32 v69, v69\n"
    "v_mfma_f32_16x16x32_bf16 v[172:175], v[196:199], a[124:127], v[172:175]\n"
    "v_exp_f32 v70, v70\n"
    "v_mfma_f32_16x16x32_bf16 v[172:175], v[192:195], a[108:111], v[172:175]\n"
    "s_waitcnt lgkmcnt(0)\n"
    "v_exp_f32 v71, v71\n"
    "v_mfma_f32_16x16x32_bf16 v[176:179], v[204:207], a[96:99], v[176:179]\n"
    "ds_read_b128 v[192:195], v233 offset:19008\n"
    "v_fma_f32 v67, v67, v240, v240\n"
    "v_mfma_f32_16x16x32_bf16 v[176:179], v[208:211], a[112:115], v[176:179]\n"
    "ds_read_b128 v[196:199], v233 offset:20064\n"
    "v_fma_f32 v68, v68, v241, v241\n"
    "v_mfma_f32_16x16x32_bf16 v[176:179], v[212:215], a[128:131], v[176:179]\n"
    "s_add_u32 m0, s56, 143616\n"
    "s_add_u32 s48, %4, 4276352\n"
    "ds_read_b128 v[200:203], v233 offset:21120\n"
    "v_mfma_f32_16x16x32_bf16 v[180:183], v[212:215], a[132:135], v[180:183]\n"
    "buffer_load_dwordx4 v219, s[92:95], s48 offen lds\n"
    "v_fma_f32 v69, v69, v241, v241\n"
    "v_mfma_f32_16x16x32_bf16 v[180:183], v[208:211], a[116:119], v[180:183]\n"
    "v_fma_f32 v70, v70, v241, v241\n"
    "v_fma_f32 v71, v71, v241, v241\n"
    "v_mfma_f32_16x16x32_bf16 v[180:183], v[204:207], a[100:103], v[180:183]\n"
    "v_rcp_f32 v64, v64\n"
    "v_mfma_f32_16x16x32_bf16 v[184:187], v[204:207], a[104:107], v[184:187]\n"
    "v_rcp_f32 v65, v65\n"
    "v_mfma_f32_16x16x32_bf16 v[184:187], v[208:211], a[120:123], v[184:187]\n"
    "v_rcp_f32 v66, v66\n"
    "v_mfma_f32_16x16x32_bf16 v[184:187], v[212:215], a[136:139], v[184:187]\n"
    "v_rcp_f32 v67, v67\n"
    "v_mfma_f32_16x16x32_bf16 v[188:191], v[212:215], a[140:143], v[188:191]\n"
    "v_rcp_f32 v68, v68\n"
    "v_mfma_f32_16x16x32_bf16 v[188:191], v[208:211], a[124:127], v[188:191]\n"
    "s_add_u32 m0, s56, 147840\n"
    "s_add_u32 s49, %4, 4571264\n"
    "v_mul_f32 v64, v64, v80\n"
    "v_mfma_f32_16x16x32_bf16 v[188:191], v[204:207], a[108:111], v[188:191]\n"
    "s_waitcnt lgkmcnt(0)\n"
    "buffer_load_dwordx4 v219, s[92:95], s49 offen lds\n"
    "v_mul_f32 v65, v65, v81\n"
    "v_mfma_f32_16x16x32_bf16 v[160:163], v[192:195], a[144:147], v[160:163]\n"
    "ds_read_b128 v[204:207], v233 offset:22176\n"
    "v_mul_f32 v66, v66, v82\n"
    "v_mfma_f32_16x16x32_bf16 v[160:163], v[196:199], a[160:163], v[160:163]\n"
    "ds_read_b128 v[208:211], v233 offset:23232\n"
    "v_mul_f32 v67, v67, v83\n"
    "v_mfma_f32_16x16x32_bf16 v[160:163], v[200:203], a[176:179], v[160:163]\n"
    "ds_read_b128 v[212:215], v233 offset:24288\n"
    "v_mul_f32 v68, v68, v84\n"
    "v_mfma_f32_16x16x32_bf16 v[164:167], v[200:203], a[180:183], v[164:167]\n"
    "v_rcp_f32 v69, v69\n"
    "v_mfma_f32_16x16x32_bf16 v[164:167], v[196:199], a[164:167], v[164:167]\n"
    "v_rcp_f32 v70, v70\n"
    "v_mfma_f32_16x16x32_bf16 v[164:167], v[192:195], a[148:151], v[164:167]\n"
    "v_rcp_f32 v71, v71\n"
    "v_mfma_f32_16x16x32_bf16 v[168:171], v[192:195], a[152:155], v[168:171]\n"
    "v_mul_f32 v69, v69, v85\n"
    "v_mul_f32 v70, v70, v86\n"
    "v_mfma_f32_16x16x32_bf16 v[168:171], v[196:199], a[168:171], v[168:171]\n"
    "v_mul_f32 v71, v71, v87\n"
    "v_cvt_pk_bf16_f32 v80, v64, v65\n"
    "v_mfma_f32_16x16x32_bf16 v[168:171], v[200:203], a[184:187], v[168:171]\n"
    "v_cvt_pk_bf16_f32 v81, v66, v67\n"
    "v_cvt_pk_bf16_f32 v82, v68, v69\n"
    "v_mfma_f32_16x16x32_bf16 v[172:175], v[200:203], a[188:191], v[172:175]\n"
    "v_cvt_pk_bf16_f32 v83, v70, v71\n"
    "v_permlane16_swap_b32 v80, v82\n"
    "v_mfma_f32_16x16x32_bf16 v[172:175], v[196:199], a[172:175], v[172:175]\n"
    "v_permlane16_swap_b32 v81, v83\n"
    "v_mul_f32 v73, v73, v238\n"
    "v_mfma_f32_16x16x32_bf16 v[172:175], v[192:195], a[156:159], v[172:175]\n"
    "s_waitcnt lgkmcnt(0)\n"
    "buffer_store_dwordx4 v[80:83], v251, s[36:39], s59 offen sc1\n"
    "v_mul_f32 v74, v74, v238\n"
    "v_mfma_f32_16x16x32_bf16 v[176:179], v[204:207], a[144:147], v[176:179]\n"
    "ds_read_b128 v[192:195], v233 offset:25344\n"
    "v_mul_f32 v75, v75, v238\n"
    "v_mfma_f32_16x16x32_bf16 v[176:179], v[208:211], a[160:163], v[176:179]\n"
    "ds_read_b128 v[196:199], v233 offset:26400\n"
    "v_mul_f32 v76, v76, v239\n"
    "v_mfma_f32_16x16x32_bf16 v[176:179], v[212:215], a[176:179], v[176:179]\n"
    "ds_read_b128 v[200:203], v233 offset:27456\n"
    "v_mul_f32 v77, v77, v239\n"
    "v_mfma_f32_16x16x32_bf16 v[180:183], v[212:215], a[180:183], v[180:183]\n"
    "v_mul_f32 v78, v78, v239\n"
    "v_mul_f32 v79, v79, v239\n"
    "v_mfma_f32_16x16x32_bf16 v[180:183], v[208:211], a[164:167], v[180:183]\n"
    "v_exp_f32 v72, v72\n"
    "v_mfma_f32_16x16x32_bf16 v[180:183], v[204:207], a[148:151], v[180:183]\n"
    "v_exp_f32 v73, v73\n"
    "v_mfma_f32_16x16x32_bf16 v[184:187], v[204:207], a[152:155], v[184:187]\n"
    "v_exp_f32 v74, v74\n"
    "v_mfma_f32_16x16x32_bf16 v[184:187], v[208:211], a[168:171], v[184:187]\n"
    "v_exp_f32 v75, v75\n"
    "v_mfma_f32_16x16x32_bf16 v[184:187], v[212:215], a[184:187], v[184:187]\n"
    "v_exp_f32 v76, v76\n"
    "v_mfma_f32_16x16x32_bf16 v[188:191], v[212:215], a[188:191], v[188:191]\n"
    "v_exp_f32 v77, v77\n"
    "v_mfma_f32_16x16x32_bf16 v[188:191], v[208:211], a[172:175], v[188:191]\n"
    "v_exp_f32 v78, v78\n"
    "v_mfma_f32_16x16x32_bf16 v[188:191], v[204:207], a[156:159], v[188:191]\n"
    "s_waitcnt lgkmcnt(0)\n"
    "v_exp_f32 v79, v79\n"
    "v_mfma_f32_16x16x32_bf16 v[160:163], v[192:195], a[192:195], v[160:163]\n"
    "s_add_u32 s57, %10, 295936\n"
    "ds_read_b128 v[204:207], v233 offset:28512\n"
    "buffer_store_dwordx4 v[252:255], v232, s[36:39], s54 offen offset:1024 sc1\n"
    "v_mfma_f32_16x16x32_bf16 v[160:163], v[196:199], a[208:211], v[160:163]\n"
    "ds_read_b128 v[208:211], v233 offset:29568\n"
    "v_fma_f32 v72, v72, v242, v242\n"
    "v_mfma_f32_16x16x32_bf16 v[160:163], v[200:203], a[224:227], v[160:163]\n"
    "ds_read_b128 v[212:215], v233 offset:30624\n"
    "v_fma_f32 v73, v73, v242, v242\n"
    "v_mfma_f32_16x16x32_bf16 v[164:167], v[200:203], a[228:231], v[164:167]\n"
    "v_fma_f32 v74, v74, v242, v242\n"
    "v_fma_f32 v75, v75, v242, v242\n"
    "v_mfma_f32_16x16x32_bf16 v[164:167], v[196:199], a[212:215], v[164:167]\n"
    "v_fma_f32 v76, v76, v243, v243\n"
    "v_fma_f32 v77, v77, v243, v243\n"
    "v_mfma_f32_16x16x32_bf16 v[164:167], v[192:195], a[196:199], v[164:167]\n"
    "v_fma_f32 v78, v78, v243, v243\n"
    "v_fma_f32 v79, v79, v243, v243\n"
    "v_mfma_f32_16x16x32_bf16 v[168:171], v[192:195], a[200:203], v[168:171]\n"
    "v_rcp_f32 v72, v72\n"
    "v_mfma_f32_16x16x32_bf16 v[168:171], v[196:199], a[216:219], v[168:171]\n"
    "v_rcp_f32 v73, v73\n"
    "v_mfma_f32_16x16x32_bf16 v[168:171], v[200:203], a[232:235], v[168:171]\n"
    "v_rcp_f32 v74, v74\n"
    "v_mfma_f32_16x16x32_bf16 v[172:175], v[200:203], a[236:239], v[172:175]\n"
    "v_rcp_f32 v75, v75\n"
    "v_mfma_f32_16x16x32_bf16 v[172:175], v[196:199], a[220:223], v[172:175]\n"
    "v_rcp_f32 v76, v76\n"
    "v_mfma_f32_16x16x32_bf16 v[172:175], v[192:195], a[204:207], v[172:175]\n"
    "s_waitcnt lgkmcnt(0)\n"
    "v_rcp_f32 v77, v77\n"
    "v_mfma_f32_16x16x32_bf16 v[176:179], v[204:207], a[192:195], v[176:179]\n"
    "ds_read_b128 v[192:195], v233 offset:31680\n"
    "v_mul_f32 v72, v72, v88\n"
    "v_mfma_f32_16x16x32_bf16 v[176:179], v[208:211], a[208:211], v[176:179]\n"
    "ds_read_b128 v[196:199], v233 offset:32736\n"
    "v_mul_f32 v73, v73, v89\n"
    "v_mfma_f32_16x16x32_bf16 v[176:179], v[212:215], a[224:227], v[176:179]\n"
    "ds_read_b128 v[200:203], v233 offset:33792\n"
    "v_mul_f32 v74, v74, v90\n"
    "v_mfma_f32_16x16x32_bf16 v[180:183], v[212:215], a[228:231], v[180:183]\n"
    "v_rcp_f32 v78, v78\n"
    "v_mfma_f32_16x16x32_bf16 v[180:183], v[208:211], a[212:215], v[180:183]\n"
    "v_rcp_f32 v79, v79\n"
    "v_mfma_f32_16x16x32_bf16 v[180:183], v[204:207], a[196:199], v[180:183]\n"
    "v_mul_f32 v75, v75, v91\n"
    "v_mul_f32 v76, v76, v92\n"
    "v_mfma_f32_16x16x32_bf16 v[184:187], v[204:207], a[200:203], v[184:187]\n"
    "v_mul_f32 v77, v77, v93\n"
    "v_mul_f32 v78, v78, v94\n"
    "v_mfma_f32_16x16x32_bf16 v[184:187], v[208:211], a[216:219], v[184:187]\n"
    "v_mul_f32 v79, v79, v95\n"
    "v_cvt_pk_bf16_f32 v88, v72, v73\n"
    "v_mfma_f32_16x16x32_bf16 v[184:187], v[212:215], a[232:235], v[184:187]\n"
    "v_cvt_pk_bf16_f32 v89, v74, v75\n"
    "v_cvt_pk_bf16_f32 v90, v76, v77\n"
    "v_mfma_f32_16x16x32_bf16 v[188:191], v[212:215], a[236:239], v[188:191]\n"
    "v_cvt_pk_bf16_f32 v91, v78, v79\n"
    "v_permlane16_swap_b32 v88, v90\n"
    "v_mfma_f32_16x16x32_bf16 v[188:191], v[208:211], a[220:223], v[188:191]\n"
    "v_permlane16_swap_b32 v89, v91\n"
    "v_mul_f32 v96, v96, v236\n"
    "v_mfma_f32_16x16x32_bf16 v[188:191], v[204:207], a[204:207], v[188:191]\n"
    "s_waitcnt lgkmcnt(0)\n"
    "buffer_store_dwordx4 v[88:91], v251, s[36:39], s57 offen sc1\n"
    "v_mul_f32 v97, v97, v236\n"
    "v_mfma_f32_16x16x32_bf16 v[160:163], v[192:195], a[240:243], v[160:163]\n"
    "ds_read_b128 v[204:207], v233 offset:34848\n"
    "v_mul_f32 v98, v98, v236\n"
    "v_mfma_f32_16x16x32_bf16 v[160:163], v[196:199], v[0:3], v[160:163]\n"
    "ds_read_b128 v[208:211], v233 offset:35904\n"
    "v_mul_f32 v99, v99, v236\n"
    "v_mfma_f32_16x16x32_bf16 v[160:163], v[200:203], v[16:19], v[160:163]\n"
    "ds_read_b128 v[212:215], v233 offset:36960\n"
    "v_mul_f32 v100, v100, v237\n"
    "v_mfma_f32_16x16x32_bf16 v[164:167], v[200:203], v[20:23], v[164:167]\n"
    "v_mul_f32 v101, v101, v237\n"
    "v_mul_f32 v102, v102, v237\n"
    "v_mfma_f32_16x16x32_bf16 v[164:167], v[196:199], v[4:7], v[164:167]\n"
    "v_mul_f32 v103, v103, v237\n"
    "v_mfma_f32_16x16x32_bf16 v[164:167], v[192:195], a[244:247], v[164:167]\n"
    "v_exp_f32 v96, v96\n"
    "v_mfma_f32_16x16x32_bf16 v[168:171], v[192:195], a[248:251], v[168:171]\n"
    "v_exp_f32 v97, v97\n"
    "v_mfma_f32_16x16x32_bf16 v[168:171], v[196:199], v[8:11], v[168:171]\n"
    "v_exp_f32 v98, v98\n"
    "v_mfma_f32_16x16x32_bf16 v[168:171], v[200:203], v[24:27], v[168:171]\n"
    "v_exp_f32 v99, v99\n"
    "v_mfma_f32_16x16x32_bf16 v[172:175], v[200:203], v[28:31], v[172:175]\n"
    "v_exp_f32 v100, v100\n"
    "v_mfma_f32_16x16x32_bf16 v[172:175], v[196:199], v[12:15], v[172:175]\n"
    "v_exp_f32 v101, v101\n"
    "v_mfma_f32_16x16x32_bf16 v[172:175], v[192:195], a[252:255], v[172:175]\n"
    "s_waitcnt lgkmcnt(0)\n"
    "v_exp_f32 v102, v102\n"
    "v_mfma_f32_16x16x32_bf16 v[176:179], v[204:207], a[240:243], v[176:179]\n"
    "ds_read_b64_tr_b16 v[192:193], v216 offset:38016\n"
    "v_fma_f32 v96, v96, v240, v240\n"
    "v_mfma_f32_16x16x32_bf16 v[176:179], v[208:211], v[0:3], v[176:179]\n"
    "ds_read_b64_tr_b16 v[194:195], v216 offset:38272\n"
    "v_fma_f32 v97, v97, v240, v240\n"
    "v_mfma_f32_16x16x32_bf16 v[176:179], v[212:215], v[16:19], v[176:179]\n"
    "ds_read_b64_tr_b16 v[196:197], v216 offset:42240\n"
    "v_fma_f32 v98, v98, v240, v240\n"
    "v_mfma_f32_16x16x32_bf16 v[180:183], v[212:215], v[20:23], v[180:183]\n"
    "ds_read_b64_tr_b16 v[198:199], v216 offset:42496\n"
    "v_fma_f32 v99, v99, v240, v240\n"
    "v_mfma_f32_16x16x32_bf16 v[180:183], v[208:211], v[4:7], v[180:183]\n"
    "ds_read_b64_tr_b16 v[200:201], v216 offset:46464\n"
    "v_fma_f32 v100, v100, v241, v241\n"
    "v_mfma_f32_16x16x32_bf16 v[180:183], v[204:207], a[244:247], v[180:183]\n"
    "ds_read_b64_tr_b16 v[202:203], v216 offset:46720\n"
    "v_fma_f32 v101, v101, v241, v241\n"
    "v_mfma_f32_16x16x32_bf16 v[184:187], v[204:207], a[248:251], v[184:187]\n"
    "v_exp_f32 v103, v103\n"
    "v_mfma_f32_16x16x32_bf16 v[184:187], v[208:211], v[8:11], v[184:187]\n"
    "v_fma_f32 v102, v102, v241, v241\n"
    "v_fma_f32 v103, v103, v241, v241\n"
    "v_mfma_f32_16x16x32_bf16 v[184:187], v[212:215], v[24:27], v[184:187]\n"
    "v_rcp_f32 v96, v96\n"
    "v_mfma_f32_16x16x32_bf16 v[188:191], v[212:215], v[28:31], v[188:191]\n"
    "v_rcp_f32 v97, v97\n"
    "v_mfma_f32_16x16x32_bf16 v[188:191], v[208:211], v[12:15], v[188:191]\n"
    "v_rcp_f32 v98, v98\n"
    "v_mfma_f32_16x16x32_bf16 v[188:191], v[204:207], a[252:255], v[188:191]\n"
    "v_rcp_f32 v99, v99\n"
    "s_waitcnt vmcnt(18)\n"
    "s_barrier\n"
    "s_waitcnt lgkmcnt(2)\n"
    "v_mfma_f32_16x16x32_bf16 v[32:35], v[192:195], a[0:3], 0\n"
    "s_add_u32 m0, s56, 0\n"
    "s_add_u32 s44, %4, 3981440\n"
    "ds_read_b64_tr_b16 v[204:205], v216 offset:50688\n"
    "v_mfma_f32_16x16x32_bf16 v[32:35], v[196:199], a[16:19], v[32:35]\n"
    "s_waitcnt lgkmcnt(1)\n"
    "ds_read_b64_tr_b16 v[206:207], v216 offset:50944\n"
    "buffer_load_dwordx4 v219, s[96:99], s44 offen lds\n"
    "v_mfma_f32_16x16x32_bf16 v[32:35], v[200:203], a[32:35], v[32:35]\n"
    "ds_read_b64_tr_b16 v[208:209], v216 offset:54912\n"
    "v_mul_f32 v96, v96, v112\n"
    "v_mfma_f32_16x16x32_bf16 v[36:39], v[200:203], a[36:39], 0\n"
    "ds_read_b64_tr_b16 v[210:211], v216 offset:55168\n"
    "v_mul_f32 v97, v97, v113\n"
    "v_mfma_f32_16x16x32_bf16 v[36:39], v[196:199], a[20:23], v[36:39]\n"
    "ds_read_b64_tr_b16 v[212:213], v216 offset:59136\n"
    "v_mul_f32 v98, v98, v114\n"
    "v_mfma_f32_16x16x32_bf16 v[36:39], v[192:195], a[4:7], v[36:39]\n"
    "s_add_u32 m0, s56, 4224\n"
    "s_add_u32 s45, %4, 4276352\n"
    "ds_read_b64_tr_b16 v[214:215], v216 offset:59392\n"
    "v_mfma_f32_16x16x32_bf16 v[40:43], v[192:195], a[8:11], 0\n"
    "buffer_load_dwordx4 v219, s[96:99], s45 offen lds\n"
    "v_mul_f32 v99, v99, v115\n"
    "v_mfma_f32_16x16x32_bf16 v[40:43], v[196:199], a[24:27], v[40:43]\n"
    "v_rcp_f32 v100, v100\n"
    "v_mfma_f32_16x16x32_bf16 v[40:43], v[200:203], a[40:43], v[40:43]\n"
    "v_rcp_f32 v101, v101\n"
    "v_mfma_f32_16x16x32_bf16 v[44:47], v[200:203], a[44:47], 0\n"
    "v_rcp_f32 v102, v102\n"
    "v_mfma_f32_16x16x32_bf16 v[44:47], v[196:199], a[28:31], v[44:47]\n"
    "v_rcp_f32 v103, v103\n"
    "v_mfma_f32_16x16x32_bf16 v[44:47], v[192:195], a[12:15], v[44:47]\n"
    "s_waitcnt lgkmcnt(2)\n"
    "v_mul_f32 v100, v100, v116\n"
    "v_mul_f32 v101, v101, v117\n"
    "v_mfma_f32_16x16x32_bf16 v[48:51], v[204:207], a[0:3], 0\n"
    "ds_read_b64_tr_b16 v[192:193], v216 offset:38048\n"
    "v_mul_f32 v102, v102, v118\n"
    "v_mfma_f32_16x16x32_bf16 v[48:51], v[208:211], a[16:19], v[48:51]\n"
    "s_waitcnt lgkmcnt(1)\n"
    "s_add_u32 m0, s56, 8448\n"
    "ds_read_b64_tr_b16 v[194:195], v216 offset:38304\n"
    "v_mfma_f32_16x16x32_bf16 v[48:51], v[212:215], a[32:35], v[48:51]\n"
    "s_add_u32 s46, %4, 4571264\n"
    "ds_read_b64_tr_b16 v[196:197], v216 offset:42272\n"
    "buffer_load_dwordx4 v219, s[96:99], s46 offen lds\n"
    "v_mfma_f32_16x16x32_bf16 v[52:55], v[212:215], a[36:39], 0\n"
    "ds_read_b64_tr_b16 v[198:199], v216 offset:42528\n"
    "v_mul_f32 v103, v103, v119\n"
    "v_mfma_f32_16x16x32_bf16 v[52:55], v[208:211], a[20:23], v[52:55]\n"
    "ds_read_b64_tr_b16 v[200:201], v216 offset:46496\n"
    "v_cvt_pk_bf16_f32 v112, v96, v97\n"
    "v_mfma_f32_16x16x32_bf16 v[52:55], v[204:207], a[4:7], v[52:55]\n"
    "ds_read_b64_tr_b16 v[202:203], v216 offset:46752\n"
    "v_cvt_pk_bf16_f32 v113, v98, v99\n"
    "v_mfma_f32_16x16x32_bf16 v[56:59], v[204:207], a[8:11], 0\n"
    "v_cvt_pk_bf16_f32 v114, v100, v101\n"
    "v_cvt_pk_bf16_f32 v115, v102, v103\n"
    "v_mfma_f32_16x16x32_bf16 v[56:59], v[208:211], a[24:27], v[56:59]\n"
    "v_permlane16_swap_b32 v112, v114\n"
    "v_permlane16_swap_b32 v113, v115\n"
    "v_mfma_f32_16x16x32_bf16 v[56:59], v[212:215], a[40:43], v[56:59]\n"
    "s_add_u32 s58, %10, 2048\n"
    "buffer_store_dwordx4 v[112:115], v251, s[36:39], s58 offen sc1\n"
    "v_mul_f32 v104, v104, v238\n"
    "v_mfma_f32_16x16x32_bf16 v[60:63], v[212:215], a[44:47], 0\n"
    "s_add_u32 m0, s56, 12672\n"
    "s_add_u32 s47, %4, 4866176\n"
    "v_mul_f32 v105, v105, v238\n"
    "v_mfma_f32_16x16x32_bf16 v[60:63], v[208:211], a[28:31], v[60:63]\n"
    "buffer_load_dwordx4 v219, s[92:95], s47 offen lds\n"
    "v_mul_f32 v106, v106, v238\n"
    "v_mfma_f32_16x16x32_bf16 v[60:63], v[204:207], a[12:15], v[60:63]\n"
    "s_waitcnt lgkmcnt(2)\n"
    "v_mul_f32 v107, v107, v238\n"
    "v_mul_f32 v108, v108, v239\n"
    "v_mfma_f32_16x16x32_bf16 v[64:67], v[192:195], a[0:3], 0\n"
    "ds_read_b64_tr_b16 v[204:205], v216 offset:50720\n"
    "v_mul_f32 v109, v109, v239\n"
    "v_mfma_f32_16x16x32_bf16 v[64:67], v[196:199], a[16:19], v[64:67]\n"
    "s_waitcnt lgkmcnt(1)\n"
    "ds_read_b64_tr_b16 v[206:207], v216 offset:50976\n"
    "v_mul_f32 v110, v110, v239\n"
    "v_mfma_f32_16x16x32_bf16 v[64:67], v[200:203], a[32:35], v[64:67]\n"
    "ds_read_b64_tr_b16 v[208:209], v216 offset:54944\n"
    "v_mul_f32 v111, v111, v239\n"
    "v_mfma_f32_16x16x32_bf16 v[68:71], v[200:203], a[36:39], 0\n"
    "s_add_u32 s59, %10, 296960\n"
    "ds_read_b64_tr_b16 v[210:211], v216 offset:55200\n"
    "v_mul_f32 v128, v128, v236\n"
    "v_mfma_f32_16x16x32_bf16 v[68:71], v[196:199], a[20:23], v[68:71]\n"
    "ds_read_b64_tr_b16 v[212:213], v216 offset:59168\n"
    "v_mul_f32 v129, v129, v236\n"
    "v_mfma_f32_16x16x32_bf16 v[68:71], v[192:195], a[4:7], v[68:71]\n"
    "s_add_u32 s55, %19, 16384\n"
    "ds_read_b64_tr_b16 v[214:215], v216 offset:59424\n"
    "v_mul_f32 v130, v130, v236\n"
    "v_mfma_f32_16x16x32_bf16 v[72:75], v[192:195], a[8:11], 0\n"
    "buffer_store_dwordx4 v[252:255], v232, s[36:39], s55 offen offset:0 sc1\n"
    "v_mul_f32 v131, v131, v236\n"
    "v_mfma_f32_16x16x32_bf16 v[72:75], v[196:199], a[24:27], v[72:75]\n"
    "v_exp_f32 v104, v104\n"
    "v_mfma_f32_16x16x32_bf16 v[72:75], v[200:203], a[40:43], v[72:75]\n"
    "v_exp_f32 v105, v105\n"
    "v_mfma_f32_16x16x32_bf16 v[76:79], v[200:203], a[44:47], 0\n"
    "v_exp_f32 v106, v106\n"
    "v_mfma_f32_16x16x32_bf16 v[76:79], v[196:199], a[28:31], v[76:79]\n"
    "v_exp_f32 v107, v107\n"
    "v_mfma_f32_16x16x32_bf16 v[76:79], v[192:195], a[12:15], v[76:79]\n"
    "s_waitcnt lgkmcnt(2)\n"
    "v_exp_f32 v108, v108\n"
    "v_mfma_f32_16x16x32_bf16 v[80:83], v[204:207], a[0:3], 0\n"
    "ds_read_b64_tr_b16 v[192:193], v216 offset:63360\n"
    "v_fma_f32 v104, v104, v242, v242\n"
    "v_mfma_f32_16x16x32_bf16 v[80:83], v[208:211], a[16:19], v[80:83]\n"
    "s_waitcnt lgkmcnt(1)\n"
    "s_add_u32 m0, s56, 16896\n"
    "ds_read_b64_tr_b16 v[194:195], v216 offset:63616\n"
    "v_mfma_f32_16x16x32_bf16 v[80:83], v[212:215], a[32:35], v[80:83]\n"
    "s_add_u32 s48, %4, 1179776\n"
    "ds_read_b64_tr_b16 v[196:197], v217 offset:2048\n"
    "buffer_load_dwordx4 v220, s[92:95], s48 offen lds\n"
    "v_mfma_f32_16x16x32_bf16 v[84:87], v[212:215], a[36:39], 0\n"
    "ds_read_b64_tr_b16 v[198:199], v217 offset:2304\n"
    "v_fma_f32 v105, v105, v242, v242\n"
    "v_mfma_f32_16x16x32_bf16 v[84:87], v[208:211], a[20:23], v[84:87]\n"
    "ds_read_b64_tr_b16 v[200:201], v217 offset:6272\n"
    "v_fma_f32 v106, v106, v242, v242\n"
    "v_mfma_f32_16x16x32_bf16 v[84:87], v[204:207], a[4:7], v[84:87]\n"
    "ds_read_b64_tr_b16 v[202:203], v217 offset:6528\n"
    "v_fma_f32 v107, v107, v242, v242\n"
    "v_mfma_f32_16x16x32_bf16 v[88:91], v[204:207], a[8:11], 0\n"
    "v_exp_f32 v109, v109\n"
    "v_mfma_f32_16x16x32_bf16 v[88:91], v[208:211], a[24:27], v[88:91]\n"
    "v_exp_f32 v110, v110\n"
    "v_mfma_f32_16x16x32_bf16 v[88:91], v[212:215], a[40:43], v[88:91]\n"
    "v_exp_f32 v111, v111\n"
    "v_mfma_f32_16x16x32_bf16 v[92:95], v[212:215], a[44:47], 0\n"
    "s_add_u32 m0, s56, 21120\n"
    "s_add_u32 s49, %4, 3833984\n"
    "v_fma_f32 v108, v108, v243, v243\n"
    "v_mfma_f32_16x16x32_bf16 v[92:95], v[208:211], a[28:31], v[92:95]\n"
    "buffer_load_dwordx4 v220, s[92:95], s49 offen lds\n"
    "v_fma_f32 v109, v109, v243, v243\n"
    "v_mfma_f32_16x16x32_bf16 v[92:95], v[204:207], a[12:15], v[92:95]\n"
    "s_waitcnt lgkmcnt(2)\n"
    "v_fma_f32 v110, v110, v243, v243\n"
    "v_fma_f32 v111, v111, v243, v243\n"
    "v_mfma_f32_16x16x32_bf16 v[32:35], v[192:195], a[48:51], v[32:35]\n"
    "ds_read_b64_tr_b16 v[204:205], v217 offset:10496\n"
    "v_mul_f32 v132, v132, v237\n"
    "v_mfma_f32_16x16x32_bf16 v[32:35], v[196:199], a[64:67], v[32:35]\n"
    "s_waitcnt lgkmcnt(1)\n"
    "ds_read_b64_tr_b16 v[206:207], v217 offset:10752\n"
    "v_mul_f32 v133, v133, v237\n"
    "v_mfma_f32_16x16x32_bf16 v[32:35], v[200:203], a[80:83], v[32:35]\n"
    "ds_read_b64_tr_b16 v[208:209], v217 offset:14720\n"
    "v_mul_f32 v134, v134, v237\n"
    "v_mfma_f32_16x16x32_bf16 v[36:39], v[200:203], a[84:87], v[36:39]\n"
    "ds_read_b64_tr_b16 v[210:211], v217 offset:14976\n"
    "v_mul_f32 v135, v135, v237\n"
    "v_mfma_f32_16x16x32_bf16 v[36:39], v[196:199], a[68:71], v[36:39]\n"
    "ds_read_b64_tr_b16 v[212:213], v217 offset:18944\n"
    "v_mfma_f32_16x16x32_bf16 v[36:39], v[192:195], a[52:55], v[36:39]\n"
    "s_add_u32 m0, s56, 25344\n"
    "s_add_u32 s44, %4, 4866176\n"
    "ds_read_b64_tr_b16 v[214:215], v217 offset:19200\n"
    "v_mfma_f32_16x16x32_bf16 v[40:43], v[192:195], a[56:59], v[40:43]\n"
    "buffer_load_dwordx4 v219, s[96:99], s44 offen lds\n"
    "v_mfma_f32_16x16x32_bf16 v[40:43], v[196:199], a[72:75], v[40:43]\n"
    "v_rcp_f32 v104, v104\n"
    "v_mfma_f32_16x16x32_bf16 v[40:43], v[200:203], a[88:91], v[40:43]\n"
    "v_rcp_f32 v105, v105\n"
    "v_mfma_f32_16x16x32_bf16 v[44:47], v[200:203], a[92:95], v[44:47]\n"
    "v_rcp_f32 v106, v106\n"
    "v_mfma_f32_16x16x32_bf16 v[44:47], v[196:199], a[76:79], v[44:47]\n"
    "v_rcp_f32 v107, v107\n"
    "v_mfma_f32_16x16x32_bf16 v[44:47], v[192:195], a[60:63], v[44:47]\n"
    "s_waitcnt lgkmcnt(2)\n"
    "v_rcp_f32 v108, v108\n"
    "v_mfma_f32_16x16x32_bf16 v[48:51], v[204:207], a[48:51], v[48:51]\n"
    "ds_read_b64_tr_b16 v[192:193], v216 offset:63392\n"
    "v_mul_f32 v104, v104, v120\n"
    "v_mfma_f32_16x16x32_bf16 v[48:51], v[208:211], a[64:67], v[48:51]\n"
    "s_waitcnt lgkmcnt(1)\n"
    "s_add_u32 m0, s56, 29568\n"
    "ds_read_b64_tr_b16 v[194:195], v216 offset:63648\n"
    "v_mfma_f32_16x16x32_bf16 v[48:51], v[212:215], a[80:83], v[48:51]\n"
    "s_add_u32 s45, %4, 1179776\n"
    "ds_read_b64_tr_b16 v[196:197], v217 offset:2080\n"
    "buffer_load_dwordx4 v220, s[96:99], s45 offen lds\n"
    "v_mfma_f32_16x16x32_bf16 v[52:55], v[212:215], a[84:87], v[52:55]\n"
    "ds_read_b64_tr_b16 v[198:199], v217 offset:2336\n"
    "v_mul_f32 v105, v105, v121\n"
    "v_mfma_f32_16x16x32_bf16 v[52:55], v[208:211], a[68:71], v[52:55]\n"
    "ds_read_b64_tr_b16 v[200:201], v217 offset:6304\n"
    "v_mul_f32 v106, v106, v122\n"
    "v_mfma_f32_16x16x32_bf16 v[52:55], v[204:207], a[52:55], v[52:55]\n"
    "ds_read_b64_tr_b16 v[202:203], v217 offset:6560\n"
    "v_mul_f32 v107, v107, v123\n"
    "v_mfma_f32_16x16x32_bf16 v[56:59], v[204:207], a[56:59], v[56:59]\n"
    "v_rcp_f32 v109, v109\n"
    "v_mfma_f32_16x16x32_bf16 v[56:59], v[208:211], a[72:75], v[56:59]\n"
    "v_rcp_f32 v110, v110\n"
    "v_mfma_f32_16x16x32_bf16 v[56:59], v[212:215], a[88:91], v[56:59]\n"
    "v_rcp_f32 v111, v111\n"
    "v_mfma_f32_16x16x32_bf16 v[60:63], v[212:215], a[92:95], v[60:63]\n"
    "s_add_u32 m0, s56, 33792\n"
    "s_add_u32 s46, %4, 3833984\n"
    "v_mul_f32 v108, v108, v124\n"
    "v_mfma_f32_16x16x32_bf16 v[60:63], v[208:211], a[76:79], v[60:63]\n"
    "buffer_load_dwordx4 v220, s[96:99], s46 offen lds\n"
    "v_mul_f32 v109, v109, v125\n"
    "v_mfma_f32_16x16x32_bf16 v[60:63], v[204:207], a[60:63], v[60:63]\n"
    "s_waitcnt lgkmcnt(2)\n"
    "v_mul_f32 v110, v110, v126\n"
    "v_mul_f32 v111, v111, v127\n"
    "v_mfma_f32_16x16x32_bf16 v[64:67], v[192:195], a[48:51], v[64:67]\n"
    "ds_read_b64_tr_b16 v[204:205], v217 offset:10528\n"
    "v_cvt_pk_bf16_f32 v120, v104, v105\n"
    "v_mfma_f32_16x16x32_bf16 v[64:67], v[196:199], a[64:67], v[64:67]\n"
    "s_waitcnt lgkmcnt(1)\n"
    "ds_read_b64_tr_b16 v[206:207], v217 offset:10784\n"
    "v_cvt_pk_bf16_f32 v121, v106, v107\n"
    "v_mfma_f32_16x16x32_bf16 v[64:67], v[200:203], a[80:83], v[64:67]\n"
    "ds_read_b64_tr_b16 v[208:209], v217 offset:14752\n"
    "v_cvt_pk_bf16_f32 v122, v108, v109\n"
    "v_mfma_f32_16x16x32_bf16 v[68:71], v[200:203], a[84:87], v[68:71]\n"
    "ds_read_b64_tr_b16 v[210:211], v217 offset:15008\n"
    "v_cvt_pk_bf16_f32 v123, v110, v111\n"
    "v_mfma_f32_16x16x32_bf16 v[68:71], v[196:199], a[68:71], v[68:71]\n"
    "ds_read_b64_tr_b16 v[212:213], v217 offset:18976\n"
    "v_permlane16_swap_b32 v120, v122\n"
    "v_mfma_f32_16x16x32_bf16 v[68:71], v[192:195], a[52:55], v[68:71]\n"
    "ds_read_b64_tr_b16 v[214:215], v217 offset:19232\n"
    "v_permlane16_swap_b32 v121, v123\n"
    "v_mfma_f32_16x16x32_bf16 v[72:75], v[192:195], a[56:59], v[72:75]\n"
    "buffer_store_dwordx4 v[120:123], v251, s[36:39], s59 offen sc1\n"
    "v_mfma_f32_16x16x32_bf16 v[72:75], v[196:199], a[72:75], v[72:75]\n"
    "v_exp_f32 v128, v128\n"
    "v_mfma_f32_16x16x32_bf16 v[72:75], v[200:203], a[88:91], v[72:75]\n"
    "v_exp_f32 v129, v129\n"
    "v_mfma_f32_16x16x32_bf16 v[76:79], v[200:203], a[92:95], v[76:79]\n"
    "v_exp_f32 v130, v130\n"
    "v_mfma_f32_16x16x32_bf16 v[76:79], v[196:199], a[76:79], v[76:79]\n"
    "v_exp_f32 v131, v131\n"
    "v_mfma_f32_16x16x32_bf16 v[76:79], v[192:195], a[60:63], v[76:79]\n"
    "s_waitcnt lgkmcnt(2)\n"
    "v_exp_f32 v132, v132\n"
    "v_mfma_f32_16x16x32_bf16 v[80:83], v[204:207], a[48:51], v[80:83]\n"
    "ds_read_b64_tr_b16 v[192:193], v217 offset:23168\n"
    "v_fma_f32 v128, v128, v240, v240\n"
    "v_mfma_f32_16x16x32_bf16 v[80:83], v[208:211], a[64:67], v[80:83]\n"
    "s_waitcnt lgkmcnt(1)\n"
    "ds_read_b64_tr_b16 v[194:195], v217 offset:23424\n"
    "v_fma_f32 v129, v129, v240, v240\n"
    "v_mfma_f32_16x16x32_bf16 v[80:83], v[212:215], a[80:83], v[80:83]\n"
    "ds_read_b64_tr_b16 v[196:197], v217 offset:27392\n"
    "v_fma_f32 v130, v130, v240, v240\n"
    "v_mfma_f32_16x16x32_bf16 v[84:87], v[212:215], a[84:87], v[84:87]\n"
    "ds_read_b64_tr_b16 v[198:199], v217 offset:27648\n"
    "v_fma_f32 v131, v131, v240, v240\n"
    "v_mfma_f32_16x16x32_bf16 v[84:87], v[208:211], a[68:71], v[84:87]\n"
    "ds_read_b64_tr_b16 v[200:201], v217 offset:31616\n"
    "v_fma_f32 v132, v132, v241, v241\n"
    "v_mfma_f32_16x16x32_bf16 v[84:87], v[204:207], a[52:55], v[84:87]\n"
    "s_add_u32 s57, %10, 3072\n"
    "ds_read_b64_tr_b16 v[202:203], v217 offset:31872\n"
    "v_mul_f32 v136, v136, v238\n"
    "v_mfma_f32_16x16x32_bf16 v[88:91], v[204:207], a[56:59], v[88:91]\n"
    "v_exp_f32 v133, v133\n"
    "v_mfma_f32_16x16x32_bf16 v[88:91], v[208:211], a[72:75], v[88:91]\n"
    "v_exp_f32 v134, v134\n"
    "v_mfma_f32_16x16x32_bf16 v[88:91], v[212:215], a[88:91], v[88:91]\n"
    "v_exp_f32 v135, v135\n"
    "v_mfma_f32_16x16x32_bf16 v[92:95], v[212:215], a[92:95], v[92:95]\n"
    "v_fma_f32 v133, v133, v241, v241\n"
    "v_fma_f32 v134, v134, v241, v241\n"
    "v_mfma_f32_16x16x32_bf16 v[92:95], v[208:211], a[76:79], v[92:95]\n"
    "v_fma_f32 v135, v135, v241, v241\n"
    "v_mul_f32 v137, v137, v238\n"
    "v_mfma_f32_16x16x32_bf16 v[92:95], v[204:207], a[60:63], v[92:95]\n"
    "v_rcp_f32 v128, v128\n"
    "s_waitcnt vmcnt(18)\n"
    "s_barrier\n"
    "s_waitcnt lgkmcnt(2)\n"
    "v_mfma_f32_16x16x32_bf16 v[32:35], v[192:195], a[96:99], v[32:35]\n"
    "ds_read_b64_tr_b16 v[204:205], v217 offset:35840\n"
    "buffer_store_dwordx4 v[252:255], v232, s[36:39], s55 offen offset:1024 sc1\n"
    "v_mfma_f32_16x16x32_bf16 v[32:35], v[196:199], a[112:115], v[32:35]\n"
    "s_waitcnt lgkmcnt(1)\n"
    "ds_read_b64_tr_b16 v[206:207], v217 offset:36096\n"
    "v_mul_f32 v128, v128, v144\n"
    "v_mfma_f32_16x16x32_bf16 v[32:35], v[200:203], a[128:131], v[32:35]\n"
    "ds_read_b64_tr_b16 v[208:209], v217 offset:40064\n"
    "v_mul_f32 v138, v138, v238\n"
    "v_mfma_f32_16x16x32_bf16 v[36:39], v[200:203], a[132:135], v[36:39]\n"
    "ds_read_b64_tr_b16 v[210:211], v217 offset:40320\n"
    "v_mul_f32 v139, v139, v238\n"
    "v_mfma_f32_16x16x32_bf16 v[36:39], v[196:199], a[116:119], v[36:39]\n"
    "ds_read_b64_tr_b16 v[212:213], v217 offset:44288\n"
    "v_mul_f32 v140, v140, v239\n"
    "v_mfma_f32_16x16x32_bf16 v[36:39], v[192:195], a[100:103], v[36:39]\n"
    "ds_read_b64_tr_b16 v[214:215], v217 offset:44544\n"
    "v_mul_f32 v141, v141, v239\n"
    "v_mfma_f32_16x16x32_bf16 v[40:43], v[192:195], a[104:107], v[40:43]\n"
    "v_rcp_f32 v129, v129\n"
    "v_mfma_f32_16x16x32_bf16 v[40:43], v[196:199], a[120:123], v[40:43]\n"
    "v_rcp_f32 v130, v130\n"
    "v_mfma_f32_16x16x32_bf16 v[40:43], v[200:203], a[136:139], v[40:43]\n"
    "v_rcp_f32 v131, v131\n"
    "v_mfma_f32_16x16x32_bf16 v[44:47], v[200:203], a[140:143], v[44:47]\n"
    "v_rcp_f32 v132, v132\n"
    "v_mfma_f32_16x16x32_bf16 v[44:47], v[196:199], a[124:127], v[44:47]\n"
    "v_rcp_f32 v133, v133\n"
    "v_mfma_f32_16x16x32_bf16 v[44:47], v[192:195], a[108:111], v[44:47]\n"
    "s_waitcnt lgkmcnt(2)\n"
    "v_rcp_f32 v134, v134\n"
    "v_mfma_f32_16x16x32_bf16 v[48:51], v[204:207], a[96:99], v[48:51]\n"
    "ds_read_b64_tr_b16 v[192:193], v217 offset:23200\n"
    "v_mul_f32 v129, v129, v145\n"
    "v_mfma_f32_16x16x32_bf16 v[48:51], v[208:211], a[112:115], v[48:51]\n"
    "s_waitcnt lgkmcnt(1)\n"
    "ds_read_b64_tr_b16 v[194:195], v217 offset:23456\n"
    "v_mul_f32 v130, v130, v146\n"
    "v_mfma_f32_16x16x32_bf16 v[48:51], v[212:215], a[128:131], v[48:51]\n"
    "ds_read_b64_tr_b16 v[196:197], v217 offset:27424\n"
    "v_mul_f32 v131, v131, v147\n"
    "v_mfma_f32_16x16x32_bf16 v[52:55], v[212:215], a[132:135], v[52:55]\n"
    "ds_read_b64_tr_b16 v[198:199], v217 offset:27680\n"
    "v_mul_f32 v132, v132, v148\n"
    "v_mfma_f32_16x16x32_bf16 v[52:55], v[208:211], a[116:119], v[52:55]\n"
    "ds_read_b64_tr_b16 v[200:201], v217 offset:31648\n"
    "v_mul_f32 v133, v133, v149\n"
    "v_mfma_f32_16x16x32_bf16 v[52:55], v[204:207], a[100:103], v[52:55]\n"
    "ds_read_b64_tr_b16 v[202:203], v217 offset:31904\n"
    "v_mul_f32 v134, v134, v150\n"
    "v_mfma_f32_16x16x32_bf16 v[56:59], v[204:207], a[104:107], v[56:59]\n"
    "v_rcp_f32 v135, v135\n"
    "v_mfma_f32_16x16x32_bf16 v[56:59], v[208:211], a[120:123], v[56:59]\n"
    "v_mul_f32 v135, v135, v151\n"
    "v_cvt_pk_bf16_f32 v144, v128, v129\n"
    "v_mfma_f32_16x16x32_bf16 v[56:59], v[212:215], a[136:139], v[56:59]\n"
    "v_cvt_pk_bf16_f32 v145, v130, v131\n"
    "v_cvt_pk_bf16_f32 v146, v132, v133\n"
    "v_mfma_f32_16x16x32_bf16 v[60:63], v[212:215], a[140:143], v[60:63]\n"
    "v_cvt_pk_bf16_f32 v147, v134, v135\n"
    "v_permlane16_swap_b32 v144, v146\n"
    "v_mfma_f32_16x16x32_bf16 v[60:63], v[208:211], a[124:127], v[60:63]\n"
    "v_permlane16_swap_b32 v145, v147\n"
    "v_mul_f32 v142, v142, v239\n"
    "v_mfma_f32_16x16x32_bf16 v[60:63], v[204:207], a[108:111], v[60:63]\n"
    "s_waitcnt lgkmcnt(2)\n"
    "buffer_store_dwordx4 v[144:147], v251, s[36:39], s57 offen sc1\n"
    "v_mul_f32 v143, v143, v239\n"
    "v_mfma_f32_16x16x32_bf16 v[64:67], v[192:195], a[96:99], v[64:67]\n"
    "s_add_u32 s58, %10, 297984\n"
    "ds_read_b64_tr_b16 v[204:205], v217 offset:35872\n"
    "v_mfma_f32_16x16x32_bf16 v[64:67], v[196:199], a[112:115], v[64:67]\n"
    "s_waitcnt lgkmcnt(1)\n"
    "ds_read_b64_tr_b16 v[206:207], v217 offset:36128\n"
    "v_mfma_f32_16x16x32_bf16 v[64:67], v[200:203], a[128:131], v[64:67]\n"
    "ds_read_b64_tr_b16 v[208:209], v217 offset:40096\n"
    "v_mul_f32 v160, v160, v236\n"
    "v_mfma_f32_16x16x32_bf16 v[68:71], v[200:203], a[132:135], v[68:71]\n"
    "ds_read_b64_tr_b16 v[210:211], v217 offset:40352\n"
    "v_mul_f32 v161, v161, v236\n"
    "v_mfma_f32_16x16x32_bf16 v[68:71], v[196:199], a[116:119], v[68:71]\n"
    "ds_read_b64_tr_b16 v[212:213], v217 offset:44320\n"
    "v_mul_f32 v162, v162, v236\n"
    "v_mfma_f32_16x16x32_bf16 v[68:71], v[192:195], a[100:103], v[68:71]\n"
    "ds_read_b64_tr_b16 v[214:215], v217 offset:44576\n"
    "v_mul_f32 v163, v163, v236\n"
    "v_mfma_f32_16x16x32_bf16 v[72:75], v[192:195], a[104:107], v[72:75]\n"
    "v_exp_f32 v136, v136\n"
    "v_mfma_f32_16x16x32_bf16 v[72:75], v[196:199], a[120:123], v[72:75]\n"
    "v_exp_f32 v137, v137\n"
    "v_mfma_f32_16x16x32_bf16 v[72:75], v[200:203], a[136:139], v[72:75]\n"
    "v_exp_f32 v138, v138\n"
    "v_mfma_f32_16x16x32_bf16 v[76:79], v[200:203], a[140:143], v[76:79]\n"
    "v_exp_f32 v139, v139\n"
    "v_mfma_f32_16x16x32_bf16 v[76:79], v[196:199], a[124:127], v[76:79]\n"
    "v_exp_f32 v140, v140\n"
    "v_mfma_f32_16x16x32_bf16 v[76:79], v[192:195], a[108:111], v[76:79]\n"
    "s_waitcnt lgkmcnt(2)\n"
    "v_exp_f32 v141, v141\n"
    "v_mfma_f32_16x16x32_bf16 v[80:83], v[204:207], a[96:99], v[80:83]\n"
    "ds_read_b64_tr_b16 v[192:193], v217 offset:48512\n"
    "v_fma_f32 v136, v136, v242, v242\n"
    "v_mfma_f32_16x16x32_bf16 v[80:83], v[208:211], a[112:115], v[80:83]\n"
    "s_waitcnt lgkmcnt(1)\n"
    "ds_read_b64_tr_b16 v[194:195], v217 offset:48768\n"
    "v_fma_f32 v137, v137, v242, v242\n"
    "v_mfma_f32_16x16x32_bf16 v[80:83], v[212:215], a[128:131], v[80:83]\n"
    "ds_read_b64_tr_b16 v[196:197], v217 offset:52736\n"
    "v_fma_f32 v138, v138, v242, v242\n"
    "v_mfma_f32_16x16x32_bf16 v[84:87], v[212:215], a[132:135], v[84:87]\n"
    "ds_read_b64_tr_b16 v[198:199], v217 offset:52992\n"
    "v_fma_f32 v139, v139, v242, v242\n"
    "v_mfma_f32_16x16x32_bf16 v[84:87], v[208:211], a[116:119], v[84:87]\n"
    "ds_read_b64_tr_b16 v[200:201], v217 offset:56960\n"
    "v_fma_f32 v140, v140, v243, v243\n"
    "v_mfma_f32_16x16x32_bf16 v[84:87], v[204:207], a[100:103], v[84:87]\n"
    "ds_read_b64_tr_b16 v[202:203], v217 offset:57216\n"
    "v_fma_f32 v141, v141, v243, v243\n"
    "v_mfma_f32_16x16x32_bf16 v[88:91], v[204:207], a[104:107], v[88:91]\n"
    "v_exp_f32 v142, v142\n"
    "v_mfma_f32_16x16x32_bf16 v[88:91], v[208:211], a[120:123], v[88:91]\n"
    "v_exp_f32 v143, v143\n"
    "v_mfma_f32_16x16x32_bf16 v[88:91], v[212:215], a[136:139], v[88:91]\n"
    "v_fma_f32 v142, v142, v243, v243\n"
    "v_fma_f32 v143, v143, v243, v243\n"
    "v_mfma_f32_16x16x32_bf16 v[92:95], v[212:215], a[140:143], v[92:95]\n"
    "v_rcp_f32 v136, v136\n"
    "v_mfma_f32_16x16x32_bf16 v[92:95], v[208:211], a[124:127], v[92:95]\n"
    "v_rcp_f32 v137, v137\n"
    "v_mfma_f32_16x16x32_bf16 v[92:95], v[204:207], a[108:111], v[92:95]\n"
    "s_waitcnt lgkmcnt(2)\n"
    "v_rcp_f32 v138, v138\n"
    "v_mfma_f32_16x16x32_bf16 v[32:35], v[192:195], a[144:147], v[32:35]\n"
    "ds_read_b64_tr_b16 v[204:205], v217 offset:61184\n"
    "v_mul_f32 v136, v136, v152\n"
    "v_mfma_f32_16x16x32_bf16 v[32:35], v[196:199], a[160:163], v[32:35]\n"
    "s_waitcnt lgkmcnt(1)\n"
    "ds_read_b64_tr_b16 v[206:207], v217 offset:61440\n"
    "v_mul_f32 v137, v137, v153\n"
    "v_mfma_f32_16x16x32_bf16 v[32:35], v[200:203], a[176:179], v[32:35]\n"
    "ds_read_b64_tr_b16 v[208:209], v217 offset:65408\n"
    "v_mul_f32 v138, v138, v154\n"
    "v_mfma_f32_16x16x32_bf16 v[36:39], v[200:203], a[180:183], v[36:39]\n"
    "ds_read_b64_tr_b16 v[210:211], v218 offset:128\n"
    "v_cvt_pk_bf16_f32 v152, v136, v137\n"
    "v_mfma_f32_16x16x32_bf16 v[36:39], v[196:199], a[164:167], v[36:39]\n"
    "ds_read_b64_tr_b16 v[212:213], v218 offset:4096\n"
    "v_mul_f32 v164, v164, v237\n"
    "v_mfma_f32_16x16x32_bf16 v[36:39], v[192:195], a[148:151], v[36:39]\n"
    "ds_read_b64_tr_b16 v[214:215], v218 offset:4352\n"
    "v_mul_f32 v165, v165, v237\n"
    "v_mfma_f32_16x16x32_bf16 v[40:43], v[192:195], a[152:155], v[40:43]\n"
    "v_rcp_f32 v139, v139\n"
    "v_mfma_f32_16x16x32_bf16 v[40:43], v[196:199], a[168:171], v[40:43]\n"
    "v_rcp_f32 v140, v140\n"
    "v_mfma_f32_16x16x32_bf16 v[40:43], v[200:203], a[184:187], v[40:43]\n"
    "v_rcp_f32 v141, v141\n"
    "v_mfma_f32_16x16x32_bf16 v[44:47], v[200:203], a[188:191], v[44:47]\n"
    "v_rcp_f32 v142, v142\n"
    "v_mfma_f32_16x16x32_bf16 v[44:47], v[196:199], a[172:175], v[44:47]\n"
    "v_rcp_f32 v143, v143\n"
    "v_mfma_f32_16x16x32_bf16 v[44:47], v[192:195], a[156:159], v[44:47]\n"
    "s_waitcnt lgkmcnt(2)\n"
    "v_mul_f32 v139, v139, v155\n"
    "v_mul_f32 v140, v140, v156\n"
    "v_mfma_f32_16x16x32_bf16 v[48:51], v[204:207], a[144:147], v[48:51]\n"
    "ds_read_b64_tr_b16 v[192:193], v217 offset:48544\n"
    "v_mul_f32 v141, v141, v157\n"
    "v_mfma_f32_16x16x32_bf16 v[48:51], v[208:211], a[160:163], v[48:51]\n"
    "s_waitcnt lgkmcnt(1)\n"
    "ds_read_b64_tr_b16 v[194:195], v217 offset:48800\n"
    "v_mul_f32 v142, v142, v158\n"
    "v_mfma_f32_16x16x32_bf16 v[48:51], v[212:215], a[176:179], v[48:51]\n"
    "ds_read_b64_tr_b16 v[196:197], v217 offset:52768\n"
    "v_mul_f32 v143, v143, v159\n"
    "v_mfma_f32_16x16x32_bf16 v[52:55], v[212:215], a[180:183], v[52:55]\n"
    "ds_read_b64_tr_b16 v[198:199], v217 offset:53024\n"
    "v_cvt_pk_bf16_f32 v153, v138, v139\n"
    "v_mfma_f32_16x16x32_bf16 v[52:55], v[208:211], a[164:167], v[52:55]\n"
    "ds_read_b64_tr_b16 v[200:201], v217 offset:56992\n"
    "v_cvt_pk_bf16_f32 v154, v140, v141\n"
    "v_mfma_f32_16x16x32_bf16 v[52:55], v[204:207], a[148:151], v[52:55]\n"
    "ds_read_b64_tr_b16 v[202:203], v217 offset:57248\n"
    "v_cvt_pk_bf16_f32 v155, v142, v143\n"
    "v_mfma_f32_16x16x32_bf16 v[56:59], v[204:207], a[152:155], v[56:59]\n"
    "v_permlane16_swap_b32 v152, v154\n"
    "v_permlane16_swap_b32 v153, v155\n"
    "v_mfma_f32_16x16x32_bf16 v[56:59], v[208:211], a[168:171], v[56:59]\n"
    "buffer_store_dwordx4 v[152:155], v251, s[36:39], s58 offen sc1\n"
    "v_mul_f32 v166, v166, v237\n"
    "v_mfma_f32_16x16x32_bf16 v[56:59], v[212:215], a[184:187], v[56:59]\n"
    "v_mul_f32 v167, v167, v237\n"
    "v_mfma_f32_16x16x32_bf16 v[60:63], v[212:215], a[188:191], v[60:63]\n"
    "v_exp_f32 v160, v160\n"
    "v_mfma_f32_16x16x32_bf16 v[60:63], v[208:211], a[172:175], v[60:63]\n"
    "v_exp_f32 v161, v161\n"
    "v_mfma_f32_16x16x32_bf16 v[60:63], v[204:207], a[156:159], v[60:63]\n"
    "s_waitcnt lgkmcnt(2)\n"
    "v_exp_f32 v162, v162\n"
    "v_mfma_f32_16x16x32_bf16 v[64:67], v[192:195], a[144:147], v[64:67]\n"
    "s_add_u32 s35, %20, s50\n"
    "ds_read_b64_tr_b16 v[204:205], v217 offset:61216\n"
    "buffer_store_dwordx4 v[252:255], v232, s[36:39], s35 offen sc1\n"
    "v_mfma_f32_16x16x32_bf16 v[64:67], v[196:199], a[160:163], v[64:67]\n"
    "s_waitcnt lgkmcnt(1)\n"
    "ds_read_b64_tr_b16 v[206:207], v217 offset:61472\n"
    "v_fma_f32 v160, v160, v240, v240\n"
    "v_mfma_f32_16x16x32_bf16 v[64:67], v[200:203], a[176:179], v[64:67]\n"
    "ds_read_b64_tr_b16 v[208:209], v217 offset:65440\n"
    "v_fma_f32 v161, v161, v240, v240\n"
    "v_mfma_f32_16x16x32_bf16 v[68:71], v[200:203], a[180:183], v[68:71]\n"
    "ds_read_b64_tr_b16 v[210:211], v218 offset:160\n"
    "v_fma_f32 v162, v162, v240, v240\n"
    "v_mfma_f32_16x16x32_bf16 v[68:71], v[196:199], a[164:167], v[68:71]\n"
    "s_add_u32 s58, %10, 8192\n"
    "ds_read_b64_tr_b16 v[212:213], v218 offset:4128\n"
    "v_mul_f32 v168, v168, v238\n"
    "v_mfma_f32_16x16x32_bf16 v[68:71], v[192:195], a[148:151], v[68:71]\n"
    "ds_read_b64_tr_b16 v[214:215], v218 offset:4384\n"
    "v_mul_f32 v169, v169, v238\n"
    "v_mfma_f32_16x16x32_bf16 v[72:75], v[192:195], a[152:155], v[72:75]\n"
    "v_exp_f32 v163, v163\n"
    "v_mfma_f32_16x16x32_bf16 v[72:75], v[196:199], a[168:171], v[72:75]\n"
    "v_exp_f32 v164, v164\n"
    "v_mfma_f32_16x16x32_bf16 v[72:75], v[200:203], a[184:187], v[72:75]\n"
    "v_exp_f32 v165, v165\n"
    "v_mfma_f32_16x16x32_bf16 v[76:79], v[200:203], a[188:191], v[76:79]\n"
    "v_exp_f32 v166, v166\n"
    "v_mfma_f32_16x16x32_bf16 v[76:79], v[196:199], a[172:175], v[76:79]\n"
    "v_exp_f32 v167, v167\n"
    "v_mfma_f32_16x16x32_bf16 v[76:79], v[192:195], a[156:159], v[76:79]\n"
    "s_waitcnt lgkmcnt(2)\n"
    "v_fma_f32 v163, v163, v240, v240\n"
    "v_fma_f32 v164, v164, v241, v241\n"
    "v_mfma_f32_16x16x32_bf16 v[80:83], v[204:207], a[144:147], v[80:83]\n"
    "ds_read_b64_tr_b16 v[192:193], v216 offset:38080\n"
    "v_fma_f32 v165, v165, v241, v241\n"
    "v_mfma_f32_16x16x32_bf16 v[80:83], v[208:211], a[160:163], v[80:83]\n"
    "s_waitcnt lgkmcnt(1)\n"
    "ds_read_b64_tr_b16 v[194:195], v216 offset:38336\n"
    "v_fma_f32 v166, v166, v241, v241\n"
    "v_mfma_f32_16x16x32_bf16 v[80:83], v[212:215], a[176:179], v[80:83]\n"
    "ds_read_b64_tr_b16 v[196:197], v216 offset:42304\n"
    "v_fma_f32 v167, v167, v241, v241\n"
    "v_mfma_f32_16x16x32_bf16 v[84:87], v[212:215], a[180:183], v[84:87]\n"
    "ds_read_b64_tr_b16 v[198:199], v216 offset:42560\n"
    "v_mul_f32 v170, v170, v238\n"
    "v_mfma_f32_16x16x32_bf16 v[84:87], v[208:211], a[164:167], v[84:87]\n"
    "ds_read_b64_tr_b16 v[200:201], v216 offset:46528\n"
    "v_mul_f32 v171, v171, v238\n"
    "v_mfma_f32_16x16x32_bf16 v[84:87], v[204:207], a[148:151], v[84:87]\n"
    "ds_read_b64_tr_b16 v[202:203], v216 offset:46784\n"
    "v_mul_f32 v172, v172, v239\n"
    "v_mfma_f32_16x16x32_bf16 v[88:91], v[204:207], a[152:155], v[88:91]\n"
    "v_rcp_f32 v160, v160\n"
    "v_mfma_f32_16x16x32_bf16 v[88:91], v[208:211], a[168:171], v[88:91]\n"
    "v_rcp_f32 v161, v161\n"
    "v_mfma_f32_16x16x32_bf16 v[88:91], v[212:215], a[184:187], v[88:91]\n"
    "v_rcp_f32 v162, v162\n"
    "v_mfma_f32_16x16x32_bf16 v[92:95], v[212:215], a[188:191], v[92:95]\n"
    "v_rcp_f32 v163, v163\n"
    "v_mfma_f32_16x16x32_bf16 v[92:95], v[208:211], a[172:175], v[92:95]\n"
    "v_rcp_f32 v164, v164\n"
    "v_mfma_f32_16x16x32_bf16 v[92:95], v[204:207], a[156:159], v[92:95]\n"
    "v_rcp_f32 v165, v165\n"
    "s_waitcnt vmcnt(4)\n"
    "s_barrier\n"
    "s_cmp_lg_u32 %7, 0\n"
    "s_cbranch_scc1 .Lsig1_%=\n"
    "s_mov_b64 exec, 1\n"
    "v_mov_b32 v245, 1\n"
    "buffer_atomic_add v245, v250, s[36:39], %13 offen offset:0\n"
    "s_mov_b64 exec, -1\n"
    "s_nop 1\n"
    ".Lsig1_%=:\n"
    "s_waitcnt lgkmcnt(2)\n"
    "v_mfma_f32_16x16x32_bf16 v[96:99], v[192:195], a[0:3], 0\n"
    "ds_read_b64_tr_b16 v[204:205], v216 offset:50752\n"
    "v_mul_f32 v160, v160, v176\n"
    "v_mfma_f32_16x16x32_bf16 v[96:99], v[196:199], a[16:19], v[96:99]\n"
    "s_waitcnt lgkmcnt(1)\n"
    "ds_read_b64_tr_b16 v[206:207], v216 offset:51008\n"
    "v_mul_f32 v161, v161, v177\n"
    "v_mfma_f32_16x16x32_bf16 v[96:99], v[200:203], a[32:35], v[96:99]\n"
    "ds_read_b64_tr_b16 v[208:209], v216 offset:54976\n"
    "v_mul_f32 v162, v162, v178\n"
    "v_mfma_f32_16x16x32_bf16 v[100:103], v[200:203], a[36:39], 0\n"
    "ds_read_b64_tr_b16 v[210:211], v216 offset:55232\n"
    "v_mul_f32 v163, v163, v179\n"
    "v_mfma_f32_16x16x32_bf16 v[100:103], v[196:199], a[20:23], v[100:103]\n"
    "ds_read_b64_tr_b16 v[212:213], v216 offset:59200\n"
    "v_mul_f32 v164, v164, v180\n"
    "v_mfma_f32_16x16x32_bf16 v[100:103], v[192:195], a[4:7], v[100:103]\n"
    "ds_read_b64_tr_b16 v[214:215], v216 offset:59456\n"
    "v_mul_f32 v165, v165, v181\n"
    "v_mfma_f32_16x16x32_bf16 v[104:107], v[192:195], a[8:11], 0\n"
    "v_rcp_f32 v166, v166\n"
    "v_mfma_f32_16x16x32_bf16 v[104:107], v[196:199], a[24:27], v[104:107]\n"
    "v_rcp_f32 v167, v167\n"
    "v_mfma_f32_16x16x32_bf16 v[104:107], v[200:203], a[40:43], v[104:107]\n"
    "v_mul_f32 v166, v166, v182\n"
    "v_mul_f32 v167, v167, v183\n"
    "v_mfma_f32_16x16x32_bf16 v[108:111], v[200:203], a[44:47], 0\n"
    "v_cvt_pk_bf16_f32 v176, v160, v161\n"
    "v_cvt_pk_bf16_f32 v177, v162, v163\n"
    "v_mfma_f32_16x16x32_bf16 v[108:111], v[196:199], a[28:31], v[108:111]\n"
    "v_cvt_pk_bf16_f32 v178, v164, v165\n"
    "v_cvt_pk_bf16_f32 v179, v166, v167\n"
    "v_mfma_f32_16x16x32_bf16 v[108:111], v[192:195], a[12:15], v[108:111]\n"
    "s_waitcnt lgkmcnt(2)\n"
    "v_permlane16_swap_b32 v176, v178\n"
    "v_permlane16_swap_b32 v177, v179\n"
    "v_mfma_f32_16x16x32_bf16 v[112:115], v[204:207], a[0:3], 0\n"
    "ds_read_b64_tr_b16 v[192:193], v216 offset:38112\n"
    "buffer_store_dwordx4 v[176:179], v251, s[36:39], s58 offen sc1\n"
    "v_mfma_f32_16x16x32_bf16 v[112:115], v[208:211], a[16:19], v[112:115]\n"
    "s_waitcnt lgkmcnt(1)\n"
    "ds_read_b64_tr_b16 v[194:195], v216 offset:38368\n"
    "v_mul_f32 v173, v173, v239\n"
    "v_mfma_f32_16x16x32_bf16 v[112:115], v[212:215], a[32:35], v[112:115]\n"
    "ds_read_b64_tr_b16 v[196:197], v216 offset:42336\n"
    "v_mul_f32 v174, v174, v239\n"
    "v_mfma_f32_16x16x32_bf16 v[116:119], v[212:215], a[36:39], 0\n"
    "ds_read_b64_tr_b16 v[198:199], v216 offset:42592\n"
    "v_mul_f32 v175, v175, v239\n"
    "v_mfma_f32_16x16x32_bf16 v[116:119], v[208:211], a[20:23], v[116:119]\n"
    "ds_read_b64_tr_b16 v[200:201], v216 offset:46560\n"
    "v_mfma_f32_16x16x32_bf16 v[116:119], v[204:207], a[4:7], v[116:119]\n"
    "ds_read_b64_tr_b16 v[202:203], v216 offset:46816\n"
    "v_mfma_f32_16x16x32_bf16 v[120:123], v[204:207], a[8:11], 0\n"
    "v_exp_f32 v168, v168\n"
    "v_mfma_f32_16x16x32_bf16 v[120:123], v[208:211], a[24:27], v[120:123]\n"
    "v_exp_f32 v169, v169\n"
    "v_mfma_f32_16x16x32_bf16 v[120:123], v[212:215], a[40:43], v[120:123]\n"
    "v_exp_f32 v170, v170\n"
    "v_mfma_f32_16x16x32_bf16 v[124:127], v[212:215], a[44:47], 0\n"
    "v_exp_f32 v171, v171\n"
    "v_mfma_f32_16x16x32_bf16 v[124:127], v[208:211], a[28:31], v[124:127]\n"
    "v_exp_f32 v172, v172\n"
    "v_mfma_f32_16x16x32_bf16 v[124:127], v[204:207], a[12:15], v[124:127]\n"
    "s_waitcnt lgkmcnt(2)\n"
    "v_exp_f32 v173, v173\n"
    "v_mfma_f32_16x16x32_bf16 v[128:131], v[192:195], a[0:3], 0\n"
    "ds_read_b64_tr_b16 v[204:205], v216 offset:50784\n"
    "v_fma_f32 v168, v168, v242, v242\n"
    "v_mfma_f32_16x16x32_bf16 v[128:131], v[196:199], a[16:19], v[128:131]\n"
    "s_waitcnt lgkmcnt(1)\n"
    "ds_read_b64_tr_b16 v[206:207], v216 offset:51040\n"
    "v_fma_f32 v169, v169, v242, v242\n"
    "v_mfma_f32_16x16x32_bf16 v[128:131], v[200:203], a[32:35], v[128:131]\n"
    "ds_read_b64_tr_b16 v[208:209], v216 offset:55008\n"
    "v_fma_f32 v170, v170, v242, v242\n"
    "v_mfma_f32_16x16x32_bf16 v[132:135], v[200:203], a[36:39], 0\n"
    "ds_read_b64_tr_b16 v[210:211], v216 offset:55264\n"
    "v_fma_f32 v171, v171, v242, v242\n"
    "v_mfma_f32_16x16x32_bf16 v[132:135], v[196:199], a[20:23], v[132:135]\n"
    "ds_read_b64_tr_b16 v[212:213], v216 offset:59232\n"
    "v_fma_f32 v172, v172, v243, v243\n"
    "v_mfma_f32_16x16x32_bf16 v[132:135], v[192:195], a[4:7], v[132:135]\n"
    "ds_read_b64_tr_b16 v[214:215], v216 offset:59488\n"
    "v_fma_f32 v173, v173, v243, v243\n"
    "v_mfma_f32_16x16x32_bf16 v[136:139], v[192:195], a[8:11], 0\n"
    "v_exp_f32 v174, v174\n"
    "v_mfma_f32_16x16x32_bf16 v[136:139], v[196:199], a[24:27], v[136:139]\n"
    "v_exp_f32 v175, v175\n"
    "v_mfma_f32_16x16x32_bf16 v[136:139], v[200:203], a[40:43], v[136:139]\n"
    "v_fma_f32 v174, v174, v243, v243\n"
    "v_fma_f32 v175, v175, v243, v243\n"
    "v_mfma_f32_16x16x32_bf16 v[140:143], v[200:203], a[44:47], 0\n"
    "v_rcp_f32 v168, v168\n"
    "v_mfma_f32_16x16x32_bf16 v[140:143], v[196:199], a[28:31], v[140:143]\n"
    "v_rcp_f32 v169, v169\n"
    "v_mfma_f32_16x16x32_bf16 v[140:143], v[192:195], a[12:15], v[140:143]\n"
    "s_waitcnt lgkmcnt(2)\n"
    "v_rcp_f32 v170, v170\n"
    "v_mfma_f32_16x16x32_bf16 v[144:147], v[204:207], a[0:3], 0\n"
    "ds_read_b64_tr_b16 v[192:193], v218 offset:8320\n"
    "v_mul_f32 v168, v168, v184\n"
    "v_mfma_f32_16x16x32_bf16 v[144:147], v[208:211], a[16:19], v[144:147]\n"
    "s_waitcnt lgkmcnt(1)\n"
    "ds_read_b64_tr_b16 v[194:195], v218 offset:8576\n"
    "v_mul_f32 v169, v169, v185\n"
    "v_mfma_f32_16x16x32_bf16 v[144:147], v[212:215], a[32:35], v[144:147]\n"
    "ds_read_b64_tr_b16 v[196:197], v218 offset:12544\n"
    "v_mul_f32 v170, v170, v186\n"
    "v_mfma_f32_16x16x32_bf16 v[148:151], v[212:215], a[36:39], 0\n"
    "ds_read_b64_tr_b16 v[198:199], v218 offset:12800\n"
    "v_cvt_pk_bf16_f32 v184, v168, v169\n"
    "v_mfma_f32_16x16x32_bf16 v[148:151], v[208:211], a[20:23], v[148:151]\n"
    "s_add_u32 s59, %10, 303104\n"
    "ds_read_b64_tr_b16 v[200:201], v218 offset:16768\n"
    "v_mfma_f32_16x16x32_bf16 v[148:151], v[204:207], a[4:7], v[148:151]\n"
    "ds_read_b64_tr_b16 v[202:203], v218 offset:17024\n"
    "v_mfma_f32_16x16x32_bf16 v[152:155], v[204:207], a[8:11], 0\n"
    "v_rcp_f32 v171, v171\n"
    "v_mfma_f32_16x16x32_bf16 v[152:155], v[208:211], a[24:27], v[152:155]\n"
    "v_rcp_f32 v172, v172\n"
    "v_mfma_f32_16x16x32_bf16 v[152:155], v[212:215], a[40:43], v[152:155]\n"
    "v_rcp_f32 v173, v173\n"
    "v_mfma_f32_16x16x32_bf16 v[156:159], v[212:215], a[44:47], 0\n"
    "v_rcp_f32 v174, v174\n"
    "v_mfma_f32_16x16x32_bf16 v[156:159], v[208:211], a[28:31], v[156:159]\n"
    "v_rcp_f32 v175, v175\n"
    "v_mfma_f32_16x16x32_bf16 v[156:159], v[204:207], a[12:15], v[156:159]\n"
    "v_mul_f32 v171, v171, v187\n"
    "v_mul_f32 v172, v172, v188\n"
    "s_barrier\n"
    "s_waitcnt lgkmcnt(2)\n"
    "v_mfma_f32_16x16x32_bf16 v[32:35], v[192:195], a[192:195], v[32:35]\n"
    "s_add_u32 m0, s56, 38016\n"
    "s_add_u32 s47, %6, 0\n"
    "ds_read_b64_tr_b16 v[204:205], v216 offset:0\n"
    "v_mfma_f32_16x16x32_bf16 v[32:35], v[196:199], a[208:211], v[32:35]\n"
    "s_waitcnt lgkmcnt(1)\n"
    "ds_read_b64_tr_b16 v[206:207], v216 offset:256\n"
    "buffer_load_dwordx4 v232, s[36:39], s47 offen lds\n"
    "v_mfma_f32_16x16x32_bf16 v[32:35], v[200:203], a[224:227], v[32:35]\n"
    "s_add_u32 m0, s56, 42240\n"
    "s_add_u32 s48, %6, 4096\n"
    "ds_read_b64_tr_b16 v[208:209], v216 offset:4224\n"
    "v_mfma_f32_16x16x32_bf16 v[36:39], v[200:203], a[228:231], v[36:39]\n"
    "ds_read_b64_tr_b16 v[210:211], v216 offset:4480\n"
    "buffer_load_dwordx4 v232, s[36:39], s48 offen lds\n"
    "v_mfma_f32_16x16x32_bf16 v[36:39], v[196:199], a[212:215], v[36:39]\n"
    "ds_read_b64_tr_b16 v[212:213], v216 offset:8448\n"
    "v_mul_f32 v173, v173, v189\n"
    "v_mfma_f32_16x16x32_bf16 v[36:39], v[192:195], a[196:199], v[36:39]\n"
    "ds_read_b64_tr_b16 v[214:215], v216 offset:8704\n"
    "v_mul_f32 v174, v174, v190\n"
    "v_mfma_f32_16x16x32_bf16 v[40:43], v[192:195], a[200:203], v[40:43]\n"
    "v_mul_f32 v175, v175, v191\n"
    "v_cvt_pk_bf16_f32 v185, v170, v171\n"
    "v_mfma_f32_16x16x32_bf16 v[40:43], v[196:199], a[216:219], v[40:43]\n"
    "s_add_u32 m0, s56, 46464\n"
    "s_add_u32 s49, %6, 8192\n"
    "v_cvt_pk_bf16_f32 v186, v172, v173\n"
    "v_mfma_f32_16x16x32_bf16 v[40:43], v[200:203], a[232:235], v[40:43]\n"
    "buffer_load_dwordx4 v232, s[36:39], s49 offen lds\n"
    "v_cvt_pk_bf16_f32 v187, v174, v175\n"
    "v_mfma_f32_16x16x32_bf16 v[44:47], v[200:203], a[236:239], v[44:47]\n"
    "v_permlane16_swap_b32 v184, v186\n"
    "v_permlane16_swap_b32 v185, v187\n"
    "v_mfma_f32_16x16x32_bf16 v[44:47], v[196:199], a[220:223], v[44:47]\n"
    "buffer_store_dwordx4 v[184:187], v251, s[36:39], s59 offen sc1\n"
    "v_mfma_f32_16x16x32_bf16 v[44:47], v[192:195], a[204:207], v[44:47]\n"
    "s_waitcnt lgkmcnt(2)\n"
    "v_mfma_f32_16x16x32_bf16 v[48:51], v[204:207], a[192:195], v[48:51]\n"
    "s_add_u32 m0, s56, 50688\n"
    "s_add_u32 s44, %6, 12288\n"
    "ds_read_b64_tr_b16 v[192:193], v218 offset:8352\n"
    "v_mfma_f32_16x16x32_bf16 v[48:51], v[208:211], a[208:211], v[48:51]\n"
    "s_waitcnt lgkmcnt(1)\n"
    "ds_read_b64_tr_b16 v[194:195], v218 offset:8608\n"
    "buffer_load_dwordx4 v232, s[36:39], s44 offen lds\n"
    "v_mfma_f32_16x16x32_bf16 v[48:51], v[212:215], a[224:227], v[48:51]\n"
    "ds_read_b64_tr_b16 v[196:197], v218 offset:12576\n"
    "v_mfma_f32_16x16x32_bf16 v[52:55], v[212:215], a[228:231], v[52:55]\n"
    "ds_read_b64_tr_b16 v[198:199], v218 offset:12832\n"
    "v_mfma_f32_16x16x32_bf16 v[52:55], v[208:211], a[212:215], v[52:55]\n"
    "ds_read_b64_tr_b16 v[200:201], v218 offset:16800\n"
    "v_mfma_f32_16x16x32_bf16 v[52:55], v[204:207], a[196:199], v[52:55]\n"
    "s_add_u32 m0, s56, 54912\n"
    "s_add_u32 s45, %6, 16384\n"
    "ds_read_b64_tr_b16 v[202:203], v218 offset:17056\n"
    "v_mfma_f32_16x16x32_bf16 v[56:59], v[204:207], a[200:203], v[56:59]\n"
    "buffer_load_dwordx4 v232, s[36:39], s45 offen lds\n"
    "v_mfma_f32_16x16x32_bf16 v[56:59], v[208:211], a[216:219], v[56:59]\n"
    "v_mfma_f32_16x16x32_bf16 v[56:59], v[212:215], a[232:235], v[56:59]\n"
    "v_mfma_f32_16x16x32_bf16 v[60:63], v[212:215], a[236:239], v[60:63]\n"
    "v_mfma_f32_16x16x32_bf16 v[60:63], v[208:211], a[220:223], v[60:63]\n"
    "s_add_u32 m0, s56, 59136\n"
    "s_add_u32 s46, %6, 20480\n"
    "v_mfma_f32_16x16x32_bf16 v[60:63], v[204:207], a[204:207], v[60:63]\n"
    "s_waitcnt lgkmcnt(2)\n"
    "buffer_load_dwordx4 v232, s[36:39], s46 offen lds\n"
    "buffer_load_dword v252, off, s[36:39], %13 offset:0 sc1\n"
    "v_mfma_f32_16x16x32_bf16 v[64:67], v[192:195], a[192:195], v[64:67]\n"
    "ds_read_b64_tr_b16 v[204:205], v216 offset:32\n"
    "v_mfma_f32_16x16x32_bf16 v[64:67], v[196:199], a[208:211], v[64:67]\n"
    "s_waitcnt lgkmcnt(1)\n"
    "ds_read_b64_tr_b16 v[206:207], v216 offset:288\n"
    "v_mfma_f32_16x16x32_bf16 v[64:67], v[200:203], a[224:227], v[64:67]\n"
    "ds_read_b64_tr_b16 v[208:209], v216 offset:4256\n"
    "v_mfma_f32_16x16x32_bf16 v[68:71], v[200:203], a[228:231], v[68:71]\n"
    "ds_read_b64_tr_b16 v[210:211], v216 offset:4512\n"
    "v_mfma_f32_16x16x32_bf16 v[68:71], v[196:199], a[212:215], v[68:71]\n"
    "ds_read_b64_tr_b16 v[212:213], v216 offset:8480\n"
    "v_mfma_f32_16x16x32_bf16 v[68:71], v[192:195], a[196:199], v[68:71]\n"
    "ds_read_b64_tr_b16 v[214:215], v216 offset:8736\n"
    "v_mfma_f32_16x16x32_bf16 v[72:75], v[192:195], a[200:203], v[72:75]\n"
    "v_mfma_f32_16x16x32_bf16 v[72:75], v[196:199], a[216:219], v[72:75]\n"
    "v_mfma_f32_16x16x32_bf16 v[72:75], v[200:203], a[232:235], v[72:75]\n"
    "v_mfma_f32_16x16x32_bf16 v[76:79], v[200:203], a[236:239], v[76:79]\n"
    "v_mfma_f32_16x16x32_bf16 v[76:79], v[196:199], a[220:223], v[76:79]\n"
    "v_mfma_f32_16x16x32_bf16 v[76:79], v[192:195], a[204:207], v[76:79]\n"
    "s_waitcnt lgkmcnt(2)\n"
    "v_mfma_f32_16x16x32_bf16 v[80:83], v[204:207], a[192:195], v[80:83]\n"
    "ds_read_b64_tr_b16 v[192:193], v216 offset:12672\n"
    "v_mfma_f32_16x16x32_bf16 v[80:83], v[208:211], a[208:211], v[80:83]\n"
    "s_waitcnt lgkmcnt(1)\n"
    "ds_read_b64_tr_b16 v[194:195], v216 offset:12928\n"
    "v_mfma_f32_16x16x32_bf16 v[80:83], v[212:215], a[224:227], v[80:83]\n"
    "ds_read_b64_tr_b16 v[196:197], v216 offset:16896\n"
    "v_mfma_f32_16x16x32_bf16 v[84:87], v[212:215], a[228:231], v[84:87]\n"
    "ds_read_b64_tr_b16 v[198:199], v216 offset:17152\n"
    "v_mfma_f32_16x16x32_bf16 v[84:87], v[208:211], a[212:215], v[84:87]\n"
    "ds_read_b64_tr_b16 v[200:201], v216 offset:21120\n"
    "v_mfma_f32_16x16x32_bf16 v[84:87], v[204:207], a[196:199], v[84:87]\n"
    "ds_read_b64_tr_b16 v[202:203], v216 offset:21376\n"
    "v_mfma_f32_16x16x32_bf16 v[88:91], v[204:207], a[200:203], v[88:91]\n"
    "v_mfma_f32_16x16x32_bf16 v[88:91], v[208:211], a[216:219], v[88:91]\n"
    "v_mfma_f32_16x16x32_bf16 v[88:91], v[212:215], a[232:235], v[88:91]\n"
    "v_mfma_f32_16x16x32_bf16 v[92:95], v[212:215], a[236:239], v[92:95]\n"
    "v_mfma_f32_16x16x32_bf16 v[92:95], v[208:211], a[220:223], v[92:95]\n"
    "v_mfma_f32_16x16x32_bf16 v[92:95], v[204:207], a[204:207], v[92:95]\n"
    "s_waitcnt lgkmcnt(2)\n"
    "v_mfma_f32_16x16x32_bf16 v[32:35], v[192:195], a[240:243], v[32:35]\n"
    "ds_read_b64_tr_b16 v[204:205], v216 offset:25344\n"
    "v_mfma_f32_16x16x32_bf16 v[32:35], v[196:199], v[0:3], v[32:35]\n"
    "s_waitcnt lgkmcnt(1)\n"
    "ds_read_b64_tr_b16 v[206:207], v216 offset:25600\n"
    "v_mfma_f32_16x16x32_bf16 v[32:35], v[200:203], v[16:19], v[32:35]\n"
    "ds_read_b64_tr_b16 v[208:209], v216 offset:29568\n"
    "v_mfma_f32_16x16x32_bf16 v[36:39], v[200:203], v[20:23], v[36:39]\n"
    "ds_read_b64_tr_b16 v[210:211], v216 offset:29824\n"
    "v_mfma_f32_16x16x32_bf16 v[36:39], v[196:199], v[4:7], v[36:39]\n"
    "ds_read_b64_tr_b16 v[212:213], v216 offset:33792\n"
    "v_mfma_f32_16x16x32_bf16 v[36:39], v[192:195], a[244:247], v[36:39]\n"
    "ds_read_b64_tr_b16 v[214:215], v216 offset:34048\n"
    "v_mfma_f32_16x16x32_bf16 v[40:43], v[192:195], a[248:251], v[40:43]\n"
    "v_mfma_f32_16x16x32_bf16 v[40:43], v[196:199], v[8:11], v[40:43]\n"
    "v_mfma_f32_16x16x32_bf16 v[40:43], v[200:203], v[24:27], v[40:43]\n"
    "v_mfma_f32_16x16x32_bf16 v[44:47], v[200:203], v[28:31], v[44:47]\n"
    "v_mfma_f32_16x16x32_bf16 v[44:47], v[196:199], v[12:15], v[44:47]\n"
    "v_mfma_f32_16x16x32_bf16 v[44:47], v[192:195], a[252:255], v[44:47]\n"
    "s_waitcnt lgkmcnt(2)\n"
    "v_mfma_f32_16x16x32_bf16 v[48:51], v[204:207], a[240:243], v[48:51]\n"
    "ds_read_b64_tr_b16 v[192:193], v216 offset:12704\n"
    "v_mfma_f32_16x16x32_bf16 v[48:51], v[208:211], v[0:3], v[48:51]\n"
    "s_waitcnt lgkmcnt(1)\n"
    "ds_read_b64_tr_b16 v[194:195], v216 offset:12960\n"
    "v_mfma_f32_16x16x32_bf16 v[48:51], v[212:215], v[16:19], v[48:51]\n"
    "ds_read_b64_tr_b16 v[196:197], v216 offset:16928\n"
    "v_mfma_f32_16x16x32_bf16 v[52:55], v[212:215], v[20:23], v[52:55]\n"
    "ds_read_b64_tr_b16 v[198:199], v216 offset:17184\n"
    "v_mfma_f32_16x16x32_bf16 v[52:55], v[208:211], v[4:7], v[52:55]\n"
    "ds_read_b64_tr_b16 v[200:201], v216 offset:21152\n"
    "v_mfma_f32_16x16x32_bf16 v[52:55], v[204:207], a[244:247], v[52:55]\n"
    "s_waitcnt vmcnt(0)\n"
    "ds_read_b64_tr_b16 v[202:203], v216 offset:21408\n"
    "v_mfma_f32_16x16x32_bf16 v[56:59], v[204:207], a[248:251], v[56:59]\n"
    "v_mfma_f32_16x16x32_bf16 v[56:59], v[208:211], v[8:11], v[56:59]\n"
    "v_readfirstlane_b32 s51, v252\n"
    "v_mfma_f32_16x16x32_bf16 v[56:59], v[212:215], v[24:27], v[56:59]\n"
    "v_mfma_f32_16x16x32_bf16 v[60:63], v[212:215], v[28:31], v[60:63]\n"
    "v_mfma_f32_16x16x32_bf16 v[60:63], v[208:211], v[12:15], v[60:63]\n"
    "s_cmp_lt_u32 s51, 8\n"
    "s_cbranch_scc1 .Lps2_%=\n"
    ".Lpr2_%=:\n"
    "v_mfma_f32_16x16x32_bf16 v[60:63], v[204:207], a[252:255], v[60:63]\n"
    "s_waitcnt lgkmcnt(2)\n"
    "v_mfma_f32_16x16x32_bf16 v[64:67], v[192:195], a[240:243], v[64:67]\n"
    "s_add_u32 s47, %11, 0\n"
    "ds_read_b64_tr_b16 v[204:205], v216 offset:25376\n"
    "buffer_load_dwordx4 a[0:3], v232, s[36:39], s47 offen sc1\n"
    "v_mfma_f32_16x16x32_bf16 v[64:67], v[196:199], v[0:3], v[64:67]\n"
    "s_waitcnt lgkmcnt(1)\n"
    "s_add_u32 s48, %11, 9216\n"
    "ds_read_b64_tr_b16 v[206:207], v216 offset:25632\n"
    "v_mfma_f32_16x16x32_bf16 v[64:67], v[200:203], v[16:19], v[64:67]\n"
    "ds_read_b64_tr_b16 v[208:209], v216 offset:29600\n"
    "buffer_load_dwordx4 a[16:19], v232, s[36:39], s48 offen sc1\n"
    "v_mfma_f32_16x16x32_bf16 v[68:71], v[200:203], v[20:23], v[68:71]\n"
    "ds_read_b64_tr_b16 v[210:211], v216 offset:29856\n"
    "v_mul_f32 v32, v32, v236\n"
    "v_mfma_f32_16x16x32_bf16 v[68:71], v[196:199], v[4:7], v[68:71]\n"
    "ds_read_b64_tr_b16 v[212:213], v216 offset:33824\n"
    "v_mul_f32 v33, v33, v236\n"
    "v_mfma_f32_16x16x32_bf16 v[68:71], v[192:195], a[244:247], v[68:71]\n"
    "ds_read_b64_tr_b16 v[214:215], v216 offset:34080\n"
    "v_mul_f32 v34, v34, v236\n"
    "v_mfma_f32_16x16x32_bf16 v[72:75], v[192:195], a[248:251], v[72:75]\n"
    "s_add_u32 s49, %11, 1024\n"
    "v_mul_f32 v35, v35, v236\n"
    "v_mul_f32 v36, v36, v237\n"
    "v_mfma_f32_16x16x32_bf16 v[72:75], v[196:199], v[8:11], v[72:75]\n"
    "buffer_load_dwordx4 a[32:35], v232, s[36:39], s49 offen sc1\n"
    "v_mul_f32 v37, v37, v237\n"
    "v_mfma_f32_16x16x32_bf16 v[72:75], v[200:203], v[24:27], v[72:75]\n"
    "v_mul_f32 v38, v38, v237\n"
    "v_mul_f32 v39, v39, v237\n"
    "v_mfma_f32_16x16x32_bf16 v[76:79], v[200:203], v[28:31], v[76:79]\n"
    "v_exp_f32 v32, v32\n"
    "v_mfma_f32_16x16x32_bf16 v[76:79], v[196:199], v[12:15], v[76:79]\n"
    "v_exp_f32 v33, v33\n"
    "v_mfma_f32_16x16x32_bf16 v[76:79], v[192:195], a[252:255], v[76:79]\n"
    "s_waitcnt lgkmcnt(2)\n"
    "s_add_u32 s44, %11, 147456\n"
    "v_fma_f32 v32, v32, v240, v240\n"
    "v_mfma_f32_16x16x32_bf16 v[80:83], v[204:207], a[240:243], v[80:83]\n"
    "ds_read_b64_tr_b16 v[192:193], v216 offset:63424\n"
    "buffer_load_dwordx4 a[4:7], v232, s[36:39], s44 offen sc1\n"
    "v_mfma_f32_16x16x32_bf16 v[80:83], v[208:211], v[0:3], v[80:83]\n"
    "s_waitcnt lgkmcnt(1)\n"
    "ds_read_b64_tr_b16 v[194:195], v216 offset:63680\n"
    "v_fma_f32 v33, v33, v240, v240\n"
    "v_mfma_f32_16x16x32_bf16 v[80:83], v[212:215], v[16:19], v[80:83]\n"
    "ds_read_b64_tr_b16 v[196:197], v217 offset:2112\n"
    "v_mfma_f32_16x16x32_bf16 v[84:87], v[212:215], v[20:23], v[84:87]\n"
    "ds_read_b64_tr_b16 v[198:199], v217 offset:2368\n"
    "v_mfma_f32_16x16x32_bf16 v[84:87], v[208:211], v[4:7], v[84:87]\n"
    "s_add_u32 s45, %11, 156672\n"
    "ds_read_b64_tr_b16 v[200:201], v217 offset:6336\n"
    "v_mfma_f32_16x16x32_bf16 v[84:87], v[204:207], a[244:247], v[84:87]\n"
    "ds_read_b64_tr_b16 v[202:203], v217 offset:6592\n"
    "buffer_load_dwordx4 a[20:23], v232, s[36:39], s45 offen sc1\n"
    "v_mfma_f32_16x16x32_bf16 v[88:91], v[204:207], a[248:251], v[88:91]\n"
    "v_exp_f32 v34, v34\n"
    "v_mfma_f32_16x16x32_bf16 v[88:91], v[208:211], v[8:11], v[88:91]\n"
    "v_exp_f32 v35, v35\n"
    "v_mfma_f32_16x16x32_bf16 v[88:91], v[212:215], v[24:27], v[88:91]\n"
    "v_exp_f32 v36, v36\n"
    "v_mfma_f32_16x16x32_bf16 v[92:95], v[212:215], v[28:31], v[92:95]\n"
    "s_add_u32 s46, %11, 148480\n"
    "v_exp_f32 v37, v37\n"
    "v_mfma_f32_16x16x32_bf16 v[92:95], v[208:211], v[12:15], v[92:95]\n"
    "buffer_load_dwordx4 a[36:39], v232, s[36:39], s46 offen sc1\n"
    "v_fma_f32 v34, v34, v240, v240\n"
    "v_mfma_f32_16x16x32_bf16 v[92:95], v[204:207], a[252:255], v[92:95]\n"
    "s_waitcnt lgkmcnt(2)\n"
    "v_exp_f32 v38, v38\n"
    "v_mfma_f32_16x16x32_bf16 v[96:99], v[192:195], a[48:51], v[96:99]\n"
    "ds_read_b64_tr_b16 v[204:205], v217 offset:10560\n"
    "v_fma_f32 v35, v35, v240, v240\n"
    "v_mfma_f32_16x16x32_bf16 v[96:99], v[196:199], a[64:67], v[96:99]\n"
    "s_waitcnt lgkmcnt(1)\n"
    "ds_read_b64_tr_b16 v[206:207], v217 offset:10816\n"
    "v_fma_f32 v36, v36, v241, v241\n"
    "v_mfma_f32_16x16x32_bf16 v[96:99], v[200:203], a[80:83], v[96:99]\n"
    "s_add_u32 s47, %12, 0\n"
    "ds_read_b64_tr_b16 v[208:209], v217 offset:14784\n"
    "v_fma_f32 v37, v37, v241, v241\n"
    "v_mfma_f32_16x16x32_bf16 v[100:103], v[200:203], a[84:87], v[100:103]\n"
    "ds_read_b64_tr_b16 v[210:211], v217 offset:15040\n"
    "buffer_load_dwordx4 a[8:11], v232, s[36:39], s47 offen sc1\n"
    "v_mfma_f32_16x16x32_bf16 v[100:103], v[196:199], a[68:71], v[100:103]\n"
    "ds_read_b64_tr_b16 v[212:213], v217 offset:19008\n"
    "v_fma_f32 v38, v38, v241, v241\n"
    "v_mfma_f32_16x16x32_bf16 v[100:103], v[192:195], a[52:55], v[100:103]\n"
    "ds_read_b64_tr_b16 v[214:215], v217 offset:19264\n"
    "v_mfma_f32_16x16x32_bf16 v[104:107], v[192:195], a[56:59], v[104:107]\n"
    "v_exp_f32 v39, v39\n"
    "v_mfma_f32_16x16x32_bf16 v[104:107], v[196:199], a[72:75], v[104:107]\n"
    "s_add_u32 s48, %12, 9216\n"
    "v_fma_f32 v39, v39, v241, v241\n"
    "v_mfma_f32_16x16x32_bf16 v[104:107], v[200:203], a[88:91], v[104:107]\n"
    "buffer_load_dwordx4 a[24:27], v232, s[36:39], s48 offen sc1\n"
    "v_mfma_f32_16x16x32_bf16 v[108:111], v[200:203], a[92:95], v[108:111]\n"
    "v_rcp_f32 v32, v32\n"
    "v_mfma_f32_16x16x32_bf16 v[108:111], v[196:199], a[76:79], v[108:111]\n"
    "v_rcp_f32 v33, v33\n"
    "v_mfma_f32_16x16x32_bf16 v[108:111], v[192:195], a[60:63], v[108:111]\n"
    "s_waitcnt lgkmcnt(2)\n"
    "v_rcp_f32 v34, v34\n"
    "v_mfma_f32_16x16x32_bf16 v[112:115], v[204:207], a[48:51], v[112:115]\n"
    "s_add_u32 s49, %12, 1024\n"
    "ds_read_b64_tr_b16 v[192:193], v216 offset:63456\n"
    "v_mul_f32 v32, v32, v48\n"
    "v_mfma_f32_16x16x32_bf16 v[112:115], v[208:211], a[64:67], v[112:115]\n"
    "s_waitcnt lgkmcnt(1)\n"
    "ds_read_b64_tr_b16 v[194:195], v216 offset:63712\n"
    "buffer_load_dwordx4 a[40:43], v232, s[36:39], s49 offen sc1\n"
    "v_mfma_f32_16x16x32_bf16 v[112:115], v[212:215], a[80:83], v[112:115]\n"
    "ds_read_b64_tr_b16 v[196:197], v217 offset:2144\n"
    "v_mul_f32 v33, v33, v49\n"
    "v_mfma_f32_16x16x32_bf16 v[116:119], v[212:215], a[84:87], v[116:119]\n"
    "ds_read_b64_tr_b16 v[198:199], v217 offset:2400\n"
    "v_mul_f32 v34, v34, v50\n"
    "v_mfma_f32_16x16x32_bf16 v[116:119], v[208:211], a[68:71], v[116:119]\n"
    "ds_read_b64_tr_b16 v[200:201], v217 offset:6368\n"
    "v_cvt_pk_bf16_f32 v48, v32, v33\n"
    "v_mfma_f32_16x16x32_bf16 v[116:119], v[204:207], a[52:55], v[116:119]\n"
    "s_add_u32 s44, %12, 147456\n"
    "s_add_u32 s59, %10, 4096\n"
    "ds_read_b64_tr_b16 v[202:203], v217 offset:6624\n"
    "v_mfma_f32_16x16x32_bf16 v[120:123], v[204:207], a[56:59], v[120:123]\n"
    "buffer_load_dwordx4 a[12:15], v232, s[36:39], s44 offen sc1\n"
    "v_mul_f32 v40, v40, v238\n"
    "v_mfma_f32_16x16x32_bf16 v[120:123], v[208:211], a[72:75], v[120:123]\n"
    "v_rcp_f32 v35, v35\n"
    "v_mfma_f32_16x16x32_bf16 v[120:123], v[212:215], a[88:91], v[120:123]\n"
    "v_rcp_f32 v36, v36\n"
    "v_mfma_f32_16x16x32_bf16 v[124:127], v[212:215], a[92:95], v[124:127]\n"
    "v_rcp_f32 v37, v37\n"
    "v_mfma_f32_16x16x32_bf16 v[124:127], v[208:211], a[76:79], v[124:127]\n"
    "s_add_u32 s45, %12, 156672\n"
    "v_rcp_f32 v38, v38\n"
    "v_mfma_f32_16x16x32_bf16 v[124:127], v[204:207], a[60:63], v[124:127]\n"
    "s_waitcnt lgkmcnt(2)\n"
    "buffer_load_dwordx4 a[28:31], v232, s[36:39], s45 offen sc1\n"
    "v_mul_f32 v35, v35, v51\n"
    "v_mfma_f32_16x16x32_bf16 v[128:131], v[192:195], a[48:51], v[128:131]\n"
    "ds_read_b64_tr_b16 v[204:205], v217 offset:10592\n"
    "v_mul_f32 v36, v36, v52\n"
    "v_mfma_f32_16x16x32_bf16 v[128:131], v[196:199], a[64:67], v[128:131]\n"
    "s_waitcnt lgkmcnt(1)\n"
    "ds_read_b64_tr_b16 v[206:207], v217 offset:10848\n"
    "v_mul_f32 v37, v37, v53\n"
    "v_mfma_f32_16x16x32_bf16 v[128:131], v[200:203], a[80:83], v[128:131]\n"
    "ds_read_b64_tr_b16 v[208:209], v217 offset:14816\n"
    "v_mul_f32 v38, v38, v54\n"
    "v_mfma_f32_16x16x32_bf16 v[132:135], v[200:203], a[84:87], v[132:135]\n"
    "s_add_u32 s46, %12, 148480\n"
    "ds_read_b64_tr_b16 v[210:211], v217 offset:15072\n"
    "v_cvt_pk_bf16_f32 v49, v34, v35\n"
    "v_mfma_f32_16x16x32_bf16 v[132:135], v[196:199], a[68:71], v[132:135]\n"
    "ds_read_b64_tr_b16 v[212:213], v217 offset:19040\n"
    "buffer_load_dwordx4 a[44:47], v232, s[36:39], s46 offen sc1\n"
    "v_mfma_f32_16x16x32_bf16 v[132:135], v[192:195], a[52:55], v[132:135]\n"
    "ds_read_b64_tr_b16 v[214:215], v217 offset:19296\n"
    "v_cvt_pk_bf16_f32 v50, v36, v37\n"
    "v_mfma_f32_16x16x32_bf16 v[136:139], v[192:195], a[56:59], v[136:139]\n"
    "v_rcp_f32 v39, v39\n"
    "v_mfma_f32_16x16x32_bf16 v[136:139], v[196:199], a[72:75], v[136:139]\n"
    "v_mul_f32 v39, v39, v55\n"
    "v_permlane16_swap_b32 v48, v50\n"
    "v_mfma_f32_16x16x32_bf16 v[136:139], v[200:203], a[88:91], v[136:139]\n"
    "v_cvt_pk_bf16_f32 v51, v38, v39\n"
    "v_mul_f32 v41, v41, v238\n"
    "v_mfma_f32_16x16x32_bf16 v[140:143], v[200:203], a[92:95], v[140:143]\n"
    "v_permlane16_swap_b32 v49, v51\n"
    "v_mul_f32 v42, v42, v238\n"
    "v_mfma_f32_16x16x32_bf16 v[140:143], v[196:199], a[76:79], v[140:143]\n"
    "buffer_store_dwordx4 v[48:51], v251, s[36:39], s59 offen sc1\n"
    "v_mul_f32 v43, v43, v238\n"
    "v_mfma_f32_16x16x32_bf16 v[140:143], v[192:195], a[60:63], v[140:143]\n"
    "s_waitcnt lgkmcnt(2)\n"
    "v_mul_f32 v44, v44, v239\n"
    "v_mul_f32 v45, v45, v239\n"
    "v_mfma_f32_16x16x32_bf16 v[144:147], v[204:207], a[48:51], v[144:147]\n"
    "ds_read_b64_tr_b16 v[192:193], v217 offset:23232\n"
    "v_mul_f32 v46, v46, v239\n"
    "v_mfma_f32_16x16x32_bf16 v[144:147], v[208:211], a[64:67], v[144:147]\n"
    "s_waitcnt lgkmcnt(1)\n"
    "ds_read_b64_tr_b16 v[194:195], v217 offset:23488\n"
    "v_mul_f32 v47, v47, v239\n"
    "v_mfma_f32_16x16x32_bf16 v[144:147], v[212:215], a[80:83], v[144:147]\n"
    "ds_read_b64_tr_b16 v[196:197], v217 offset:27456\n"
    "v_mfma_f32_16x16x32_bf16 v[148:151], v[212:215], a[84:87], v[148:151]\n"
    "ds_read_b64_tr_b16 v[198:199], v217 offset:27712\n"
    "v_mfma_f32_16x16x32_bf16 v[148:151], v[208:211], a[68:71], v[148:151]\n"
    "ds_read_b64_tr_b16 v[200:201], v217 offset:31680\n"
    "v_mfma_f32_16x16x32_bf16 v[148:151], v[204:207], a[52:55], v[148:151]\n"
    "ds_read_b64_tr_b16 v[202:203], v217 offset:31936\n"
    "v_mfma_f32_16x16x32_bf16 v[152:155], v[204:207], a[56:59], v[152:155]\n"
    "v_exp_f32 v40, v40\n"
    "v_mfma_f32_16x16x32_bf16 v[152:155], v[208:211], a[72:75], v[152:155]\n"
    "v_exp_f32 v41, v41\n"
    "v_mfma_f32_16x16x32_bf16 v[152:155], v[212:215], a[88:91], v[152:155]\n"
    "v_exp_f32 v42, v42\n"
    "v_mfma_f32_16x16x32_bf16 v[156:159], v[212:215], a[92:95], v[156:159]\n"
    "v_exp_f32 v43, v43\n"
    "v_mfma_f32_16x16x32_bf16 v[156:159], v[208:211], a[76:79], v[156:159]\n"
    "v_exp_f32 v44, v44\n"
    "v_mfma_f32_16x16x32_bf16 v[156:159], v[204:207], a[60:63], v[156:159]\n"
    "v_exp_f32 v45, v45\n"
    "s_barrier\n"
    "s_cmp_lg_u32 %7, 0\n"
    "s_cbranch_scc1 .Lsig3_%=\n"
    "s_mov_b64 exec, 1\n"
    "v_mov_b32 v245, 1\n"
    "buffer_atomic_add v245, v250, s[36:39], %13 offen offset:128\n"
    "s_mov_b64 exec, -1\n"
    "s_nop 1\n"
    ".Lsig3_%=:\n"
    "s_cmp_lg_u32 %7, 0\n"
    "s_cbranch_scc1 .Lsig4_%=\n"
    "s_mov_b64 exec, 1\n"
    "v_mov_b32 v245, 1\n"
    "buffer_atomic_add v245, v250, s[36:39], %14 offen offset:0\n"
    "s_mov_b64 exec, -1\n"
    "s_nop 1\n"
    ".Lsig4_%=:\n"
    "s_waitcnt lgkmcnt(2)\n"
    "v_mfma_f32_16x16x32_bf16 v[96:99], v[192:195], a[96:99], v[96:99]\n"
    "s_add_u32 m0, s56, 63360\n"
    "s_add_u32 s47, %6, 24576\n"
    "ds_read_b64_tr_b16 v[204:205], v217 offset:35904\n"
    "v_mfma_f32_16x16x32_bf16 v[96:99], v[196:199], a[112:115], v[96:99]\n"
    "s_waitcnt lgkmcnt(1)\n"
    "ds_read_b64_tr_b16 v[206:207], v217 offset:36160\n"
    "buffer_load_dwordx4 v232, s[36:39], s47 offen lds\n"
    "v_mfma_f32_16x16x32_bf16 v[96:99], v[200:203], a[128:131], v[96:99]\n"
    "s_add_u32 m0, s56, 67584\n"
    "s_add_u32 s48, %6, 28672\n"
    "ds_read_b64_tr_b16 v[208:209], v217 offset:40128\n"
    "v_mfma_f32_16x16x32_bf16 v[100:103], v[200:203], a[132:135], v[100:103]\n"
    "ds_read_b64_tr_b16 v[210:211], v217 offset:40384\n"
    "buffer_load_dwordx4 v232, s[36:39], s48 offen lds\n"
    "v_mfma_f32_16x16x32_bf16 v[100:103], v[196:199], a[116:119], v[100:103]\n"
    "ds_read_b64_tr_b16 v[212:213], v217 offset:44352\n"
    "v_fma_f32 v40, v40, v242, v242\n"
    "v_mfma_f32_16x16x32_bf16 v[100:103], v[192:195], a[100:103], v[100:103]\n"
    "ds_read_b64_tr_b16 v[214:215], v217 offset:44608\n"
    "v_fma_f32 v41, v41, v242, v242\n"
    "v_mfma_f32_16x16x32_bf16 v[104:107], v[192:195], a[104:107], v[104:107]\n"
    "v_exp_f32 v46, v46\n"
    "v_mfma_f32_16x16x32_bf16 v[104:107], v[196:199], a[120:123], v[104:107]\n"
    "s_add_u32 m0, s56, 71808\n"
    "s_add_u32 s49, %6, 32768\n"
    "v_fma_f32 v42, v42, v242, v242\n"
    "v_mfma_f32_16x16x32_bf16 v[104:107], v[200:203], a[136:139], v[104:107]\n"
    "buffer_load_dwordx4 v232, s[36:39], s49 offen lds\n"
    "v_fma_f32 v43, v43, v242, v242\n"
    "v_mfma_f32_16x16x32_bf16 v[108:111], v[200:203], a[140:143], v[108:111]\n"
    "v_exp_f32 v47, v47\n"
    "v_mfma_f32_16x16x32_bf16 v[108:111], v[196:199], a[124:127], v[108:111]\n"
    "v_fma_f32 v44, v44, v243, v243\n"
    "v_fma_f32 v45, v45, v243, v243\n"
    "v_mfma_f32_16x16x32_bf16 v[108:111], v[192:195], a[108:111], v[108:111]\n"
    "s_waitcnt lgkmcnt(2)\n"
    "v_fma_f32 v46, v46, v243, v243\n"
    "v_fma_f32 v47, v47, v243, v243\n"
    "v_mfma_f32_16x16x32_bf16 v[112:115], v[204:207], a[96:99], v[112:115]\n"
    "s_add_u32 s44, %11, 10240\n"
    "s_add_u32 s57, %10, 299008\n"
    "ds_read_b64_tr_b16 v[192:193], v217 offset:23264\n"
    "v_mfma_f32_16x16x32_bf16 v[112:115], v[208:211], a[112:115], v[112:115]\n"
    "s_waitcnt lgkmcnt(1)\n"
    "ds_read_b64_tr_b16 v[194:195], v217 offset:23520\n"
    "buffer_load_dwordx4 a[48:51], v232, s[36:39], s44 offen sc1\n"
    "v_mfma_f32_16x16x32_bf16 v[112:115], v[212:215], a[128:131], v[112:115]\n"
    "ds_read_b64_tr_b16 v[196:197], v217 offset:27488\n"
    "v_mul_f32 v64, v64, v236\n"
    "v_mfma_f32_16x16x32_bf16 v[116:119], v[212:215], a[132:135], v[116:119]\n"
    "ds_read_b64_tr_b16 v[198:199], v217 offset:27744\n"
    "v_mul_f32 v65, v65, v236\n"
    "v_mfma_f32_16x16x32_bf16 v[116:119], v[208:211], a[116:119], v[116:119]\n"
    "ds_read_b64_tr_b16 v[200:201], v217 offset:31712\n"
    "v_mul_f32 v66, v66, v236\n"
    "v_mfma_f32_16x16x32_bf16 v[116:119], v[204:207], a[100:103], v[116:119]\n"
    "s_add_u32 s45, %11, 2048\n"
    "ds_read_b64_tr_b16 v[202:203], v217 offset:31968\n"
    "v_mul_f32 v67, v67, v236\n"
    "v_mfma_f32_16x16x32_bf16 v[120:123], v[204:207], a[104:107], v[120:123]\n"
    "buffer_load_dwordx4 a[64:67], v232, s[36:39], s45 offen sc1\n"
    "v_mul_f32 v68, v68, v237\n"
    "v_mfma_f32_16x16x32_bf16 v[120:123], v[208:211], a[120:123], v[120:123]\n"
    "v_rcp_f32 v40, v40\n"
    "v_mfma_f32_16x16x32_bf16 v[120:123], v[212:215], a[136:139], v[120:123]\n"
    "v_rcp_f32 v41, v41\n"
    "v_mfma_f32_16x16x32_bf16 v[124:127], v[212:215], a[140:143], v[124:127]\n"
    "v_rcp_f32 v42, v42\n"
    "v_mfma_f32_16x16x32_bf16 v[124:127], v[208:211], a[124:127], v[124:127]\n"
    "s_add_u32 s46, %11, 11264\n"
    "v_rcp_f32 v43, v43\n"
    "v_mfma_f32_16x16x32_bf16 v[124:127], v[204:207], a[108:111], v[124:127]\n"
    "s_waitcnt lgkmcnt(2)\n"
    "buffer_load_dwordx4 a[80:83], v232, s[36:39], s46 offen sc1\n"
    "v_mul_f32 v40, v40, v56\n"
    "v_mfma_f32_16x16x32_bf16 v[128:131], v[192:195], a[96:99], v[128:131]\n"
    "ds_read_b64_tr_b16 v[204:205], v217 offset:35936\n"
    "v_mul_f32 v41, v41, v57\n"
    "v_mfma_f32_16x16x32_bf16 v[128:131], v[196:199], a[112:115], v[128:131]\n"
    "s_waitcnt lgkmcnt(1)\n"
    "ds_read_b64_tr_b16 v[206:207], v217 offset:36192\n"
    "v_mul_f32 v42, v42, v58\n"
    "v_mfma_f32_16x16x32_bf16 v[128:131], v[200:203], a[128:131], v[128:131]\n"
    "ds_read_b64_tr_b16 v[208:209], v217 offset:40160\n"
    "v_mul_f32 v43, v43, v59\n"
    "v_mfma_f32_16x16x32_bf16 v[132:135], v[200:203], a[132:135], v[132:135]\n"
    "s_add_u32 s47, %11, 157696\n"
    "ds_read_b64_tr_b16 v[210:211], v217 offset:40416\n"
    "v_cvt_pk_bf16_f32 v56, v40, v41\n"
    "v_mfma_f32_16x16x32_bf16 v[132:135], v[196:199], a[116:119], v[132:135]\n"
    "ds_read_b64_tr_b16 v[212:213], v217 offset:44384\n"
    "buffer_load_dwordx4 a[52:55], v232, s[36:39], s47 offen sc1\n"
    "v_mfma_f32_16x16x32_bf16 v[132:135], v[192:195], a[100:103], v[132:135]\n"
    "ds_read_b64_tr_b16 v[214:215], v217 offset:44640\n"
    "v_cvt_pk_bf16_f32 v57, v42, v43\n"
    "v_mfma_f32_16x16x32_bf16 v[136:139], v[192:195], a[104:107], v[136:139]\n"
    "v_rcp_f32 v44, v44\n"
    "v_mfma_f32_16x16x32_bf16 v[136:139], v[196:199], a[120:123], v[136:139]\n"
    "v_rcp_f32 v45, v45\n"
    "v_mfma_f32_16x16x32_bf16 v[136:139], v[200:203], a[136:139], v[136:139]\n"
    "s_add_u32 s48, %11, 149504\n"
    "v_rcp_f32 v46, v46\n"
    "v_mfma_f32_16x16x32_bf16 v[140:143], v[200:203], a[140:143], v[140:143]\n"
    "buffer_load_dwordx4 a[68:71], v232, s[36:39], s48 offen sc1\n"
    "v_mul_f32 v44, v44, v60\n"
    "v_mfma_f32_16x16x32_bf16 v[140:143], v[196:199], a[124:127], v[140:143]\n"
    "v_rcp_f32 v47, v47\n"
    "v_mfma_f32_16x16x32_bf16 v[140:143], v[192:195], a[108:111], v[140:143]\n"
    "s_waitcnt lgkmcnt(2)\n"
    "v_mul_f32 v45, v45, v61\n"
    "v_mul_f32 v46, v46, v62\n"
    "v_mfma_f32_16x16x32_bf16 v[144:147], v[204:207], a[96:99], v[144:147]\n"
    "ds_read_b64_tr_b16 v[192:193], v217 offset:48576\n"
    "v_mul_f32 v47, v47, v63\n"
    "v_mfma_f32_16x16x32_bf16 v[144:147], v[208:211], a[112:115], v[144:147]\n"
    "s_waitcnt lgkmcnt(1)\n"
    "s_add_u32 s49, %11, 158720\n"
    "ds_read_b64_tr_b16 v[194:195], v217 offset:48832\n"
    "v_mfma_f32_16x16x32_bf16 v[144:147], v[212:215], a[128:131], v[144:147]\n"
    "ds_read_b64_tr_b16 v[196:197], v217 offset:52800\n"
    "buffer_load_dwordx4 a[84:87], v232, s[36:39], s49 offen sc1\n"
    "v_mfma_f32_16x16x32_bf16 v[148:151], v[212:215], a[132:135], v[148:151]\n"
    "ds_read_b64_tr_b16 v[198:199], v217 offset:53056\n"
    "v_cvt_pk_bf16_f32 v58, v44, v45\n"
    "v_mfma_f32_16x16x32_bf16 v[148:151], v[208:211], a[116:119], v[148:151]\n"
    "ds_read_b64_tr_b16 v[200:201], v217 offset:57024\n"
    "v_cvt_pk_bf16_f32 v59, v46, v47\n"
    "v_mfma_f32_16x16x32_bf16 v[148:151], v[204:207], a[100:103], v[148:151]\n"
    "ds_read_b64_tr_b16 v[202:203], v217 offset:57280\n"
    "v_permlane16_swap_b32 v56, v58\n"
    "v_mfma_f32_16x16x32_bf16 v[152:155], v[204:207], a[104:107], v[152:155]\n"
    "s_add_u32 s44, %12, 10240\n"
    "v_permlane16_swap_b32 v57, v59\n"
    "v_mul_f32 v69, v69, v237\n"
    "v_mfma_f32_16x16x32_bf16 v[152:155], v[208:211], a[120:123], v[152:155]\n"
    "buffer_load_dwordx4 a[56:59], v232, s[36:39], s44 offen sc1\n"
    "v_mul_f32 v70, v70, v237\n"
    "v_mfma_f32_16x16x32_bf16 v[152:155], v[212:215], a[136:139], v[152:155]\n"
    "buffer_store_dwordx4 v[56:59], v251, s[36:39], s57 offen sc1\n"
    "v_mul_f32 v71, v71, v237\n"
    "v_mfma_f32_16x16x32_bf16 v[156:159], v[212:215], a[140:143], v[156:159]\n"
    "v_exp_f32 v64, v64\n"
    "v_mfma_f32_16x16x32_bf16 v[156:159], v[208:211], a[124:127], v[156:159]\n"
    "v_exp_f32 v65, v65\n"
    "v_mfma_f32_16x16x32_bf16 v[156:159], v[204:207], a[108:111], v[156:159]\n"
    "s_waitcnt lgkmcnt(2)\n"
    "s_add_u32 s45, %12, 2048\n"
    "v_fma_f32 v64, v64, v240, v240\n"
    "v_mfma_f32_16x16x32_bf16 v[96:99], v[192:195], a[144:147], v[96:99]\n"
    "ds_read_b64_tr_b16 v[204:205], v217 offset:61248\n"
    "buffer_load_dwordx4 a[72:75], v232, s[36:39], s45 offen sc1\n"
    "v_mfma_f32_16x16x32_bf16 v[96:99], v[196:199], a[160:163], v[96:99]\n"
    "s_waitcnt lgkmcnt(1)\n"
    "ds_read_b64_tr_b16 v[206:207], v217 offset:61504\n"
    "v_fma_f32 v65, v65, v240, v240\n"
    "v_mfma_f32_16x16x32_bf16 v[96:99], v[200:203], a[176:179], v[96:99]\n"
    "ds_read_b64_tr_b16 v[208:209], v217 offset:65472\n"
    "v_mfma_f32_16x16x32_bf16 v[100:103], v[200:203], a[180:183], v[100:103]\n"
    "ds_read_b64_tr_b16 v[210:211], v218 offset:192\n"
    "v_mfma_f32_16x16x32_bf16 v[100:103], v[196:199], a[164:167], v[100:103]\n"
    "s_add_u32 s46, %12, 11264\n"
    "ds_read_b64_tr_b16 v[212:213], v218 offset:4160\n"
    "v_mfma_f32_16x16x32_bf16 v[100:103], v[192:195], a[148:151], v[100:103]\n"
    "ds_read_b64_tr_b16 v[214:215], v218 offset:4416\n"
    "buffer_load_dwordx4 a[88:91], v232, s[36:39], s46 offen sc1\n"
    "v_mfma_f32_16x16x32_bf16 v[104:107], v[192:195], a[152:155], v[104:107]\n"
    "v_exp_f32 v66, v66\n"
    "v_mfma_f32_16x16x32_bf16 v[104:107], v[196:199], a[168:171], v[104:107]\n"
    "v_exp_f32 v67, v67\n"
    "v_mfma_f32_16x16x32_bf16 v[104:107], v[200:203], a[184:187], v[104:107]\n"
    "v_exp_f32 v68, v68\n"
    "v_mfma_f32_16x16x32_bf16 v[108:111], v[200:203], a[188:191], v[108:111]\n"
    "s_add_u32 s47, %12, 157696\n"
    "v_exp_f32 v69, v69\n"
    "v_mfma_f32_16x16x32_bf16 v[108:111], v[196:199], a[172:175], v[108:111]\n"
    "buffer_load_dwordx4 a[60:63], v232, s[36:39], s47 offen sc1\n"
    "v_fma_f32 v66, v66, v240, v240\n"
    "v_mfma_f32_16x16x32_bf16 v[108:111], v[192:195], a[156:159], v[108:111]\n"
    "s_waitcnt lgkmcnt(2)\n"
    "v_exp_f32 v70, v70\n"
    "v_mfma_f32_16x16x32_bf16 v[112:115], v[204:207], a[144:147], v[112:115]\n"
    "ds_read_b64_tr_b16 v[192:193], v218 offset:8384\n"
    "v_fma_f32 v67, v67, v240, v240\n"
    "v_mfma_f32_16x16x32_bf16 v[112:115], v[208:211], a[160:163], v[112:115]\n"
    "s_waitcnt lgkmcnt(1)\n"
    "ds_read_b64_tr_b16 v[194:195], v218 offset:8640\n"
    "v_fma_f32 v68, v68, v241, v241\n"
    "v_mfma_f32_16x16x32_bf16 v[112:115], v[212:215], a[176:179], v[112:115]\n"
    "s_add_u32 s48, %12, 149504\n"
    "ds_read_b64_tr_b16 v[196:197], v218 offset:12608\n"
    "v_fma_f32 v69, v69, v241, v241\n"
    "v_mfma_f32_16x16x32_bf16 v[116:119], v[212:215], a[180:183], v[116:119]\n"
    "s_add_u32 s58, %10, 5120\n"
    "ds_read_b64_tr_b16 v[198:199], v218 offset:12864\n"
    "buffer_load_dwordx4 a[76:79], v232, s[36:39], s48 offen sc1\n"
    "v_mfma_f32_16x16x32_bf16 v[116:119], v[208:211], a[164:167], v[116:119]\n"
    "ds_read_b64_tr_b16 v[200:201], v218 offset:16832\n"
    "v_fma_f32 v70, v70, v241, v241\n"
    "v_mfma_f32_16x16x32_bf16 v[116:119], v[204:207], a[148:151], v[116:119]\n"
    "ds_read_b64_tr_b16 v[202:203], v218 offset:17088\n"
    "v_mul_f32 v72, v72, v238\n"
    "v_mfma_f32_16x16x32_bf16 v[120:123], v[204:207], a[152:155], v[120:123]\n"
    "v_exp_f32 v71, v71\n"
    "v_mfma_f32_16x16x32_bf16 v[120:123], v[208:211], a[168:171], v[120:123]\n"
    "s_add_u32 s49, %12, 158720\n"
    "v_fma_f32 v71, v71, v241, v241\n"
    "v_mul_f32 v73, v73, v238\n"
    "v_mfma_f32_16x16x32_bf16 v[120:123], v[212:215], a[184:187], v[120:123]\n"
    "buffer_load_dwordx4 a[92:95], v232, s[36:39], s49 offen sc1\n"
    "v_mul_f32 v74, v74, v238\n"
    "v_mfma_f32_16x16x32_bf16 v[124:127], v[212:215], a[188:191], v[124:127]\n"
    "v_rcp_f32 v64, v64\n"
    "v_mfma_f32_16x16x32_bf16 v[124:127], v[208:211], a[172:175], v[124:127]\n"
    "v_rcp_f32 v65, v65\n"
    "v_mfma_f32_16x16x32_bf16 v[124:127], v[204:207], a[156:159], v[124:127]\n"
    "s_waitcnt lgkmcnt(2)\n"
    "v_rcp_f32 v66, v66\n"
    "buffer_load_dword v253, off, s[36:39], %13 offset:128 sc1\n"
    "v_mfma_f32_16x16x32_bf16 v[96:99], v[192:195], a[192:195], v[96:99]\n"
    "s_add_u32 m0, s56, 76032\n"
    "s_add_u32 s44, %6, 36864\n"
    "ds_read_b64_tr_b16 v[204:205], v216 offset:64\n"
    "v_mfma_f32_16x16x32_bf16 v[96:99], v[196:199], a[208:211], v[96:99]\n"
    "s_waitcnt lgkmcnt(1)\n"
    "ds_read_b64_tr_b16 v[206:207], v216 offset:320\n"
    "buffer_load_dwordx4 v232, s[36:39], s44 offen lds\n"
    "v_mfma_f32_16x16x32_bf16 v[96:99], v[200:203], a[224:227], v[96:99]\n"
    "ds_read_b64_tr_b16 v[208:209], v216 offset:4288\n"
    "v_mul_f32 v64, v64, v80\n"
    "v_mfma_f32_16x16x32_bf16 v[100:103], v[200:203], a[228:231], v[100:103]\n"
    "ds_read_b64_tr_b16 v[210:211], v216 offset:4544\n"
    "v_mul_f32 v65, v65, v81\n"
    "v_mfma_f32_16x16x32_bf16 v[100:103], v[196:199], a[212:215], v[100:103]\n"
    "ds_read_b64_tr_b16 v[212:213], v216 offset:8512\n"
    "v_mul_f32 v66, v66, v82\n"
    "v_mfma_f32_16x16x32_bf16 v[100:103], v[192:195], a[196:199], v[100:103]\n"
    "s_add_u32 m0, s56, 80256\n"
    "s_add_u32 s45, %6, 40960\n"
    "ds_read_b64_tr_b16 v[214:215], v216 offset:8768\n"
    "v_mfma_f32_16x16x32_bf16 v[104:107], v[192:195], a[200:203], v[104:107]\n"
    "buffer_load_dwordx4 v232, s[36:39], s45 offen lds\n"
    "v_cvt_pk_bf16_f32 v80, v64, v65\n"
    "v_mfma_f32_16x16x32_bf16 v[104:107], v[196:199], a[216:219], v[104:107]\n"
    "v_rcp_f32 v67, v67\n"
    "v_mfma_f32_16x16x32_bf16 v[104:107], v[200:203], a[232:235], v[104:107]\n"
    "v_rcp_f32 v68, v68\n"
    "v_mfma_f32_16x16x32_bf16 v[108:111], v[200:203], a[236:239], v[108:111]\n"
    "v_rcp_f32 v69, v69\n"
    "v_mfma_f32_16x16x32_bf16 v[108:111], v[196:199], a[220:223], v[108:111]\n"
    "s_add_u32 m0, s56, 84480\n"
    "s_add_u32 s46, %6, 45056\n"
    "v_mul_f32 v67, v67, v83\n"
    "v_mfma_f32_16x16x32_bf16 v[108:111], v[192:195], a[204:207], v[108:111]\n"
    "s_waitcnt lgkmcnt(2)\n"
    "buffer_load_dwordx4 v232, s[36:39], s46 offen lds\n"
    "v_mul_f32 v68, v68, v84\n"
    "v_mfma_f32_16x16x32_bf16 v[112:115], v[204:207], a[192:195], v[112:115]\n"
    "ds_read_b64_tr_b16 v[192:193], v217 offset:48608\n"
    "v_mul_f32 v69, v69, v85\n"
    "v_mfma_f32_16x16x32_bf16 v[112:115], v[208:211], a[208:211], v[112:115]\n"
    "s_waitcnt lgkmcnt(1)\n"
    "ds_read_b64_tr_b16 v[194:195], v217 offset:48864\n"
    "v_cvt_pk_bf16_f32 v81, v66, v67\n"
    "v_mfma_f32_16x16x32_bf16 v[112:115], v[212:215], a[224:227], v[112:115]\n"
    "ds_read_b64_tr_b16 v[196:197], v217 offset:52832\n"
    "v_cvt_pk_bf16_f32 v82, v68, v69\n"
    "v_mfma_f32_16x16x32_bf16 v[116:119], v[212:215], a[228:231], v[116:119]\n"
    "ds_read_b64_tr_b16 v[198:199], v217 offset:53088\n"
    "v_permlane16_swap_b32 v80, v82\n"
    "v_mfma_f32_16x16x32_bf16 v[116:119], v[208:211], a[212:215], v[116:119]\n"
    "ds_read_b64_tr_b16 v[200:201], v217 offset:57056\n"
    "v_mul_f32 v75, v75, v238\n"
    "v_mfma_f32_16x16x32_bf16 v[116:119], v[204:207], a[196:199], v[116:119]\n"
    "ds_read_b64_tr_b16 v[202:203], v217 offset:57312\n"
    "v_mul_f32 v76, v76, v239\n"
    "v_mfma_f32_16x16x32_bf16 v[120:123], v[204:207], a[200:203], v[120:123]\n"
    "v_rcp_f32 v70, v70\n"
    "v_mfma_f32_16x16x32_bf16 v[120:123], v[208:211], a[216:219], v[120:123]\n"
    "v_rcp_f32 v71, v71\n"
    "v_mfma_f32_16x16x32_bf16 v[120:123], v[212:215], a[232:235], v[120:123]\n"
    "v_mul_f32 v70, v70, v86\n"
    "v_mul_f32 v71, v71, v87\n"
    "v_mfma_f32_16x16x32_bf16 v[124:127], v[212:215], a[236:239], v[124:127]\n"
    "v_cvt_pk_bf16_f32 v83, v70, v71\n"
    "v_mul_f32 v77, v77, v239\n"
    "v_mfma_f32_16x16x32_bf16 v[124:127], v[208:211], a[220:223], v[124:127]\n"
    "v_permlane16_swap_b32 v81, v83\n"
    "v_mul_f32 v78, v78, v239\n"
    "v_mfma_f32_16x16x32_bf16 v[124:127], v[204:207], a[204:207], v[124:127]\n"
    "s_waitcnt lgkmcnt(2)\n"
    "buffer_store_dwordx4 v[80:83], v251, s[36:39], s58 offen sc1\n"
    "v_mul_f32 v79, v79, v239\n"
    "v_mfma_f32_16x16x32_bf16 v[128:131], v[192:195], a[144:147], v[128:131]\n"
    "ds_read_b64_tr_b16 v[204:205], v217 offset:61280\n"
    "v_mfma_f32_16x16x32_bf16 v[128:131], v[196:199], a[160:163], v[128:131]\n"
    "s_waitcnt lgkmcnt(1)\n"
    "ds_read_b64_tr_b16 v[206:207], v217 offset:61536\n"
    "v_mfma_f32_16x16x32_bf16 v[128:131], v[200:203], a[176:179], v[128:131]\n"
    "ds_read_b64_tr_b16 v[208:209], v217 offset:65504\n"
    "v_mfma_f32_16x16x32_bf16 v[132:135], v[200:203], a[180:183], v[132:135]\n"
    "ds_read_b64_tr_b16 v[210:211], v218 offset:224\n"
    "v_mfma_f32_16x16x32_bf16 v[132:135], v[196:199], a[164:167], v[132:135]\n"
    "ds_read_b64_tr_b16 v[212:213], v218 offset:4192\n"
    "v_mfma_f32_16x16x32_bf16 v[132:135], v[192:195], a[148:151], v[132:135]\n"
    "ds_read_b64_tr_b16 v[214:215], v218 offset:4448\n"
    "v_mfma_f32_16x16x32_bf16 v[136:139], v[192:195], a[152:155], v[136:139]\n"
    "v_exp_f32 v72, v72\n"
    "v_mfma_f32_16x16x32_bf16 v[136:139], v[196:199], a[168:171], v[136:139]\n"
    "v_exp_f32 v73, v73\n"
    "v_mfma_f32_16x16x32_bf16 v[136:139], v[200:203], a[184:187], v[136:139]\n"
    "v_exp_f32 v74, v74\n"
    "v_mfma_f32_16x16x32_bf16 v[140:143], v[200:203], a[188:191], v[140:143]\n"
    "v_exp_f32 v75, v75\n"
    "v_mfma_f32_16x16x32_bf16 v[140:143], v[196:199], a[172:175], v[140:143]\n"
    "v_exp_f32 v76, v76\n"
    "v_mfma_f32_16x16x32_bf16 v[140:143], v[192:195], a[156:159], v[140:143]\n"
    "s_waitcnt lgkmcnt(2)\n"
    "v_exp_f32 v77, v77\n"
    "v_mfma_f32_16x16x32_bf16 v[144:147], v[204:207], a[144:147], v[144:147]\n"
    "ds_read_b64_tr_b16 v[192:193], v216 offset:12736\n"
    "v_fma_f32 v72, v72, v242, v242\n"
    "v_mfma_f32_16x16x32_bf16 v[144:147], v[208:211], a[160:163], v[144:147]\n"
    "s_waitcnt lgkmcnt(1)\n"
    "ds_read_b64_tr_b16 v[194:195], v216 offset:12992\n"
    "v_fma_f32 v73, v73, v242, v242\n"
    "v_mfma_f32_16x16x32_bf16 v[144:147], v[212:215], a[176:179], v[144:147]\n"
    "ds_read_b64_tr_b16 v[196:197], v216 offset:16960\n"
    "v_fma_f32 v74, v74, v242, v242\n"
    "v_mfma_f32_16x16x32_bf16 v[148:151], v[212:215], a[180:183], v[148:151]\n"
    "ds_read_b64_tr_b16 v[198:199], v216 offset:17216\n"
    "v_fma_f32 v75, v75, v242, v242\n"
    "v_mfma_f32_16x16x32_bf16 v[148:151], v[208:211], a[164:167], v[148:151]\n"
    "ds_read_b64_tr_b16 v[200:201], v216 offset:21184\n"
    "v_fma_f32 v76, v76, v243, v243\n"
    "v_mfma_f32_16x16x32_bf16 v[148:151], v[204:207], a[148:151], v[148:151]\n"
    "s_waitcnt vmcnt(4)\n"
    "ds_read_b64_tr_b16 v[202:203], v216 offset:21440\n"
    "v_fma_f32 v77, v77, v243, v243\n"
    "v_mfma_f32_16x16x32_bf16 v[152:155], v[204:207], a[152:155], v[152:155]\n"
    "v_exp_f32 v78, v78\n"
    "v_mfma_f32_16x16x32_bf16 v[152:155], v[208:211], a[168:171], v[152:155]\n"
    "v_readfirstlane_b32 s51, v253\n"
    "v_fma_f32 v78, v78, v243, v243\n"
    "v_mfma_f32_16x16x32_bf16 v[152:155], v[212:215], a[184:187], v[152:155]\n"
    "v_exp_f32 v79, v79\n"
    "v_mfma_f32_16x16x32_bf16 v[156:159], v[212:215], a[188:191], v[156:159]\n"
    "v_fma_f32 v79, v79, v243, v243\n"
    "v_mfma_f32_16x16x32_bf16 v[156:159], v[208:211], a[172:175], v[156:159]\n"
    "s_cmp_lt_u32 s51, 8\n"
    "s_cbranch_scc1 .Lps5_%=\n"
    ".Lpr5_%=:\n"
    "s_add_u32 s59, %10, 300032\n"
    "v_mfma_f32_16x16x32_bf16 v[156:159], v[204:207], a[156:159], v[156:159]\n"
    "v_rcp_f32 v72, v72\n"
    "s_barrier\n"
    "s_waitcnt lgkmcnt(2)\n"
    "v_mfma_f32_16x16x32_bf16 v[96:99], v[192:195], a[240:243], v[96:99]\n"
    "s_add_u32 m0, s56, 88704\n"
    "s_add_u32 s47, %6, 49152\n"
    "ds_read_b64_tr_b16 v[204:205], v216 offset:25408\n"
    "v_mfma_f32_16x16x32_bf16 v[96:99], v[196:199], v[0:3], v[96:99]\n"
    "s_waitcnt lgkmcnt(1)\n"
    "ds_read_b64_tr_b16 v[206:207], v216 offset:25664\n"
    "buffer_load_dwordx4 v232, s[36:39], s47 offen lds\n"
    "v_mfma_f32_16x16x32_bf16 v[96:99], v[200:203], v[16:19], v[96:99]\n"
    "s_add_u32 m0, s56, 92928\n"
    "s_add_u32 s48, %6, 53248\n"
    "ds_read_b64_tr_b16 v[208:209], v216 offset:29632\n"
    "v_mfma_f32_16x16x32_bf16 v[100:103], v[200:203], v[20:23], v[100:103]\n"
    "ds_read_b64_tr_b16 v[210:211], v216 offset:29888\n"
    "buffer_load_dwordx4 v232, s[36:39], s48 offen lds\n"
    "v_mfma_f32_16x16x32_bf16 v[100:103], v[196:199], v[4:7], v[100:103]\n"
    "ds_read_b64_tr_b16 v[212:213], v216 offset:33856\n"
    "v_mul_f32 v72, v72, v88\n"
    "v_mfma_f32_16x16x32_bf16 v[100:103], v[192:195], a[244:247], v[100:103]\n"
    "ds_read_b64_tr_b16 v[214:215], v216 offset:34112\n"
    "v_mfma_f32_16x16x32_bf16 v[104:107], v[192:195], a[248:251], v[104:107]\n"
    "v_rcp_f32 v73, v73\n"
    "v_mfma_f32_16x16x32_bf16 v[104:107], v[196:199], v[8:11], v[104:107]\n"
    "s_add_u32 m0, s56, 97152\n"
    "s_add_u32 s49, %6, 57344\n"
    "v_mul_f32 v73, v73, v89\n"
    "v_mfma_f32_16x16x32_bf16 v[104:107], v[200:203], v[24:27], v[104:107]\n"
    "buffer_load_dwordx4 v232, s[36:39], s49 offen lds\n"
    "v_cvt_pk_bf16_f32 v88, v72, v73\n"
    "v_mfma_f32_16x16x32_bf16 v[108:111], v[200:203], v[28:31], v[108:111]\n"
    "v_rcp_f32 v74, v74\n"
    "v_mfma_f32_16x16x32_bf16 v[108:111], v[196:199], v[12:15], v[108:111]\n"
    "v_rcp_f32 v75, v75\n"
    "v_mfma_f32_16x16x32_bf16 v[108:111], v[192:195], a[252:255], v[108:111]\n"
    "s_waitcnt lgkmcnt(2)\n"
    "v_rcp_f32 v76, v76\n"
    "v_mfma_f32_16x16x32_bf16 v[112:115], v[204:207], a[240:243], v[112:115]\n"
    "s_add_u32 m0, s56, 101376\n"
    "s_add_u32 s44, %6, 61440\n"
    "ds_read_b64_tr_b16 v[192:193], v218 offset:8416\n"
    "v_mfma_f32_16x16x32_bf16 v[112:115], v[208:211], v[0:3], v[112:115]\n"
    "s_waitcnt lgkmcnt(1)\n"
    "ds_read_b64_tr_b16 v[194:195], v218 offset:8672\n"
    "buffer_load_dwordx4 v232, s[36:39], s44 offen lds\n"
    "v_mfma_f32_16x16x32_bf16 v[112:115], v[212:215], v[16:19], v[112:115]\n"
    "ds_read_b64_tr_b16 v[196:197], v218 offset:12640\n"
    "v_mul_f32 v74, v74, v90\n"
    "v_mfma_f32_16x16x32_bf16 v[116:119], v[212:215], v[20:23], v[116:119]\n"
    "ds_read_b64_tr_b16 v[198:199], v218 offset:12896\n"
    "v_mul_f32 v75, v75, v91\n"
    "v_mfma_f32_16x16x32_bf16 v[116:119], v[208:211], v[4:7], v[116:119]\n"
    "ds_read_b64_tr_b16 v[200:201], v218 offset:16864\n"
    "v_mul_f32 v76, v76, v92\n"
    "v_mfma_f32_16x16x32_bf16 v[116:119], v[204:207], a[244:247], v[116:119]\n"
    "s_add_u32 m0, s56, 105600\n"
    "s_add_u32 s45, %6, 65536\n"
    "ds_read_b64_tr_b16 v[202:203], v218 offset:17120\n"
    "v_mfma_f32_16x16x32_bf16 v[120:123], v[204:207], a[248:251], v[120:123]\n"
    "buffer_load_dwordx4 v232, s[36:39], s45 offen lds\n"
    "v_cvt_pk_bf16_f32 v89, v74, v75\n"
    "v_mfma_f32_16x16x32_bf16 v[120:123], v[208:211], v[8:11], v[120:123]\n"
    "v_rcp_f32 v77, v77\n"
    "v_mfma_f32_16x16x32_bf16 v[120:123], v[212:215], v[24:27], v[120:123]\n"
    "v_rcp_f32 v78, v78\n"
    "v_mfma_f32_16x16x32_bf16 v[124:127], v[212:215], v[28:31], v[124:127]\n"
    "v_rcp_f32 v79, v79\n"
    "v_mfma_f32_16x16x32_bf16 v[124:127], v[208:211], v[12:15], v[124:127]\n"
    "s_add_u32 m0, s56, 109824\n"
    "s_add_u32 s46, %6, 69632\n"
    "v_mul_f32 v77, v77, v93\n"
    "v_mfma_f32_16x16x32_bf16 v[124:127], v[204:207], a[252:255], v[124:127]\n"
    "s_waitcnt lgkmcnt(2)\n"
    "buffer_load_dwordx4 v232, s[36:39], s46 offen lds\n"
    "v_mul_f32 v78, v78, v94\n"
    "v_mfma_f32_16x16x32_bf16 v[128:131], v[192:195], a[192:195], v[128:131]\n"
    "ds_read_b64_tr_b16 v[204:205], v216 offset:96\n"
    "v_mul_f32 v79, v79, v95\n"
    "v_mfma_f32_16x16x32_bf16 v[128:131], v[196:199], a[208:211], v[128:131]\n"
    "s_waitcnt lgkmcnt(1)\n"
    "ds_read_b64_tr_b16 v[206:207], v216 offset:352\n"
    "v_cvt_pk_bf16_f32 v90, v76, v77\n"
    "v_mfma_f32_16x16x32_bf16 v[128:131], v[200:203], a[224:227], v[128:131]\n"
    "ds_read_b64_tr_b16 v[208:209], v216 offset:4320\n"
    "v_cvt_pk_bf16_f32 v91, v78, v79\n"
    "v_mfma_f32_16x16x32_bf16 v[132:135], v[200:203], a[228:231], v[132:135]\n"
    "s_add_u32 m0, s56, 114048\n"
    "s_add_u32 s47, %6, 73728\n"
    "ds_read_b64_tr_b16 v[210:211], v216 offset:4576\n"
    "v_mfma_f32_16x16x32_bf16 v[132:135], v[196:199], a[212:215], v[132:135]\n"
    "ds_read_b64_tr_b16 v[212:213], v216 offset:8544\n"
    "buffer_load_dwordx4 v232, s[36:39], s47 offen lds\n"
    "v_mfma_f32_16x16x32_bf16 v[132:135], v[192:195], a[196:199], v[132:135]\n"
    "ds_read_b64_tr_b16 v[214:215], v216 offset:8800\n"
    "v_permlane16_swap_b32 v88, v90\n"
    "v_mfma_f32_16x16x32_bf16 v[136:139], v[192:195], a[200:203], v[136:139]\n"
    "v_permlane16_swap_b32 v89, v91\n"
    "v_mul_f32 v96, v96, v236\n"
    "v_mfma_f32_16x16x32_bf16 v[136:139], v[196:199], a[216:219], v[136:139]\n"
    "buffer_store_dwordx4 v[88:91], v251, s[36:39], s59 offen sc1\n"
    "v_mul_f32 v97, v97, v236\n"
    "v_mfma_f32_16x16x32_bf16 v[136:139], v[200:203], a[232:235], v[136:139]\n"
    "s_add_u32 m0, s56, 118272\n"
    "s_add_u32 s48, %6, 77824\n"
    "v_mul_f32 v98, v98, v236\n"
    "v_mfma_f32_16x16x32_bf16 v[140:143], v[200:203], a[236:239], v[140:143]\n"
    "buffer_load_dwordx4 v232, s[36:39], s48 offen lds\n"
    "v_mul_f32 v99, v99, v236\n"
    "v_mfma_f32_16x16x32_bf16 v[140:143], v[196:199], a[220:223], v[140:143]\n"
    "v_mul_f32 v100, v100, v237\n"
    "v_mul_f32 v101, v101, v237\n"
    "v_mfma_f32_16x16x32_bf16 v[140:143], v[192:195], a[204:207], v[140:143]\n"
    "s_waitcnt lgkmcnt(2)\n"
    "v_mul_f32 v102, v102, v237\n"
    "v_mul_f32 v103, v103, v237\n"
    "v_mfma_f32_16x16x32_bf16 v[144:147], v[204:207], a[192:195], v[144:147]\n"
    "ds_read_b64_tr_b16 v[192:193], v216 offset:12768\n"
    "v_mfma_f32_16x16x32_bf16 v[144:147], v[208:211], a[208:211], v[144:147]\n"
    "s_waitcnt lgkmcnt(1)\n"
    "s_add_u32 m0, s56, 122496\n"
    "ds_read_b64_tr_b16 v[194:195], v216 offset:13024\n"
    "v_mfma_f32_16x16x32_bf16 v[144:147], v[212:215], a[224:227], v[144:147]\n"
    "s_add_u32 s49, %6, 81920\n"
    "ds_read_b64_tr_b16 v[196:197], v216 offset:16992\n"
    "buffer_load_dwordx4 v232, s[36:39], s49 offen lds\n"
    "v_mfma_f32_16x16x32_bf16 v[148:151], v[212:215], a[228:231], v[148:151]\n"
    "ds_read_b64_tr_b16 v[198:199], v216 offset:17248\n"
    "v_mfma_f32_16x16x32_bf16 v[148:151], v[208:211], a[212:215], v[148:151]\n"
    "ds_read_b64_tr_b16 v[200:201], v216 offset:21216\n"
    "v_mfma_f32_16x16x32_bf16 v[148:151], v[204:207], a[196:199], v[148:151]\n"
    "ds_read_b64_tr_b16 v[202:203], v216 offset:21472\n"
    "v_mfma_f32_16x16x32_bf16 v[152:155], v[204:207], a[200:203], v[152:155]\n"
    "s_add_u32 s44, %11, 3072\n"
    "v_exp_f32 v96, v96\n"
    "v_mfma_f32_16x16x32_bf16 v[152:155], v[208:211], a[216:219], v[152:155]\n"
    "buffer_load_dwordx4 a[96:99], v232, s[36:39], s44 offen sc1\n"
    "v_fma_f32 v96, v96, v240, v240\n"
    "v_mfma_f32_16x16x32_bf16 v[152:155], v[212:215], a[232:235], v[152:155]\n"
    "v_exp_f32 v97, v97\n"
    "v_mfma_f32_16x16x32_bf16 v[156:159], v[212:215], a[236:239], v[156:159]\n"
    "v_exp_f32 v98, v98\n"
    "v_mfma_f32_16x16x32_bf16 v[156:159], v[208:211], a[220:223], v[156:159]\n"
    "v_exp_f32 v99, v99\n"
    "v_mfma_f32_16x16x32_bf16 v[156:159], v[204:207], a[204:207], v[156:159]\n"
    "s_waitcnt lgkmcnt(2)\n"
    "s_add_u32 s45, %11, 12288\n"
    "v_fma_f32 v97, v97, v240, v240\n"
    "v_mfma_f32_16x16x32_bf16 v[128:131], v[192:195], a[240:243], v[128:131]\n"
    "ds_read_b64_tr_b16 v[204:205], v216 offset:25440\n"
    "buffer_load_dwordx4 a[112:115], v232, s[36:39], s45 offen sc1\n"
    "v_mfma_f32_16x16x32_bf16 v[128:131], v[196:199], v[0:3], v[128:131]\n"
    "s_waitcnt lgkmcnt(1)\n"
    "ds_read_b64_tr_b16 v[206:207], v216 offset:25696\n"
    "v_fma_f32 v98, v98, v240, v240\n"
    "v_mfma_f32_16x16x32_bf16 v[128:131], v[200:203], v[16:19], v[128:131]\n"
    "ds_read_b64_tr_b16 v[208:209], v216 offset:29664\n"
    "v_fma_f32 v99, v99, v240, v240\n"
    "v_mfma_f32_16x16x32_bf16 v[132:135], v[200:203], v[20:23], v[132:135]\n"
    "ds_read_b64_tr_b16 v[210:211], v216 offset:29920\n"
    "v_mfma_f32_16x16x32_bf16 v[132:135], v[196:199], v[4:7], v[132:135]\n"
    "s_add_u32 s46, %11, 8192\n"
    "ds_read_b64_tr_b16 v[212:213], v216 offset:33888\n"
    "v_mfma_f32_16x16x32_bf16 v[132:135], v[192:195], a[244:247], v[132:135]\n"
    "ds_read_b64_tr_b16 v[214:215], v216 offset:34144\n"
    "buffer_load_dwordx4 a[128:131], v232, s[36:39], s46 offen sc1\n"
    "v_mfma_f32_16x16x32_bf16 v[136:139], v[192:195], a[248:251], v[136:139]\n"
    "v_exp_f32 v100, v100\n"
    "v_mfma_f32_16x16x32_bf16 v[136:139], v[196:199], v[8:11], v[136:139]\n"
    "v_exp_f32 v101, v101\n"
    "v_mfma_f32_16x16x32_bf16 v[136:139], v[200:203], v[24:27], v[136:139]\n"
    "v_exp_f32 v102, v102\n"
    "v_mfma_f32_16x16x32_bf16 v[140:143], v[200:203], v[28:31], v[140:143]\n"
    "s_add_u32 s47, %11, 150528\n"
    "v_exp_f32 v103, v103\n"
    "v_mfma_f32_16x16x32_bf16 v[140:143], v[196:199], v[12:15], v[140:143]\n"
    "buffer_load_dwordx4 a[100:103], v232, s[36:39], s47 offen sc1\n"
    "v_fma_f32 v100, v100, v241, v241\n"
    "v_mfma_f32_16x16x32_bf16 v[140:143], v[192:195], a[252:255], v[140:143]\n"
    "s_waitcnt lgkmcnt(2)\n"
    "v_fma_f32 v101, v101, v241, v241\n"
    "v_fma_f32 v102, v102, v241, v241\n"
    "s_lshr_b32 s51, %7, 1\n"
    "s_mul_i32 s51, s51, 1056\n"
    "v_add_u32 v233, s51, v233\n"
    "v_add_u32 v234, s51, v234\n"
    "v_add_u32 v235, s51, v235\n"
    "v_mfma_f32_16x16x32_bf16 v[144:147], v[204:207], a[240:243], v[144:147]\n"
    "ds_read_b128 v[192:195], v233 offset:38016\n"
    "v_fma_f32 v103, v103, v241, v241\n"
    "v_mfma_f32_16x16x32_bf16 v[144:147], v[208:211], v[0:3], v[144:147]\n"
    "s_waitcnt lgkmcnt(1)\n"
    "s_add_u32 s57, %10, 6144\n"
    "ds_read_b128 v[196:199], v233 offset:40128\n"
    "v_mfma_f32_16x16x32_bf16 v[144:147], v[212:215], v[16:19], v[144:147]\n"
    "s_add_u32 s48, %11, 159744\n"
    "ds_read_b128 v[200:203], v233 offset:42240\n"
    "v_mul_f32 v104, v104, v238\n"
    "v_mfma_f32_16x16x32_bf16 v[148:151], v[212:215], v[20:23], v[148:151]\n"
    "buffer_load_dwordx4 a[116:119], v232, s[36:39], s48 offen sc1\n"
    "v_mul_f32 v105, v105, v238\n"
    "v_mfma_f32_16x16x32_bf16 v[148:151], v[208:211], v[4:7], v[148:151]\n"
    "v_rcp_f32 v96, v96\n"
    "v_mfma_f32_16x16x32_bf16 v[148:151], v[204:207], a[244:247], v[148:151]\n"
    "v_rcp_f32 v97, v97\n"
    "v_mfma_f32_16x16x32_bf16 v[152:155], v[204:207], a[248:251], v[152:155]\n"
    "v_rcp_f32 v98, v98\n"
    "v_mfma_f32_16x16x32_bf16 v[152:155], v[208:211], v[8:11], v[152:155]\n"
    "s_add_u32 s49, %11, 155648\n"
    "v_rcp_f32 v99, v99\n"
    "v_mfma_f32_16x16x32_bf16 v[152:155], v[212:215], v[24:27], v[152:155]\n"
    "buffer_load_dwordx4 a[132:135], v232, s[36:39], s49 offen sc1\n"
    "v_mul_f32 v96, v96, v112\n"
    "v_mfma_f32_16x16x32_bf16 v[156:159], v[212:215], v[28:31], v[156:159]\n"
    "v_rcp_f32 v100, v100\n"
    "v_mfma_f32_16x16x32_bf16 v[156:159], v[208:211], v[12:15], v[156:159]\n"
    "v_rcp_f32 v101, v101\n"
    "v_mfma_f32_16x16x32_bf16 v[156:159], v[204:207], a[252:255], v[156:159]\n"
    "s_waitcnt lgkmcnt(0)\n"
    "v_rcp_f32 v102, v102\n"
    "v_mfma_f32_16x16x32_bf16 v[160:163], v[192:195], a[0:3], 0\n"
    "s_add_u32 s44, %12, 3072\n"
    "ds_read_b128 v[204:207], v233 offset:44352\n"
    "v_mul_f32 v97, v97, v113\n"
    "v_mfma_f32_16x16x32_bf16 v[160:163], v[196:199], a[16:19], v[160:163]\n"
    "ds_read_b128 v[208:211], v233 offset:46464\n"
    "buffer_load_dwordx4 a[104:107], v232, s[36:39], s44 offen sc1\n"
    "v_mfma_f32_16x16x32_bf16 v[160:163], v[200:203], a[32:35], v[160:163]\n"
    "ds_read_b128 v[212:215], v233 offset:48576\n"
    "v_mul_f32 v98, v98, v114\n"
    "v_mfma_f32_16x16x32_bf16 v[164:167], v[200:203], a[36:39], 0\n"
    "v_rcp_f32 v103, v103\n"
    "v_mfma_f32_16x16x32_bf16 v[164:167], v[196:199], a[20:23], v[164:167]\n"
    "v_mul_f32 v99, v99, v115\n"
    "v_mul_f32 v100, v100, v116\n"
    "v_mfma_f32_16x16x32_bf16 v[164:167], v[192:195], a[4:7], v[164:167]\n"
    "s_add_u32 s45, %12, 12288\n"
    "v_mul_f32 v101, v101, v117\n"
    "v_mul_f32 v102, v102, v118\n"
    "v_mfma_f32_16x16x32_bf16 v[168:171], v[192:195], a[8:11], 0\n"
    "buffer_load_dwordx4 a[120:123], v232, s[36:39], s45 offen sc1\n"
    "v_mul_f32 v103, v103, v119\n"
    "v_mfma_f32_16x16x32_bf16 v[168:171], v[196:199], a[24:27], v[168:171]\n"
    "v_cvt_pk_bf16_f32 v112, v96, v97\n"
    "v_cvt_pk_bf16_f32 v113, v98, v99\n"
    "v_mfma_f32_16x16x32_bf16 v[168:171], v[200:203], a[40:43], v[168:171]\n"
    "v_cvt_pk_bf16_f32 v114, v100, v101\n"
    "v_cvt_pk_bf16_f32 v115, v102, v103\n"
    "v_mfma_f32_16x16x32_bf16 v[172:175], v[200:203], a[44:47], 0\n"
    "v_permlane16_swap_b32 v112, v114\n"
    "v_permlane16_swap_b32 v113, v115\n"
    "v_mfma_f32_16x16x32_bf16 v[172:175], v[196:199], a[28:31], v[172:175]\n"
    "s_add_u32 s46, %12, 8192\n"
    "buffer_store_dwordx4 v[112:115], v251, s[36:39], s57 offen sc1\n"
    "v_mul_f32 v106, v106, v238\n"
    "v_mfma_f32_16x16x32_bf16 v[172:175], v[192:195], a[12:15], v[172:175]\n"
    "s_waitcnt lgkmcnt(0)\n"
    "buffer_load_dwordx4 a[136:139], v232, s[36:39], s46 offen sc1\n"
    "v_mul_f32 v107, v107, v238\n"
    "v_mfma_f32_16x16x32_bf16 v[176:179], v[204:207], a[0:3], 0\n"
    "ds_read_b128 v[192:195], v233 offset:50688\n"
    "v_mul_f32 v108, v108, v239\n"
    "v_mfma_f32_16x16x32_bf16 v[176:179], v[208:211], a[16:19], v[176:179]\n"
    "ds_read_b128 v[196:199], v233 offset:52800\n"
    "v_mul_f32 v109, v109, v239\n"
    "v_mfma_f32_16x16x32_bf16 v[176:179], v[212:215], a[32:35], v[176:179]\n"
    "ds_read_b128 v[200:203], v233 offset:54912\n"
    "v_mul_f32 v110, v110, v239\n"
    "v_mfma_f32_16x16x32_bf16 v[180:183], v[212:215], a[36:39], 0\n"
    "s_add_u32 s47, %12, 150528\n"
    "v_mul_f32 v111, v111, v239\n"
    "v_mfma_f32_16x16x32_bf16 v[180:183], v[208:211], a[20:23], v[180:183]\n"
    "buffer_load_dwordx4 a[108:111], v232, s[36:39], s47 offen sc1\n"
    "v_mfma_f32_16x16x32_bf16 v[180:183], v[204:207], a[4:7], v[180:183]\n"
    "v_exp_f32 v104, v104\n"
    "v_mfma_f32_16x16x32_bf16 v[184:187], v[204:207], a[8:11], 0\n"
    "v_exp_f32 v105, v105\n"
    "v_mfma_f32_16x16x32_bf16 v[184:187], v[208:211], a[24:27], v[184:187]\n"
    "v_exp_f32 v106, v106\n"
    "v_mfma_f32_16x16x32_bf16 v[184:187], v[212:215], a[40:43], v[184:187]\n"
    "s_add_u32 s48, %12, 159744\n"
    "v_exp_f32 v107, v107\n"
    "v_mfma_f32_16x16x32_bf16 v[188:191], v[212:215], a[44:47], 0\n"
    "buffer_load_dwordx4 a[124:127], v232, s[36:39], s48 offen sc1\n"
    "v_fma_f32 v104, v104, v242, v242\n"
    "v_mfma_f32_16x16x32_bf16 v[188:191], v[208:211], a[28:31], v[188:191]\n"
    "v_exp_f32 v108, v108\n"
    "v_mfma_f32_16x16x32_bf16 v[188:191], v[204:207], a[12:15], v[188:191]\n"
    "s_waitcnt lgkmcnt(0)\n"
    "v_exp_f32 v109, v109\n"
    "v_mfma_f32_16x16x32_bf16 v[32:35], v[192:195], a[0:3], 0\n"
    "ds_read_b128 v[204:207], v233 offset:57024\n"
    "v_fma_f32 v105, v105, v242, v242\n"
    "v_mfma_f32_16x16x32_bf16 v[32:35], v[196:199], a[16:19], v[32:35]\n"
    "s_add_u32 s49, %12, 155648\n"
    "ds_read_b128 v[208:211], v233 offset:59136\n"
    "v_fma_f32 v106, v106, v242, v242\n"
    "v_mfma_f32_16x16x32_bf16 v[32:35], v[200:203], a[32:35], v[32:35]\n"
    "s_add_u32 s58, %10, 301056\n"
    "ds_read_b128 v[212:215], v233 offset:61248\n"
    "buffer_load_dwordx4 a[140:143], v232, s[36:39], s49 offen sc1\n"
    "v_mfma_f32_16x16x32_bf16 v[36:39], v[200:203], a[36:39], 0\n"
    "v_exp_f32 v110, v110\n"
    "v_mfma_f32_16x16x32_bf16 v[36:39], v[196:199], a[20:23], v[36:39]\n"
    "v_exp_f32 v111, v111\n"
    "v_mfma_f32_16x16x32_bf16 v[36:39], v[192:195], a[4:7], v[36:39]\n"
    "v_fma_f32 v107, v107, v242, v242\n"
    "v_fma_f32 v108, v108, v243, v243\n"
    "v_mfma_f32_16x16x32_bf16 v[40:43], v[192:195], a[8:11], 0\n"
    "s_add_u32 m0, s56, 126720\n"
    "s_add_u32 s44, %6, 86016\n"
    "v_fma_f32 v109, v109, v243, v243\n"
    "v_mfma_f32_16x16x32_bf16 v[40:43], v[196:199], a[24:27], v[40:43]\n"
    "buffer_load_dwordx4 v232, s[36:39], s44 offen lds\n"
    "v_fma_f32 v110, v110, v243, v243\n"
    "v_mfma_f32_16x16x32_bf16 v[40:43], v[200:203], a[40:43], v[40:43]\n"
    "v_fma_f32 v111, v111, v243, v243\n"
    "v_mul_f32 v128, v128, v236\n"
    "v_mfma_f32_16x16x32_bf16 v[44:47], v[200:203], a[44:47], 0\n"
    "v_rcp_f32 v104, v104\n"
    "v_mfma_f32_16x16x32_bf16 v[44:47], v[196:199], a[28:31], v[44:47]\n"
    "v_rcp_f32 v105, v105\n"
    "v_mfma_f32_16x16x32_bf16 v[44:47], v[192:195], a[12:15], v[44:47]\n"
    "s_waitcnt lgkmcnt(0)\n"
    "s_add_u32 m0, s56, 130944\n"
    "s_add_u32 s45, %6, 90112\n"
    "v_mfma_f32_16x16x32_bf16 v[48:51], v[204:207], a[0:3], 0\n"
    "ds_read_b128 v[192:195], v233 offset:63360\n"
    "buffer_load_dwordx4 v232, s[36:39], s45 offen lds\n"
    "v_mfma_f32_16x16x32_bf16 v[48:51], v[208:211], a[16:19], v[48:51]\n"
    "ds_read_b128 v[196:199], v233 offset:65472\n"
    "v_mul_f32 v104, v104, v120\n"
    "v_mfma_f32_16x16x32_bf16 v[48:51], v[212:215], a[32:35], v[48:51]\n"
    "ds_read_b128 v[200:203], v234 offset:2048\n"
    "v_mul_f32 v105, v105, v121\n"
    "v_mfma_f32_16x16x32_bf16 v[52:55], v[212:215], a[36:39], 0\n"
    "v_rcp_f32 v106, v106\n"
    "v_mfma_f32_16x16x32_bf16 v[52:55], v[208:211], a[20:23], v[52:55]\n"
    "s_add_u32 m0, s56, 135168\n"
    "s_add_u32 s46, %6, 94208\n"
    "v_mul_f32 v106, v106, v122\n"
    "v_mfma_f32_16x16x32_bf16 v[52:55], v[204:207], a[4:7], v[52:55]\n"
    "buffer_load_dwordx4 v232, s[36:39], s46 offen lds\n"
    "v_cvt_pk_bf16_f32 v120, v104, v105\n"
    "v_mfma_f32_16x16x32_bf16 v[56:59], v[204:207], a[8:11], 0\n"
    "v_rcp_f32 v107, v107\n"
    "v_mfma_f32_16x16x32_bf16 v[56:59], v[208:211], a[24:27], v[56:59]\n"
    "v_rcp_f32 v108, v108\n"
    "v_mfma_f32_16x16x32_bf16 v[56:59], v[212:215], a[40:43], v[56:59]\n"
    "v_rcp_f32 v109, v109\n"
    "v_mfma_f32_16x16x32_bf16 v[60:63], v[212:215], a[44:47], 0\n"
    "s_add_u32 s47, %11, 17408\n"
    "v_rcp_f32 v110, v110\n"
    "v_mfma_f32_16x16x32_bf16 v[60:63], v[208:211], a[28:31], v[60:63]\n"
    "buffer_load_dwordx4 a[144:147], v232, s[36:39], s47 offen sc1\n"
    "v_mul_f32 v107, v107, v123\n"
    "v_mfma_f32_16x16x32_bf16 v[60:63], v[204:207], a[12:15], v[60:63]\n"
    "s_waitcnt lgkmcnt(0)\n"
    "v_rcp_f32 v111, v111\n"
    "v_mfma_f32_16x16x32_bf16 v[64:67], v[192:195], a[0:3], 0\n"
    "ds_read_b128 v[204:207], v234 offset:4160\n"
    "v_mul_f32 v108, v108, v124\n"
    "v_mfma_f32_16x16x32_bf16 v[64:67], v[196:199], a[16:19], v[64:67]\n"
    "ds_read_b128 v[208:211], v234 offset:6272\n"
    "v_mul_f32 v109, v109, v125\n"
    "v_mfma_f32_16x16x32_bf16 v[64:67], v[200:203], a[32:35], v[64:67]\n"
    "s_add_u32 s48, %11, 164864\n"
    "ds_read_b128 v[212:215], v234 offset:8384\n"
    "v_mul_f32 v110, v110, v126\n"
    "v_mfma_f32_16x16x32_bf16 v[68:71], v[200:203], a[36:39], 0\n"
    "buffer_load_dwordx4 a[148:151], v232, s[36:39], s48 offen sc1\n"
    "v_mul_f32 v111, v111, v127\n"
    "v_mfma_f32_16x16x32_bf16 v[68:71], v[196:199], a[20:23], v[68:71]\n"
    "v_cvt_pk_bf16_f32 v121, v106, v107\n"
    "v_cvt_pk_bf16_f32 v122, v108, v109\n"
    "v_mfma_f32_16x16x32_bf16 v[68:71], v[192:195], a[4:7], v[68:71]\n"
    "v_cvt_pk_bf16_f32 v123, v110, v111\n"
    "v_permlane16_swap_b32 v120, v122\n"
    "v_mfma_f32_16x16x32_bf16 v[72:75], v[192:195], a[8:11], 0\n"
    "v_permlane16_swap_b32 v121, v123\n"
    "v_mul_f32 v129, v129, v236\n"
    "v_mfma_f32_16x16x32_bf16 v[72:75], v[196:199], a[24:27], v[72:75]\n"
    "s_add_u32 s49, %12, 17408\n"
    "buffer_store_dwordx4 v[120:123], v251, s[36:39], s58 offen sc1\n"
    "v_mul_f32 v130, v130, v236\n"
    "v_mfma_f32_16x16x32_bf16 v[72:75], v[200:203], a[40:43], v[72:75]\n"
    "buffer_load_dwordx4 a[152:155], v232, s[36:39], s49 offen sc1\n"
    "v_mul_f32 v131, v131, v236\n"
    "v_mfma_f32_16x16x32_bf16 v[76:79], v[200:203], a[44:47], 0\n"
    "v_mul_f32 v132, v132, v237\n"
    "v_mul_f32 v133, v133, v237\n"
    "v_mfma_f32_16x16x32_bf16 v[76:79], v[196:199], a[28:31], v[76:79]\n"
    "v_mul_f32 v134, v134, v237\n"
    "v_mul_f32 v135, v135, v237\n"
    "v_mfma_f32_16x16x32_bf16 v[76:79], v[192:195], a[12:15], v[76:79]\n"
    "v_exp_f32 v128, v128\n"
    "s_waitcnt vmcnt(20)\n"
    "s_barrier\n"
    "s_cmp_lg_u32 %7, 0\n"
    "s_cbranch_scc1 .Lsig6_%=\n"
    "s_mov_b64 exec, 1\n"
    "v_mov_b32 v245, 1\n"
    "buffer_atomic_add v245, v250, s[36:39], %13 offen offset:256\n"
    "s_mov_b64 exec, -1\n"
    "s_nop 1\n"
    ".Lsig6_%=:\n"
    "s_waitcnt lgkmcnt(0)\n"
    "v_mfma_f32_16x16x32_bf16 v[80:83], v[204:207], a[0:3], 0\n"
    "s_add_u32 m0, s56, 139392\n"
    "s_add_u32 s44, %6, 98304\n"
    "ds_read_b128 v[192:195], v234 offset:10496\n"
    "v_mfma_f32_16x16x32_bf16 v[80:83], v[208:211], a[16:19], v[80:83]\n"
    "ds_read_b128 v[196:199], v234 offset:12608\n"
    "buffer_load_dwordx4 v232, s[36:39], s44 offen lds\n"
    "v_mfma_f32_16x16x32_bf16 v[80:83], v[212:215], a[32:35], v[80:83]\n"
    "ds_read_b128 v[200:203], v234 offset:14720\n"
    "v_fma_f32 v128, v128, v240, v240\n"
    "v_mfma_f32_16x16x32_bf16 v[84:87], v[212:215], a[36:39], 0\n"
    "v_exp_f32 v129, v129\n"
    "v_mfma_f32_16x16x32_bf16 v[84:87], v[208:211], a[20:23], v[84:87]\n"
    "v_exp_f32 v130, v130\n"
    "v_mfma_f32_16x16x32_bf16 v[84:87], v[204:207], a[4:7], v[84:87]\n"
    "s_add_u32 m0, s56, 143616\n"
    "s_add_u32 s45, %6, 102400\n"
    "v_fma_f32 v129, v129, v240, v240\n"
    "v_mfma_f32_16x16x32_bf16 v[88:91], v[204:207], a[8:11], 0\n"
    "buffer_load_dwordx4 v232, s[36:39], s45 offen lds\n"
    "v_fma_f32 v130, v130, v240, v240\n"
    "v_mfma_f32_16x16x32_bf16 v[88:91], v[208:211], a[24:27], v[88:91]\n"
    "v_exp_f32 v131, v131\n"
    "v_mfma_f32_16x16x32_bf16 v[88:91], v[212:215], a[40:43], v[88:91]\n"
    "v_exp_f32 v132, v132\n"
    "v_mfma_f32_16x16x32_bf16 v[92:95], v[212:215], a[44:47], 0\n"
    "v_exp_f32 v133, v133\n"
    "v_mfma_f32_16x16x32_bf16 v[92:95], v[208:211], a[28:31], v[92:95]\n"
    "s_add_u32 m0, s56, 147840\n"
    "s_add_u32 s46, %6, 106496\n"
    "v_fma_f32 v131, v131, v240, v240\n"
    "v_mfma_f32_16x16x32_bf16 v[92:95], v[204:207], a[12:15], v[92:95]\n"
    "s_waitcnt lgkmcnt(0)\n"
    "buffer_load_dwordx4 v232, s[36:39], s46 offen lds\n"
    "v_fma_f32 v132, v132, v241, v241\n"
    "v_mfma_f32_16x16x32_bf16 v[96:99], v[192:195], a[0:3], 0\n"
    "ds_read_b128 v[204:207], v234 offset:16832\n"
    "v_fma_f32 v133, v133, v241, v241\n"
    "v_mfma_f32_16x16x32_bf16 v[96:99], v[196:199], a[16:19], v[96:99]\n"
    "s_add_u32 s59, %10, 7168\n"
    "ds_read_b128 v[208:211], v234 offset:18944\n"
    "v_mul_f32 v136, v136, v238\n"
    "v_mfma_f32_16x16x32_bf16 v[96:99], v[200:203], a[32:35], v[96:99]\n"
    "ds_read_b128 v[212:215], v234 offset:21056\n"
    "v_mul_f32 v137, v137, v238\n"
    "v_mfma_f32_16x16x32_bf16 v[100:103], v[200:203], a[36:39], 0\n"
    "s_add_u32 m0, s56, 0\n"
    "s_add_u32 s47, %6, 110592\n"
    "v_mul_f32 v138, v138, v238\n"
    "v_mfma_f32_16x16x32_bf16 v[100:103], v[196:199], a[20:23], v[100:103]\n"
    "buffer_load_dwordx4 v232, s[36:39], s47 offen lds\n"
    "v_mul_f32 v139, v139, v238\n"
    "v_mfma_f32_16x16x32_bf16 v[100:103], v[192:195], a[4:7], v[100:103]\n"
    "v_exp_f32 v134, v134\n"
    "v_mfma_f32_16x16x32_bf16 v[112:115], v[192:195], a[8:11], 0\n"
    "v_exp_f32 v135, v135\n"
    "v_mfma_f32_16x16x32_bf16 v[112:115], v[196:199], a[24:27], v[112:115]\n"
    "v_fma_f32 v134, v134, v241, v241\n"
    "v_fma_f32 v135, v135, v241, v241\n"
    "v_mfma_f32_16x16x32_bf16 v[112:115], v[200:203], a[40:43], v[112:115]\n"
    "s_add_u32 m0, s56, 4224\n"
    "s_add_u32 s48, %6, 114688\n"
    "v_mul_f32 v140, v140, v239\n"
    "v_mfma_f32_16x16x32_bf16 v[116:119], v[200:203], a[44:47], 0\n"
    "buffer_load_dwordx4 v232, s[36:39], s48 offen lds\n"
    "v_mul_f32 v141, v141, v239\n"
    "v_mfma_f32_16x16x32_bf16 v[116:119], v[196:199], a[28:31], v[116:119]\n"
    "v_rcp_f32 v128, v128\n"
    "v_mfma_f32_16x16x32_bf16 v[116:119], v[192:195], a[12:15], v[116:119]\n"
    "s_waitcnt lgkmcnt(0)\n"
    "v_rcp_f32 v129, v129\n"
    "v_mfma_f32_16x16x32_bf16 v[160:163], v[204:207], a[48:51], v[160:163]\n"
    "ds_read_b128 v[192:195], v234 offset:23168\n"
    "v_mul_f32 v128, v128, v144\n"
    "v_mfma_f32_16x16x32_bf16 v[160:163], v[208:211], a[64:67], v[160:163]\n"
    "s_add_u32 m0, s56, 8448\n"
    "s_add_u32 s49, %6, 118784\n"
    "ds_read_b128 v[196:199], v234 offset:25280\n"
    "v_mfma_f32_16x16x32_bf16 v[160:163], v[212:215], a[80:83], v[160:163]\n"
    "ds_read_b128 v[200:203], v234 offset:27392\n"
    "buffer_load_dwordx4 v232, s[36:39], s49 offen lds\n"
    "v_mfma_f32_16x16x32_bf16 v[164:167], v[212:215], a[84:87], v[164:167]\n"
    "v_rcp_f32 v130, v130\n"
    "v_mfma_f32_16x16x32_bf16 v[164:167], v[208:211], a[68:71], v[164:167]\n"
    "v_rcp_f32 v131, v131\n"
    "v_mfma_f32_16x16x32_bf16 v[164:167], v[204:207], a[52:55], v[164:167]\n"
    "v_rcp_f32 v132, v132\n"
    "v_mfma_f32_16x16x32_bf16 v[168:171], v[204:207], a[56:59], v[168:171]\n"
    "s_add_u32 m0, s56, 12672\n"
    "s_add_u32 s44, %6, 122880\n"
    "v_mul_f32 v129, v129, v145\n"
    "v_mfma_f32_16x16x32_bf16 v[168:171], v[208:211], a[72:75], v[168:171]\n"
    "buffer_load_dwordx4 v232, s[36:39], s44 offen lds\n"
    "v_mul_f32 v130, v130, v146\n"
    "v_mfma_f32_16x16x32_bf16 v[168:171], v[212:215], a[88:91], v[168:171]\n"
    "v_rcp_f32 v133, v133\n"
    "v_mfma_f32_16x16x32_bf16 v[172:175], v[212:215], a[92:95], v[172:175]\n"
    "v_rcp_f32 v134, v134\n"
    "v_mfma_f32_16x16x32_bf16 v[172:175], v[208:211], a[76:79], v[172:175]\n"
    "v_rcp_f32 v135, v135\n"
    "v_mfma_f32_16x16x32_bf16 v[172:175], v[204:207], a[60:63], v[172:175]\n"
    "s_waitcnt lgkmcnt(0)\n"
    "s_add_u32 m0, s56, 16896\n"
    "s_add_u32 s45, %6, 126976\n"
    "v_mfma_f32_16x16x32_bf16 v[176:179], v[192:195], a[48:51], v[176:179]\n"
    "ds_read_b128 v[204:207], v234 offset:29504\n"
    "buffer_load_dwordx4 v232, s[36:39], s45 offen lds\n"
    "v_mfma_f32_16x16x32_bf16 v[176:179], v[196:199], a[64:67], v[176:179]\n"
    "ds_read_b128 v[208:211], v234 offset:31616\n"
    "v_mul_f32 v131, v131, v147\n"
    "v_mfma_f32_16x16x32_bf16 v[176:179], v[200:203], a[80:83], v[176:179]\n"
    "ds_read_b128 v[212:215], v234 offset:33728\n"
    "v_mul_f32 v132, v132, v148\n"
    "v_mfma_f32_16x16x32_bf16 v[180:183], v[200:203], a[84:87], v[180:183]\n"
    "v_mul_f32 v133, v133, v149\n"
    "v_mul_f32 v134, v134, v150\n"
    "v_mfma_f32_16x16x32_bf16 v[180:183], v[196:199], a[68:71], v[180:183]\n"
    "s_add_u32 s46, %12, 164864\n"
    "v_mul_f32 v135, v135, v151\n"
    "v_cvt_pk_bf16_f32 v144, v128, v129\n"
    "v_mfma_f32_16x16x32_bf16 v[180:183], v[192:195], a[52:55], v[180:183]\n"
    "buffer_load_dwordx4 a[156:159], v232, s[36:39], s46 offen sc1\n"
    "v_cvt_pk_bf16_f32 v145, v130, v131\n"
    "v_mfma_f32_16x16x32_bf16 v[184:187], v[192:195], a[56:59], v[184:187]\n"
    "v_cvt_pk_bf16_f32 v146, v132, v133\n"
    "v_cvt_pk_bf16_f32 v147, v134, v135\n"
    "v_mfma_f32_16x16x32_bf16 v[184:187], v[196:199], a[72:75], v[184:187]\n"
    "v_permlane16_swap_b32 v144, v146\n"
    "v_permlane16_swap_b32 v145, v147\n"
    "v_mfma_f32_16x16x32_bf16 v[184:187], v[200:203], a[88:91], v[184:187]\n"
    "buffer_store_dwordx4 v[144:147], v251, s[36:39], s59 offen sc1\n"
    "v_mul_f32 v142, v142, v239\n"
    "v_mfma_f32_16x16x32_bf16 v[188:191], v[200:203], a[92:95], v[188:191]\n"
    "s_add_u32 m0, s56, 21120\n"
    "s_add_u32 s47, %6, 131072\n"
    "v_mul_f32 v143, v143, v239\n"
    "v_mfma_f32_16x16x32_bf16 v[188:191], v[196:199], a[76:79], v[188:191]\n"
    "s_add_u32 s57, %10, 302080\n"
    "buffer_load_dwordx4 v232, s[36:39], s47 offen lds\n"
    "v_mfma_f32_16x16x32_bf16 v[188:191], v[192:195], a[60:63], v[188:191]\n"
    "s_waitcnt lgkmcnt(0)\n"
    "v_exp_f32 v136, v136\n"
    "v_mfma_f32_16x16x32_bf16 v[32:35], v[204:207], a[48:51], v[32:35]\n"
    "ds_read_b128 v[192:195], v234 offset:35840\n"
    "v_fma_f32 v136, v136, v242, v242\n"
    "v_mfma_f32_16x16x32_bf16 v[32:35], v[208:211], a[64:67], v[32:35]\n"
    "ds_read_b128 v[196:199], v234 offset:37952\n"
    "v_mfma_f32_16x16x32_bf16 v[32:35], v[212:215], a[80:83], v[32:35]\n"
    "s_add_u32 m0, s56, 25344\n"
    "s_add_u32 s48, %6, 135168\n"
    "ds_read_b128 v[200:203], v234 offset:40064\n"
    "v_mfma_f32_16x16x32_bf16 v[36:39], v[212:215], a[84:87], v[36:39]\n"
    "buffer_load_dwordx4 v232, s[36:39], s48 offen lds\n"
    "v_mfma_f32_16x16x32_bf16 v[36:39], v[208:211], a[68:71], v[36:39]\n"
    "v_exp_f32 v137, v137\n"
    "v_mfma_f32_16x16x32_bf16 v[36:39], v[204:207], a[52:55], v[36:39]\n"
    "v_exp_f32 v138, v138\n"
    "v_mfma_f32_16x16x32_bf16 v[40:43], v[204:207], a[56:59], v[40:43]\n"
    "v_exp_f32 v139, v139\n"
    "v_mfma_f32_16x16x32_bf16 v[40:43], v[208:211], a[72:75], v[40:43]\n"
    "s_add_u32 m0, s56, 29568\n"
    "s_add_u32 s49, %6, 139264\n"
    "v_fma_f32 v137, v137, v242, v242\n"
    "v_mfma_f32_16x16x32_bf16 v[40:43], v[212:215], a[88:91], v[40:43]\n"
    "buffer_load_dwordx4 v232, s[36:39], s49 offen lds\n"
    "v_fma_f32 v138, v138, v242, v242\n"
    "v_mfma_f32_16x16x32_bf16 v[44:47], v[212:215], a[92:95], v[44:47]\n"
    "v_exp_f32 v140, v140\n"
    "v_mfma_f32_16x16x32_bf16 v[44:47], v[208:211], a[76:79], v[44:47]\n"
    "v_exp_f32 v141, v141\n"
    "v_mfma_f32_16x16x32_bf16 v[44:47], v[204:207], a[60:63], v[44:47]\n"
    "s_waitcnt lgkmcnt(0)\n"
    "v_exp_f32 v142, v142\n"
    "buffer_load_dword v254, off, s[36:39], %13 offset:256 sc1\n"
    "v_mfma_f32_16x16x32_bf16 v[48:51], v[192:195], a[48:51], v[48:51]\n"
    "s_add_u32 m0, s56, 33792\n"
    "s_add_u32 s44, %6, 143360\n"
    "ds_read_b128 v[204:207], v234 offset:42176\n"
    "v_mfma_f32_16x16x32_bf16 v[48:51], v[196:199], a[64:67], v[48:51]\n"
    "ds_read_b128 v[208:211], v234 offset:44288\n"
    "buffer_load_dwordx4 v232, s[36:39], s44 offen lds\n"
    "v_mfma_f32_16x16x32_bf16 v[48:51], v[200:203], a[80:83], v[48:51]\n"
    "ds_read_b128 v[212:215], v234 offset:46400\n"
    "v_fma_f32 v139, v139, v242, v242\n"
    "v_mfma_f32_16x16x32_bf16 v[52:55], v[200:203], a[84:87], v[52:55]\n"
    "v_exp_f32 v143, v143\n"
    "v_mfma_f32_16x16x32_bf16 v[52:55], v[196:199], a[68:71], v[52:55]\n"
    "v_fma_f32 v140, v140, v243, v243\n"
    "v_fma_f32 v141, v141, v243, v243\n"
    "v_mfma_f32_16x16x32_bf16 v[52:55], v[192:195], a[52:55], v[52:55]\n"
    "s_add_u32 m0, s56, 38016\n"
    "s_add_u32 s45, %6, 147456\n"
    "v_fma_f32 v142, v142, v243, v243\n"
    "v_mfma_f32_16x16x32_bf16 v[56:59], v[192:195], a[56:59], v[56:59]\n"
    "buffer_load_dwordx4 v232, s[36:39], s45 offen lds\n"
    "v_fma_f32 v143, v143, v243, v243\n"
    "v_mfma_f32_16x16x32_bf16 v[56:59], v[196:199], a[72:75], v[56:59]\n"
    "v_rcp_f32 v136, v136\n"
    "v_mfma_f32_16x16x32_bf16 v[56:59], v[200:203], a[88:91], v[56:59]\n"
    "v_rcp_f32 v137, v137\n"
    "v_mfma_f32_16x16x32_bf16 v[60:63], v[200:203], a[92:95], v[60:63]\n"
    "v_rcp_f32 v138, v138\n"
    "v_mfma_f32_16x16x32_bf16 v[60:63], v[196:199], a[76:79], v[60:63]\n"
    "s_add_u32 m0, s56, 42240\n"
    "s_add_u32 s46, %6, 151552\n"
    "v_mul_f32 v136, v136, v152\n"
    "v_mfma_f32_16x16x32_bf16 v[60:63], v[192:195], a[60:63], v[60:63]\n"
    "s_waitcnt lgkmcnt(0)\n"
    "buffer_load_dwordx4 v232, s[36:39], s46 offen lds\n"
    "v_mul_f32 v137, v137, v153\n"
    "v_mfma_f32_16x16x32_bf16 v[64:67], v[204:207], a[48:51], v[64:67]\n"
    "ds_read_b128 v[192:195], v234 offset:48512\n"
    "v_mul_f32 v138, v138, v154\n"
    "v_mfma_f32_16x16x32_bf16 v[64:67], v[208:211], a[64:67], v[64:67]\n"
    "ds_read_b128 v[196:199], v234 offset:50624\n"
    "v_cvt_pk_bf16_f32 v152, v136, v137\n"
    "v_mfma_f32_16x16x32_bf16 v[64:67], v[212:215], a[80:83], v[64:67]\n"
    "ds_read_b128 v[200:203], v234 offset:52736\n"
    "v_mfma_f32_16x16x32_bf16 v[68:71], v[212:215], a[84:87], v[68:71]\n"
    "s_add_u32 m0, s56, 46464\n"
    "s_add_u32 s47, %6, 155648\n"
    "v_mfma_f32_16x16x32_bf16 v[68:71], v[208:211], a[68:71], v[68:71]\n"
    "buffer_load_dwordx4 v232, s[36:39], s47 offen lds\n"
    "v_mfma_f32_16x16x32_bf16 v[68:71], v[204:207], a[52:55], v[68:71]\n"
    "v_rcp_f32 v139, v139\n"
    "v_mfma_f32_16x16x32_bf16 v[72:75], v[204:207], a[56:59], v[72:75]\n"
    "v_rcp_f32 v140, v140\n"
    "v_mfma_f32_16x16x32_bf16 v[72:75], v[208:211], a[72:75], v[72:75]\n"
    "v_rcp_f32 v141, v141\n"
    "v_mfma_f32_16x16x32_bf16 v[72:75], v[212:215], a[88:91], v[72:75]\n"
    "s_add_u32 m0, s56, 50688\n"
    "s_add_u32 s48, %6, 159744\n"
    "v_mul_f32 v139, v139, v155\n"
    "v_mfma_f32_16x16x32_bf16 v[76:79], v[212:215], a[92:95], v[76:79]\n"
    "buffer_load_dwordx4 v232, s[36:39], s48 offen lds\n"
    "v_mul_f32 v140, v140, v156\n"
    "v_mfma_f32_16x16x32_bf16 v[76:79], v[208:211], a[76:79], v[76:79]\n"
    "v_rcp_f32 v142, v142\n"
    "v_mfma_f32_16x16x32_bf16 v[76:79], v[204:207], a[60:63], v[76:79]\n"
    "s_waitcnt lgkmcnt(0)\n"
    "v_rcp_f32 v143, v143\n"
    "v_mfma_f32_16x16x32_bf16 v[80:83], v[192:195], a[48:51], v[80:83]\n"
    "ds_read_b128 v[204:207], v234 offset:54848\n"
    "v_mul_f32 v141, v141, v157\n"
    "v_mfma_f32_16x16x32_bf16 v[80:83], v[196:199], a[64:67], v[80:83]\n"
    "s_add_u32 m0, s56, 54912\n"
    "s_add_u32 s49, %6, 163840\n"
    "ds_read_b128 v[208:211], v234 offset:56960\n"
    "v_mfma_f32_16x16x32_bf16 v[80:83], v[200:203], a[80:83], v[80:83]\n"
    "ds_read_b128 v[212:215], v234 offset:59072\n"
    "buffer_load_dwordx4 v232, s[36:39], s49 offen lds\n"
    "v_mfma_f32_16x16x32_bf16 v[84:87], v[200:203], a[84:87], v[84:87]\n"
    "v_mul_f32 v142, v142, v158\n"
    "v_mul_f32 v143, v143, v159\n"
    "v_mfma_f32_16x16x32_bf16 v[84:87], v[196:199], a[68:71], v[84:87]\n"
    "v_cvt_pk_bf16_f32 v153, v138, v139\n"
    "v_cvt_pk_bf16_f32 v154, v140, v141\n"
    "v_mfma_f32_16x16x32_bf16 v[84:87], v[192:195], a[52:55], v[84:87]\n"
    "v_cvt_pk_bf16_f32 v155, v142, v143\n"
    "v_permlane16_swap_b32 v152, v154\n"
    "v_mfma_f32_16x16x32_bf16 v[88:91], v[192:195], a[56:59], v[88:91]\n"
    "s_add_u32 m0, s56, 59136\n"
    "s_add_u32 s44, %6, 167936\n"
    "v_permlane16_swap_b32 v153, v155\n"
    "v_mfma_f32_16x16x32_bf16 v[88:91], v[196:199], a[72:75], v[88:91]\n"
    "buffer_load_dwordx4 v232, s[36:39], s44 offen lds\n"
    "v_mfma_f32_16x16x32_bf16 v[88:91], v[200:203], a[88:91], v[88:91]\n"
    "buffer_store_dwordx4 v[152:155], v251, s[36:39], s57 offen sc1\n"
    "v_mfma_f32_16x16x32_bf16 v[92:95], v[200:203], a[92:95], v[92:95]\n"
    "v_mfma_f32_16x16x32_bf16 v[92:95], v[196:199], a[76:79], v[92:95]\n"
    "v_mfma_f32_16x16x32_bf16 v[92:95], v[192:195], a[60:63], v[92:95]\n"
    "s_add_u32 m0, s56, 63360\n"
    "s_add_u32 s45, %6, 172032\n"
    "buffer_load_dwordx4 v232, s[36:39], s45 offen lds\n"
    "s_waitcnt vmcnt(15)\n"
    "s_barrier\n"
    "s_cmp_lg_u32 %7, 0\n"
    "s_cbranch_scc1 .Lsig7_%=\n"
    "s_mov_b64 exec, 1\n"
    "v_mov_b32 v245, 1\n"
    "buffer_atomic_add v245, v250, s[36:39], %13 offen offset:384\n"
    "s_mov_b64 exec, -1\n"
    "s_nop 1\n"
    ".Lsig7_%=:\n"
    "s_waitcnt lgkmcnt(0)\n"
    "v_mfma_f32_16x16x32_bf16 v[96:99], v[204:207], a[48:51], v[96:99]\n"
    "s_add_u32 m0, s56, 67584\n"
    "s_add_u32 s46, %6, 176128\n"
    "ds_read_b128 v[192:195], v234 offset:61184\n"
    "v_mfma_f32_16x16x32_bf16 v[96:99], v[208:211], a[64:67], v[96:99]\n"
    "ds_read_b128 v[196:199], v234 offset:63296\n"
    "buffer_load_dwordx4 v232, s[36:39], s46 offen lds\n"
    "v_mfma_f32_16x16x32_bf16 v[96:99], v[212:215], a[80:83], v[96:99]\n"
    "ds_read_b128 v[200:203], v234 offset:65408\n"
    "v_mfma_f32_16x16x32_bf16 v[100:103], v[212:215], a[84:87], v[100:103]\n"
    "v_mfma_f32_16x16x32_bf16 v[100:103], v[208:211], a[68:71], v[100:103]\n"
    "s_add_u32 m0, s56, 71808\n"
    "s_add_u32 s47, %6, 180224\n"
    "v_mfma_f32_16x16x32_bf16 v[100:103], v[204:207], a[52:55], v[100:103]\n"
    "s_waitcnt vmcnt(10)\n"
    "buffer_load_dwordx4 v232, s[36:39], s47 offen lds\n"
    "v_mfma_f32_16x16x32_bf16 v[112:115], v[204:207], a[56:59], v[112:115]\n"
    "v_mfma_f32_16x16x32_bf16 v[112:115], v[208:211], a[72:75], v[112:115]\n"
    "v_readfirstlane_b32 s51, v254\n"
    "v_mfma_f32_16x16x32_bf16 v[112:115], v[212:215], a[88:91], v[112:115]\n"
    "v_mfma_f32_16x16x32_bf16 v[116:119], v[212:215], a[92:95], v[116:119]\n"
    "s_add_u32 m0, s56, 76032\n"
    "s_add_u32 s48, %6, 184320\n"
    "v_mfma_f32_16x16x32_bf16 v[116:119], v[208:211], a[76:79], v[116:119]\n"
    "s_cmp_lt_u32 s51, 8\n"
    "s_cbranch_scc1 .Lps8_%=\n"
    ".Lpr8_%=:\n"
    "buffer_load_dwordx4 v232, s[36:39], s48 offen lds\n"
    "v_mfma_f32_16x16x32_bf16 v[116:119], v[204:207], a[60:63], v[116:119]\n"
    "s_waitcnt lgkmcnt(0)\n"
    "v_mfma_f32_16x16x32_bf16 v[160:163], v[192:195], a[96:99], v[160:163]\n"
    "ds_read_b128 v[204:207], v235 offset:1984\n"
    "v_mfma_f32_16x16x32_bf16 v[160:163], v[196:199], a[112:115], v[160:163]\n"
    "ds_read_b128 v[208:211], v235 offset:4096\n"
    "v_mfma_f32_16x16x32_bf16 v[160:163], v[200:203], a[128:131], v[160:163]\n"
    "s_add_u32 s49, %11, 4096\n"
    "ds_read_b128 v[212:215], v235 offset:6208\n"
    "v_mfma_f32_16x16x32_bf16 v[164:167], v[200:203], a[132:135], v[164:167]\n"
    "buffer_load_dwordx4 a[160:163], v232, s[36:39], s49 offen sc1\n"
    "v_mfma_f32_16x16x32_bf16 v[164:167], v[196:199], a[116:119], v[164:167]\n"
    "v_mfma_f32_16x16x32_bf16 v[164:167], v[192:195], a[100:103], v[164:167]\n"
    "v_mfma_f32_16x16x32_bf16 v[168:171], v[192:195], a[104:107], v[168:171]\n"
    "v_mfma_f32_16x16x32_bf16 v[168:171], v[196:199], a[120:123], v[168:171]\n"
    "s_add_u32 s44, %11, 13312\n"
    "v_mfma_f32_16x16x32_bf16 v[168:171], v[200:203], a[136:139], v[168:171]\n"
    "buffer_load_dwordx4 a[176:179], v232, s[36:39], s44 offen sc1\n"
    "v_mfma_f32_16x16x32_bf16 v[172:175], v[200:203], a[140:143], v[172:175]\n"
    "v_mfma_f32_16x16x32_bf16 v[172:175], v[196:199], a[124:127], v[172:175]\n"
    "v_mfma_f32_16x16x32_bf16 v[172:175], v[192:195], a[108:111], v[172:175]\n"
    "s_waitcnt lgkmcnt(0)\n"
    "v_mfma_f32_16x16x32_bf16 v[176:179], v[204:207], a[96:99], v[176:179]\n"
    "s_add_u32 s45, %11, 151552\n"
    "ds_read_b128 v[192:195], v235 offset:8320\n"
    "v_mfma_f32_16x16x32_bf16 v[176:179], v[208:211], a[112:115], v[176:179]\n"
    "ds_read_b128 v[196:199], v235 offset:10432\n"
    "buffer_load_dwordx4 a[164:167], v232, s[36:39], s45 offen sc1\n"
    "v_mfma_f32_16x16x32_bf16 v[176:179], v[212:215], a[128:131], v[176:179]\n"
    "ds_read_b128 v[200:203], v235 offset:12544\n"
    "v_mfma_f32_16x16x32_bf16 v[180:183], v[212:215], a[132:135], v[180:183]\n"
    "v_mfma_f32_16x16x32_bf16 v[180:183], v[208:211], a[116:119], v[180:183]\n"
    "v_mfma_f32_16x16x32_bf16 v[180:183], v[204:207], a[100:103], v[180:183]\n"
    "s_add_u32 s46, %11, 160768\n"
    "v_mfma_f32_16x16x32_bf16 v[184:187], v[204:207], a[104:107], v[184:187]\n"
    "buffer_load_dwordx4 a[180:183], v232, s[36:39], s46 offen sc1\n"
    "v_mfma_f32_16x16x32_bf16 v[184:187], v[208:211], a[120:123], v[184:187]\n"
    "v_mfma_f32_16x16x32_bf16 v[184:187], v[212:215], a[136:139], v[184:187]\n"
    "v_mfma_f32_16x16x32_bf16 v[188:191], v[212:215], a[140:143], v[188:191]\n"
    "v_mfma_f32_16x16x32_bf16 v[188:191], v[208:211], a[124:127], v[188:191]\n"
    "s_add_u32 s47, %12, 4096\n"
    "v_mfma_f32_16x16x32_bf16 v[188:191], v[204:207], a[108:111], v[188:191]\n"
    "s_waitcnt lgkmcnt(0)\n"
    "buffer_load_dwordx4 a[168:171], v232, s[36:39], s47 offen sc1\n"
    "v_mfma_f32_16x16x32_bf16 v[32:35], v[192:195], a[96:99], v[32:35]\n"
    "ds_read_b128 v[204:207], v235 offset:14656\n"
    "v_mfma_f32_16x16x32_bf16 v[32:35], v[196:199], a[112:115], v[32:35]\n"
    "ds_read_b128 v[208:211], v235 offset:16768\n"
    "v_mfma_f32_16x16x32_bf16 v[32:35], v[200:203], a[128:131], v[32:35]\n"
    "ds_read_b128 v[212:215], v235 offset:18880\n"
    "v_mfma_f32_16x16x32_bf16 v[36:39], v[200:203], a[132:135], v[36:39]\n"
    "s_add_u32 s48, %12, 13312\n"
    "v_mfma_f32_16x16x32_bf16 v[36:39], v[196:199], a[116:119], v[36:39]\n"
    "buffer_load_dwordx4 a[184:187], v232, s[36:39], s48 offen sc1\n"
    "v_mfma_f32_16x16x32_bf16 v[36:39], v[192:195], a[100:103], v[36:39]\n"
    "v_mfma_f32_16x16x32_bf16 v[40:43], v[192:195], a[104:107], v[40:43]\n"
    "v_mfma_f32_16x16x32_bf16 v[40:43], v[196:199], a[120:123], v[40:43]\n"
    "v_mfma_f32_16x16x32_bf16 v[40:43], v[200:203], a[136:139], v[40:43]\n"
    "s_add_u32 s49, %12, 151552\n"
    "v_mfma_f32_16x16x32_bf16 v[44:47], v[200:203], a[140:143], v[44:47]\n"
    "buffer_load_dwordx4 a[172:175], v232, s[36:39], s49 offen sc1\n"
    "v_mfma_f32_16x16x32_bf16 v[44:47], v[196:199], a[124:127], v[44:47]\n"
    "v_mfma_f32_16x16x32_bf16 v[44:47], v[192:195], a[108:111], v[44:47]\n"
    "s_waitcnt lgkmcnt(0)\n"
    "v_mfma_f32_16x16x32_bf16 v[48:51], v[204:207], a[96:99], v[48:51]\n"
    "ds_read_b128 v[192:195], v233 offset:0\n"
    "v_mfma_f32_16x16x32_bf16 v[48:51], v[208:211], a[112:115], v[48:51]\n"
    "s_add_u32 s44, %12, 160768\n"
    "ds_read_b128 v[196:199], v233 offset:2112\n"
    "v_mfma_f32_16x16x32_bf16 v[48:51], v[212:215], a[128:131], v[48:51]\n"
    "ds_read_b128 v[200:203], v233 offset:4224\n"
    "buffer_load_dwordx4 a[188:191], v232, s[36:39], s44 offen sc1\n"
    "v_mfma_f32_16x16x32_bf16 v[52:55], v[212:215], a[132:135], v[52:55]\n"
    "v_mfma_f32_16x16x32_bf16 v[52:55], v[208:211], a[116:119], v[52:55]\n"
    "v_mfma_f32_16x16x32_bf16 v[52:55], v[204:207], a[100:103], v[52:55]\n"
    "v_mfma_f32_16x16x32_bf16 v[56:59], v[204:207], a[104:107], v[56:59]\n"
    "s_add_u32 s45, %11, 5120\n"
    "v_mfma_f32_16x16x32_bf16 v[56:59], v[208:211], a[120:123], v[56:59]\n"
    "buffer_load_dwordx4 a[192:195], v232, s[36:39], s45 offen sc1\n"
    "v_mfma_f32_16x16x32_bf16 v[56:59], v[212:215], a[136:139], v[56:59]\n"
    "v_mfma_f32_16x16x32_bf16 v[60:63], v[212:215], a[140:143], v[60:63]\n"
    "v_mfma_f32_16x16x32_bf16 v[60:63], v[208:211], a[124:127], v[60:63]\n"
    "v_mfma_f32_16x16x32_bf16 v[60:63], v[204:207], a[108:111], v[60:63]\n"
    "s_waitcnt lgkmcnt(0)\n"
    "s_add_u32 s46, %11, 14336\n"
    "buffer_load_dword v255, off, s[36:39], %13 offset:384 sc1\n"
    "v_mfma_f32_16x16x32_bf16 v[64:67], v[192:195], a[96:99], v[64:67]\n"
    "ds_read_b128 v[204:207], v233 offset:6336\n"
    "buffer_load_dwordx4 a[208:211], v232, s[36:39], s46 offen sc1\n"
    "v_mfma_f32_16x16x32_bf16 v[64:67], v[196:199], a[112:115], v[64:67]\n"
    "ds_read_b128 v[208:211], v233 offset:8448\n"
    "v_mfma_f32_16x16x32_bf16 v[64:67], v[200:203], a[128:131], v[64:67]\n"
    "ds_read_b128 v[212:215], v233 offset:10560\n"
    "v_mfma_f32_16x16x32_bf16 v[68:71], v[200:203], a[132:135], v[68:71]\n"
    "v_mfma_f32_16x16x32_bf16 v[68:71], v[196:199], a[116:119], v[68:71]\n"
    "s_add_u32 s47, %11, 152576\n"
    "v_mfma_f32_16x16x32_bf16 v[68:71], v[192:195], a[100:103], v[68:71]\n"
    "buffer_load_dwordx4 a[196:199], v232, s[36:39], s47 offen sc1\n"
    "v_mfma_f32_16x16x32_bf16 v[72:75], v[192:195], a[104:107], v[72:75]\n"
    "v_mfma_f32_16x16x32_bf16 v[72:75], v[196:199], a[120:123], v[72:75]\n"
    "v_mfma_f32_16x16x32_bf16 v[72:75], v[200:203], a[136:139], v[72:75]\n"
    "v_mfma_f32_16x16x32_bf16 v[76:79], v[200:203], a[140:143], v[76:79]\n"
    "s_add_u32 s48, %11, 161792\n"
    "v_mfma_f32_16x16x32_bf16 v[76:79], v[196:199], a[124:127], v[76:79]\n"
    "buffer_load_dwordx4 a[212:215], v232, s[36:39], s48 offen sc1\n"
    "v_mfma_f32_16x16x32_bf16 v[76:79], v[192:195], a[108:111], v[76:79]\n"
    "s_waitcnt lgkmcnt(0)\n"
    "v_mfma_f32_16x16x32_bf16 v[80:83], v[204:207], a[96:99], v[80:83]\n"
    "ds_read_b128 v[192:195], v233 offset:12672\n"
    "v_mfma_f32_16x16x32_bf16 v[80:83], v[208:211], a[112:115], v[80:83]\n"
    "ds_read_b128 v[196:199], v233 offset:14784\n"
    "v_mfma_f32_16x16x32_bf16 v[80:83], v[212:215], a[128:131], v[80:83]\n"
    "s_add_u32 s49, %12, 5120\n"
    "ds_read_b128 v[200:203], v233 offset:16896\n"
    "v_mfma_f32_16x16x32_bf16 v[84:87], v[212:215], a[132:135], v[84:87]\n"
    "buffer_load_dwordx4 a[200:203], v232, s[36:39], s49 offen sc1\n"
    "v_mfma_f32_16x16x32_bf16 v[84:87], v[208:211], a[116:119], v[84:87]\n"
    "v_mfma_f32_16x16x32_bf16 v[84:87], v[204:207], a[100:103], v[84:87]\n"
    "v_mfma_f32_16x16x32_bf16 v[88:91], v[204:207], a[104:107], v[88:91]\n"
    "v_mfma_f32_16x16x32_bf16 v[88:91], v[208:211], a[120:123], v[88:91]\n"
    "s_add_u32 s44, %12, 14336\n"
    "v_mfma_f32_16x16x32_bf16 v[88:91], v[212:215], a[136:139], v[88:91]\n"
    "buffer_load_dwordx4 a[216:219], v232, s[36:39], s44 offen sc1\n"
    "v_mfma_f32_16x16x32_bf16 v[92:95], v[212:215], a[140:143], v[92:95]\n"
    "v_mfma_f32_16x16x32_bf16 v[92:95], v[208:211], a[124:127], v[92:95]\n"
    "v_mfma_f32_16x16x32_bf16 v[92:95], v[204:207], a[108:111], v[92:95]\n"
    "s_waitcnt vmcnt(17)\n"
    "s_barrier\n"
    "s_cmp_lg_u32 %7, 0\n"
    "s_cbranch_scc1 .Lsig9_%=\n"
    "s_mov_b64 exec, 1\n"
    "v_mov_b32 v245, 1\n"
    "buffer_atomic_add v245, v250, s[36:39], %13 offen offset:512\n"
    "s_mov_b64 exec, -1\n"
    "s_nop 1\n"
    ".Lsig9_%=:\n"
    "s_waitcnt lgkmcnt(0)\n"
    "v_mfma_f32_16x16x32_bf16 v[96:99], v[192:195], a[96:99], v[96:99]\n"
    "s_add_u32 s45, %12, 152576\n"
    "ds_read_b128 v[204:207], v233 offset:19008\n"
    "v_lshrrev_b32 v248, 4, v232\n"
    "v_mfma_f32_16x16x32_bf16 v[96:99], v[196:199], a[112:115], v[96:99]\n"
    "ds_read_b128 v[208:211], v233 offset:21120\n"
    "buffer_load_dwordx4 a[204:207], v232, s[36:39], s45 offen sc1\n"
    "v_mfma_f32_16x16x32_bf16 v[96:99], v[200:203], a[128:131], v[96:99]\n"
    "ds_read_b128 v[212:215], v233 offset:23232\n"
    "v_and_b32 v249, 15, v248\n"
    "v_mfma_f32_16x16x32_bf16 v[100:103], v[200:203], a[132:135], v[100:103]\n"
    "v_mfma_f32_16x16x32_bf16 v[100:103], v[196:199], a[116:119], v[100:103]\n"
    "v_lshlrev_b32 v230, 2, v249\n"
    "v_mfma_f32_16x16x32_bf16 v[100:103], v[192:195], a[100:103], v[100:103]\n"
    "s_add_u32 s46, %12, 161792\n"
    "v_mfma_f32_16x16x32_bf16 v[112:115], v[192:195], a[104:107], v[112:115]\n"
    "s_add_u32 s57, %21, 0\n"
    "buffer_load_dwordx4 a[220:223], v232, s[36:39], s46 offen sc1\n"
    "v_mfma_f32_16x16x32_bf16 v[112:115], v[196:199], a[120:123], v[112:115]\n"
    "v_mfma_f32_16x16x32_bf16 v[112:115], v[200:203], a[136:139], v[112:115]\n"
    "buffer_load_dwordx4 v[216:219], v232, s[36:39], s57 offen\n"
    "v_mfma_f32_16x16x32_bf16 v[116:119], v[200:203], a[140:143], v[116:119]\n"
    "v_mfma_f32_16x16x32_bf16 v[116:119], v[196:199], a[124:127], v[116:119]\n"
    "s_add_u32 m0, s56, 80256\n"
    "s_add_u32 s47, %6, 188416\n"
    "s_add_u32 s58, %21, 18432\n"
    "v_mfma_f32_16x16x32_bf16 v[116:119], v[192:195], a[108:111], v[116:119]\n"
    "s_waitcnt lgkmcnt(0)\n"
    "buffer_load_dwordx4 v232, s[36:39], s47 offen lds\n"
    "v_mfma_f32_16x16x32_bf16 v[160:163], v[204:207], a[144:147], v[160:163]\n"
    "s_waitcnt vmcnt(18)\n"
    "ds_read_b128 v[192:195], v233 offset:25344\n"
    "buffer_load_dwordx4 v[220:223], v232, s[36:39], s58 offen\n"
    "v_mfma_f32_16x16x32_bf16 v[160:163], v[208:211], a[160:163], v[160:163]\n"
    "s_waitcnt vmcnt(18)\n"
    "ds_read_b128 v[196:199], v233 offset:27456\n"
    "v_mfma_f32_16x16x32_bf16 v[160:163], v[212:215], a[176:179], v[160:163]\n"
    "s_waitcnt vmcnt(16)\n"
    "ds_read_b128 v[200:203], v233 offset:29568\n"
    "v_lshrrev_b32 v231, 5, v248\n"
    "v_mfma_f32_16x16x32_bf16 v[164:167], v[212:215], a[180:183], v[164:167]\n"
    "s_add_u32 m0, s56, 84480\n"
    "s_add_u32 s48, %6, 192512\n"
    "v_mfma_f32_16x16x32_bf16 v[164:167], v[208:211], a[164:167], v[164:167]\n"
    "buffer_load_dwordx4 v232, s[36:39], s48 offen lds\n"
    "v_lshl_add_u32 v231, v231, 4, v249\n"
    "v_mfma_f32_16x16x32_bf16 v[164:167], v[204:207], a[148:151], v[164:167]\n"
    "s_waitcnt vmcnt(11)\n"
    "v_mfma_f32_16x16x32_bf16 v[168:171], v[204:207], a[152:155], v[168:171]\n"
    "v_bfe_u32 v251, v248, 4, 1\n"
    "v_mfma_f32_16x16x32_bf16 v[168:171], v[208:211], a[168:171], v[168:171]\n"
    "v_readfirstlane_b32 s51, v255\n"
    "v_mfma_f32_16x16x32_bf16 v[168:171], v[212:215], a[184:187], v[168:171]\n"
    "s_add_u32 m0, s56, 88704\n"
    "s_add_u32 s49, %6, 196608\n"
    "v_lshl_add_u32 v231, v231, 1, v251\n"
    "v_mfma_f32_16x16x32_bf16 v[172:175], v[212:215], a[188:191], v[172:175]\n"
    "buffer_load_dwordx4 v232, s[36:39], s49 offen lds\n"
    "v_mfma_f32_16x16x32_bf16 v[172:175], v[208:211], a[172:175], v[172:175]\n"
    "s_cmp_lt_u32 s51, 8\n"
    "s_cbranch_scc1 .Lps10_%=\n"
    ".Lpr10_%=:\n"
    "v_lshlrev_b32 v231, 3, v231\n"
    "v_mfma_f32_16x16x32_bf16 v[172:175], v[204:207], a[156:159], v[172:175]\n"
    "s_waitcnt lgkmcnt(0)\n"
    "v_mfma_f32_16x16x32_bf16 v[176:179], v[192:195], a[144:147], v[176:179]\n"
    "ds_read_b128 v[204:207], v233 offset:31680\n"
    "v_mfma_f32_16x16x32_bf16 v[176:179], v[196:199], a[160:163], v[176:179]\n"
    "s_add_u32 s44, %11, 6144\n"
    "s_add_u32 s59, %22, 0\n"
    "ds_read_b128 v[208:211], v233 offset:33792\n"
    "v_mfma_f32_16x16x32_bf16 v[176:179], v[200:203], a[176:179], v[176:179]\n"
    "ds_read_b128 v[212:215], v233 offset:35904\n"
    "buffer_load_dwordx4 a[224:227], v232, s[36:39], s44 offen sc1\n"
    "v_mfma_f32_16x16x32_bf16 v[180:183], v[200:203], a[180:183], v[180:183]\n"
    "buffer_load_dwordx2 v[224:225], v231, s[36:39], s59 offen\n"
    "v_mfma_f32_16x16x32_bf16 v[180:183], v[196:199], a[164:167], v[180:183]\n"
    "v_mfma_f32_16x16x32_bf16 v[180:183], v[192:195], a[148:151], v[180:183]\n"
    "s_add_u32 s57, %22, 18432\n"
    "v_mfma_f32_16x16x32_bf16 v[184:187], v[192:195], a[152:155], v[184:187]\n"
    "s_add_u32 s45, %11, 153600\n"
    "v_mfma_f32_16x16x32_bf16 v[184:187], v[196:199], a[168:171], v[184:187]\n"
    "buffer_load_dwordx4 a[228:231], v232, s[36:39], s45 offen sc1\n"
    "v_mul_u32_u24 v249, 1152, v249\n"
    "v_mfma_f32_16x16x32_bf16 v[184:187], v[200:203], a[184:187], v[184:187]\n"
    "buffer_load_dwordx2 v[226:227], v231, s[36:39], s57 offen\n"
    "v_mfma_f32_16x16x32_bf16 v[188:191], v[200:203], a[188:191], v[188:191]\n"
    "buffer_load_dword v228, v230, s[36:39], %23 offen offset:0\n"
    "v_mfma_f32_16x16x32_bf16 v[188:191], v[196:199], a[172:175], v[188:191]\n"
    "v_mfma_f32_16x16x32_bf16 v[188:191], v[192:195], a[156:159], v[188:191]\n"
    "s_waitcnt lgkmcnt(0)\n"
    "s_add_u32 s46, %12, 6144\n"
    "buffer_load_dword v229, v230, s[36:39], %23 offen offset:64\n"
    "v_mfma_f32_16x16x32_bf16 v[32:35], v[204:207], a[144:147], v[32:35]\n"
    "ds_read_b128 v[192:195], v233 offset:38016\n"
    "buffer_load_dwordx4 a[232:235], v232, s[36:39], s46 offen sc1\n"
    "v_mfma_f32_16x16x32_bf16 v[32:35], v[208:211], a[160:163], v[32:35]\n"
    "ds_read_b128 v[196:199], v233 offset:40128\n"
    "v_mfma_f32_16x16x32_bf16 v[32:35], v[212:215], a[176:179], v[32:35]\n"
    "ds_read_b128 v[200:203], v233 offset:42240\n"
    "v_mfma_f32_16x16x32_bf16 v[36:39], v[212:215], a[180:183], v[36:39]\n"
    "v_and_b32 v248, 48, v248\n"
    "v_mfma_f32_16x16x32_bf16 v[36:39], v[208:211], a[164:167], v[36:39]\n"
    "s_add_u32 s47, %12, 153600\n"
    "v_mfma_f32_16x16x32_bf16 v[36:39], v[204:207], a[148:151], v[36:39]\n"
    "buffer_load_dwordx4 a[236:239], v232, s[36:39], s47 offen sc1\n"
    "v_add_u32 v230, v248, v249\n"
    "v_mfma_f32_16x16x32_bf16 v[40:43], v[204:207], a[152:155], v[40:43]\n"
    "v_mfma_f32_16x16x32_bf16 v[40:43], v[208:211], a[168:171], v[40:43]\n"
    "v_lshrrev_b32 v248, 1, v248\n"
    "v_mfma_f32_16x16x32_bf16 v[40:43], v[212:215], a[184:187], v[40:43]\n"
    "v_mfma_f32_16x16x32_bf16 v[44:47], v[212:215], a[188:191], v[44:47]\n"
    "s_add_u32 m0, s56, 92928\n"
    "s_add_u32 s48, %6, 200704\n"
    "v_add_u32 v231, v248, v249\n"
    "v_mfma_f32_16x16x32_bf16 v[44:47], v[208:211], a[172:175], v[44:47]\n"
    "buffer_load_dwordx4 v232, s[36:39], s48 offen lds\n"
    "v_mfma_f32_16x16x32_bf16 v[44:47], v[204:207], a[156:159], v[44:47]\n"
    "s_waitcnt lgkmcnt(0)\n"
    "buffer_load_dword v252, off, s[36:39], %13 offset:512 sc1\n"
    "v_mfma_f32_16x16x32_bf16 v[48:51], v[192:195], a[144:147], v[48:51]\n"
    "ds_read_b128 v[204:207], v233 offset:44352\n"
    "v_mfma_f32_16x16x32_bf16 v[48:51], v[196:199], a[160:163], v[48:51]\n"
    "ds_read_b128 v[208:211], v233 offset:46464\n"
    "v_mfma_f32_16x16x32_bf16 v[48:51], v[200:203], a[176:179], v[48:51]\n"
    "s_add_u32 m0, s56, 97152\n"
    "s_add_u32 s49, %6, 204800\n"
    "ds_read_b128 v[212:215], v233 offset:48576\n"
    "v_mfma_f32_16x16x32_bf16 v[52:55], v[200:203], a[180:183], v[52:55]\n"
    "buffer_load_dwordx4 v232, s[36:39], s49 offen lds\n"
    "v_mfma_f32_16x16x32_bf16 v[52:55], v[196:199], a[164:167], v[52:55]\n"
    "v_mfma_f32_16x16x32_bf16 v[52:55], v[192:195], a[148:151], v[52:55]\n"
    "v_mfma_f32_16x16x32_bf16 v[56:59], v[192:195], a[152:155], v[56:59]\n"
    "v_mfma_f32_16x16x32_bf16 v[56:59], v[196:199], a[168:171], v[56:59]\n"
    "s_add_u32 m0, s56, 101376\n"
    "s_add_u32 s44, %6, 208896\n"
    "v_mfma_f32_16x16x32_bf16 v[56:59], v[200:203], a[184:187], v[56:59]\n"
    "buffer_load_dwordx4 v232, s[36:39], s44 offen lds\n"
    "v_mfma_f32_16x16x32_bf16 v[60:63], v[200:203], a[188:191], v[60:63]\n"
    "v_mfma_f32_16x16x32_bf16 v[60:63], v[196:199], a[172:175], v[60:63]\n"
    "v_mfma_f32_16x16x32_bf16 v[60:63], v[192:195], a[156:159], v[60:63]\n"
    "s_waitcnt lgkmcnt(0)\n"
    "v_mfma_f32_16x16x32_bf16 v[64:67], v[204:207], a[144:147], v[64:67]\n"
    "s_add_u32 m0, s56, 105600\n"
    "s_add_u32 s45, %6, 212992\n"
    "ds_read_b128 v[192:195], v233 offset:50688\n"
    "v_mfma_f32_16x16x32_bf16 v[64:67], v[208:211], a[160:163], v[64:67]\n"
    "ds_read_b128 v[196:199], v233 offset:52800\n"
    "buffer_load_dwordx4 v232, s[36:39], s45 offen lds\n"
    "v_mfma_f32_16x16x32_bf16 v[64:67], v[212:215], a[176:179], v[64:67]\n"
    "ds_read_b128 v[200:203], v233 offset:54912\n"
    "v_mfma_f32_16x16x32_bf16 v[68:71], v[212:215], a[180:183], v[68:71]\n"
    "v_mfma_f32_16x16x32_bf16 v[68:71], v[208:211], a[164:167], v[68:71]\n"
    "v_mfma_f32_16x16x32_bf16 v[68:71], v[204:207], a[148:151], v[68:71]\n"
    "s_add_u32 m0, s56, 109824\n"
    "s_add_u32 s46, %6, 217088\n"
    "v_mfma_f32_16x16x32_bf16 v[72:75], v[204:207], a[152:155], v[72:75]\n"
    "buffer_load_dwordx4 v232, s[36:39], s46 offen lds\n"
    "v_mfma_f32_16x16x32_bf16 v[72:75], v[208:211], a[168:171], v[72:75]\n"
    "v_mfma_f32_16x16x32_bf16 v[72:75], v[212:215], a[184:187], v[72:75]\n"
    "v_mfma_f32_16x16x32_bf16 v[76:79], v[212:215], a[188:191], v[76:79]\n"
    "v_mfma_f32_16x16x32_bf16 v[76:79], v[208:211], a[172:175], v[76:79]\n"
    "s_add_u32 s47, %11, 15360\n"
    "v_mfma_f32_16x16x32_bf16 v[76:79], v[204:207], a[156:159], v[76:79]\n"
    "s_waitcnt lgkmcnt(0)\n"
    "buffer_load_dwordx4 a[240:243], v232, s[36:39], s47 offen sc1\n"
    "v_mfma_f32_16x16x32_bf16 v[80:83], v[192:195], a[144:147], v[80:83]\n"
    "ds_read_b128 v[204:207], v233 offset:57024\n"
    "v_mfma_f32_16x16x32_bf16 v[80:83], v[196:199], a[160:163], v[80:83]\n"
    "ds_read_b128 v[208:211], v233 offset:59136\n"
    "v_mfma_f32_16x16x32_bf16 v[80:83], v[200:203], a[176:179], v[80:83]\n"
    "ds_read_b128 v[212:215], v233 offset:61248\n"
    "v_mfma_f32_16x16x32_bf16 v[84:87], v[200:203], a[180:183], v[84:87]\n"
    "s_add_u32 s48, %11, 162816\n"
    "v_mfma_f32_16x16x32_bf16 v[84:87], v[196:199], a[164:167], v[84:87]\n"
    "buffer_load_dwordx4 a[244:247], v232, s[36:39], s48 offen sc1\n"
    "v_mfma_f32_16x16x32_bf16 v[84:87], v[192:195], a[148:151], v[84:87]\n"
    "v_mfma_f32_16x16x32_bf16 v[88:91], v[192:195], a[152:155], v[88:91]\n"
    "v_mfma_f32_16x16x32_bf16 v[88:91], v[196:199], a[168:171], v[88:91]\n"
    "v_mfma_f32_16x16x32_bf16 v[88:91], v[200:203], a[184:187], v[88:91]\n"
    "s_add_u32 s49, %12, 15360\n"
    "v_mfma_f32_16x16x32_bf16 v[92:95], v[200:203], a[188:191], v[92:95]\n"
    "buffer_load_dwordx4 a[248:251], v232, s[36:39], s49 offen sc1\n"
    "v_mfma_f32_16x16x32_bf16 v[92:95], v[196:199], a[172:175], v[92:95]\n"
    "v_mfma_f32_16x16x32_bf16 v[92:95], v[192:195], a[156:159], v[92:95]\n"
    "s_waitcnt lgkmcnt(0)\n"
    "v_mfma_f32_16x16x32_bf16 v[96:99], v[204:207], a[144:147], v[96:99]\n"
    "ds_read_b128 v[192:195], v233 offset:63360\n"
    "v_mfma_f32_16x16x32_bf16 v[96:99], v[208:211], a[160:163], v[96:99]\n"
    "s_add_u32 s44, %12, 162816\n"
    "ds_read_b128 v[196:199], v233 offset:65472\n"
    "v_mfma_f32_16x16x32_bf16 v[96:99], v[212:215], a[176:179], v[96:99]\n"
    "ds_read_b128 v[200:203], v234 offset:2048\n"
    "buffer_load_dwordx4 a[252:255], v232, s[36:39], s44 offen sc1\n"
    "v_mfma_f32_16x16x32_bf16 v[100:103], v[212:215], a[180:183], v[100:103]\n"
    "v_mfma_f32_16x16x32_bf16 v[100:103], v[208:211], a[164:167], v[100:103]\n"
    "v_mfma_f32_16x16x32_bf16 v[100:103], v[204:207], a[148:151], v[100:103]\n"
    "s_waitcnt vmcnt(8)\n"
    "v_mfma_f32_16x16x32_bf16 v[112:115], v[204:207], a[152:155], v[112:115]\n"
    "s_add_u32 m0, s56, 114048\n"
    "s_add_u32 s45, %6, 221184\n"
    "v_mfma_f32_16x16x32_bf16 v[112:115], v[208:211], a[168:171], v[112:115]\n"
    "v_readfirstlane_b32 s51, v252\n"
    "buffer_load_dwordx4 v232, s[36:39], s45 offen lds\n"
    "v_mfma_f32_16x16x32_bf16 v[112:115], v[212:215], a[184:187], v[112:115]\n"
    "v_mfma_f32_16x16x32_bf16 v[116:119], v[212:215], a[188:191], v[116:119]\n"
    "v_mfma_f32_16x16x32_bf16 v[116:119], v[208:211], a[172:175], v[116:119]\n"
    "s_cmp_lt_u32 s51, 8\n"
    "s_cbranch_scc1 .Lps11_%=\n"
    ".Lpr11_%=:\n"
    "v_mfma_f32_16x16x32_bf16 v[116:119], v[204:207], a[156:159], v[116:119]\n"
    "s_add_u32 m0, s56, 118272\n"
    "s_add_u32 s46, %6, 225280\n"
    "buffer_load_dwordx4 v232, s[36:39], s46 offen lds\n"
    "s_waitcnt vmcnt(6)\n"
    "s_barrier\n"
    "buffer_load_dword v253, off, s[36:39], %14 offset:0 sc1\n"
    "s_waitcnt lgkmcnt(0)\n"
    "v_mfma_f32_16x16x32_bf16 v[160:163], v[192:195], a[192:195], v[160:163]\n"
    "s_add_u32 s47, %11, 7168\n"
    "ds_read_b128 v[204:207], v234 offset:4160\n"
    "buffer_load_dwordx4 v[0:3], v232, s[36:39], s47 offen sc1\n"
    "v_mfma_f32_16x16x32_bf16 v[160:163], v[196:199], a[208:211], v[160:163]\n"
    "ds_read_b128 v[208:211], v234 offset:6272\n"
    "v_mfma_f32_16x16x32_bf16 v[160:163], v[200:203], a[224:227], v[160:163]\n"
    "ds_read_b128 v[212:215], v234 offset:8384\n"
    "v_mfma_f32_16x16x32_bf16 v[164:167], v[200:203], a[228:231], v[164:167]\n"
    "v_mfma_f32_16x16x32_bf16 v[164:167], v[196:199], a[212:215], v[164:167]\n"
    "s_add_u32 s48, %11, 16384\n"
    "v_mfma_f32_16x16x32_bf16 v[164:167], v[192:195], a[196:199], v[164:167]\n"
    "buffer_load_dwordx4 v[16:19], v232, s[36:39], s48 offen sc1\n"
    "v_mfma_f32_16x16x32_bf16 v[168:171], v[192:195], a[200:203], v[168:171]\n"
    "v_mfma_f32_16x16x32_bf16 v[168:171], v[196:199], a[216:219], v[168:171]\n"
    "v_mfma_f32_16x16x32_bf16 v[168:171], v[200:203], a[232:235], v[168:171]\n"
    "v_mfma_f32_16x16x32_bf16 v[172:175], v[200:203], a[236:239], v[172:175]\n"
    "s_add_u32 s49, %11, 154624\n"
    "v_mfma_f32_16x16x32_bf16 v[172:175], v[196:199], a[220:223], v[172:175]\n"
    "buffer_load_dwordx4 v[4:7], v232, s[36:39], s49 offen sc1\n"
    "v_mfma_f32_16x16x32_bf16 v[172:175], v[192:195], a[204:207], v[172:175]\n"
    "s_waitcnt lgkmcnt(0)\n"
    "v_mfma_f32_16x16x32_bf16 v[176:179], v[204:207], a[192:195], v[176:179]\n"
    "ds_read_b128 v[192:195], v234 offset:10496\n"
    "v_mfma_f32_16x16x32_bf16 v[176:179], v[208:211], a[208:211], v[176:179]\n"
    "ds_read_b128 v[196:199], v234 offset:12608\n"
    "v_lshlrev_b32 v128, 16, v216\n"
    "v_mfma_f32_16x16x32_bf16 v[176:179], v[212:215], a[224:227], v[176:179]\n"
    "s_add_u32 s44, %11, 163840\n"
    "ds_read_b128 v[200:203], v234 offset:14720\n"
    "v_and_b32 v129, 0xffff0000, v216\n"
    "v_mfma_f32_16x16x32_bf16 v[180:183], v[212:215], a[228:231], v[180:183]\n"
    "buffer_load_dwordx4 v[20:23], v232, s[36:39], s44 offen sc1\n"
    "v_add_f32 v128, v128, v228\n"
    "v_mfma_f32_16x16x32_bf16 v[180:183], v[208:211], a[212:215], v[180:183]\n"
    "v_add_f32 v129, v129, v228\n"
    "v_mfma_f32_16x16x32_bf16 v[180:183], v[204:207], a[196:199], v[180:183]\n"
    "v_lshlrev_b32 v130, 16, v217\n"
    "v_mfma_f32_16x16x32_bf16 v[184:187], v[204:207], a[200:203], v[184:187]\n"
    "v_mfma_f32_16x16x32_bf16 v[184:187], v[208:211], a[216:219], v[184:187]\n"
    "s_add_u32 s45, %12, 7168\n"
    "v_and_b32 v131, 0xffff0000, v217\n"
    "v_mfma_f32_16x16x32_bf16 v[184:187], v[212:215], a[232:235], v[184:187]\n"
    "buffer_load_dwordx4 v[8:11], v232, s[36:39], s45 offen sc1\n"
    "v_add_f32 v130, v130, v228\n"
    "v_mfma_f32_16x16x32_bf16 v[188:191], v[212:215], a[236:239], v[188:191]\n"
    "v_add_f32 v131, v131, v228\n"
    "v_mfma_f32_16x16x32_bf16 v[188:191], v[208:211], a[220:223], v[188:191]\n"
    "v_lshlrev_b32 v144, 16, v218\n"
    "v_mfma_f32_16x16x32_bf16 v[188:191], v[204:207], a[204:207], v[188:191]\n"
    "s_waitcnt lgkmcnt(0)\n"
    "v_and_b32 v145, 0xffff0000, v218\n"
    "v_mfma_f32_16x16x32_bf16 v[32:35], v[192:195], a[192:195], v[32:35]\n"
    "s_add_u32 s46, %12, 16384\n"
    "ds_read_b128 v[204:207], v234 offset:16832\n"
    "v_add_f32 v144, v144, v228\n"
    "v_mfma_f32_16x16x32_bf16 v[32:35], v[196:199], a[208:211], v[32:35]\n"
    "ds_read_b128 v[208:211], v234 offset:18944\n"
    "buffer_load_dwordx4 v[24:27], v232, s[36:39], s46 offen sc1\n"
    "v_mfma_f32_16x16x32_bf16 v[32:35], v[200:203], a[224:227], v[32:35]\n"
    "ds_read_b128 v[212:215], v234 offset:21056\n"
    "v_add_f32 v145, v145, v228\n"
    "v_mfma_f32_16x16x32_bf16 v[36:39], v[200:203], a[228:231], v[36:39]\n"
    "v_lshlrev_b32 v146, 16, v219\n"
    "v_mfma_f32_16x16x32_bf16 v[36:39], v[196:199], a[212:215], v[36:39]\n"
    "v_and_b32 v147, 0xffff0000, v219\n"
    "v_mfma_f32_16x16x32_bf16 v[36:39], v[192:195], a[196:199], v[36:39]\n"
    "s_add_u32 s47, %12, 154624\n"
    "v_add_f32 v146, v146, v228\n"
    "v_mfma_f32_16x16x32_bf16 v[40:43], v[192:195], a[200:203], v[40:43]\n"
    "buffer_load_dwordx4 v[12:15], v232, s[36:39], s47 offen sc1\n"
    "v_add_f32 v147, v147, v228\n"
    "v_mfma_f32_16x16x32_bf16 v[40:43], v[196:199], a[216:219], v[40:43]\n"
    "v_mfma_f32_16x16x32_bf16 v[40:43], v[200:203], a[232:235], v[40:43]\n"
    "v_lshlrev_b32 v104, 16, v224\n"
    "v_mfma_f32_16x16x32_bf16 v[44:47], v[200:203], a[236:239], v[44:47]\n"
    "v_and_b32 v105, 0xffff0000, v224\n"
    "v_mfma_f32_16x16x32_bf16 v[44:47], v[196:199], a[220:223], v[44:47]\n"
    "s_add_u32 s48, %12, 163840\n"
    "v_add_f32 v104, v104, v228\n"
    "v_mfma_f32_16x16x32_bf16 v[44:47], v[192:195], a[204:207], v[44:47]\n"
    "s_waitcnt lgkmcnt(0)\n"
    "buffer_load_dwordx4 v[28:31], v232, s[36:39], s48 offen sc1\n"
    "v_add_f32 v105, v105, v228\n"
    "v_mfma_f32_16x16x32_bf16 v[48:51], v[204:207], a[192:195], v[48:51]\n"
    "ds_read_b128 v[192:195], v234 offset:23168\n"
    "v_lshlrev_b32 v106, 16, v225\n"
    "v_mfma_f32_16x16x32_bf16 v[48:51], v[208:211], a[208:211], v[48:51]\n"
    "ds_read_b128 v[196:199], v234 offset:25280\n"
    "v_and_b32 v107, 0xffff0000, v225\n"
    "v_mfma_f32_16x16x32_bf16 v[48:51], v[212:215], a[224:227], v[48:51]\n"
    "ds_read_b128 v[200:203], v234 offset:27392\n"
    "v_mfma_f32_16x16x32_bf16 v[52:55], v[212:215], a[228:231], v[52:55]\n"
    "s_add_u32 m0, s56, 122496\n"
    "s_add_u32 s49, %6, 229376\n"
    "v_add_f32 v106, v106, v228\n"
    "v_mfma_f32_16x16x32_bf16 v[52:55], v[208:211], a[212:215], v[52:55]\n"
    "buffer_load_dwordx4 v232, s[36:39], s49 offen lds\n"
    "v_add_f32 v107, v107, v228\n"
    "v_mfma_f32_16x16x32_bf16 v[52:55], v[204:207], a[196:199], v[52:55]\n"
    "v_lshlrev_b32 v132, 16, v220\n"
    "v_mfma_f32_16x16x32_bf16 v[56:59], v[204:207], a[200:203], v[56:59]\n"
    "v_and_b32 v133, 0xffff0000, v220\n"
    "v_mfma_f32_16x16x32_bf16 v[56:59], v[208:211], a[216:219], v[56:59]\n"
    "v_add_f32 v132, v132, v229\n"
    "v_mfma_f32_16x16x32_bf16 v[56:59], v[212:215], a[232:235], v[56:59]\n"
    "s_add_u32 m0, s56, 126720\n"
    "s_add_u32 s44, %6, 233472\n"
    "v_add_f32 v133, v133, v229\n"
    "v_mfma_f32_16x16x32_bf16 v[60:63], v[212:215], a[236:239], v[60:63]\n"
    "buffer_load_dwordx4 v232, s[36:39], s44 offen lds\n"
    "v_mfma_f32_16x16x32_bf16 v[60:63], v[208:211], a[220:223], v[60:63]\n"
    "v_lshlrev_b32 v134, 16, v221\n"
    "v_mfma_f32_16x16x32_bf16 v[60:63], v[204:207], a[204:207], v[60:63]\n"
    "s_waitcnt lgkmcnt(0)\n"
    "v_and_b32 v135, 0xffff0000, v221\n"
    "v_mfma_f32_16x16x32_bf16 v[64:67], v[192:195], a[192:195], v[64:67]\n"
    "ds_read_b128 v[204:207], v234 offset:29504\n"
    "v_add_f32 v134, v134, v229\n"
    "v_mfma_f32_16x16x32_bf16 v[64:67], v[196:199], a[208:211], v[64:67]\n"
    "s_add_u32 m0, s56, 130944\n"
    "s_add_u32 s45, %6, 237568\n"
    "ds_read_b128 v[208:211], v234 offset:31616\n"
    "v_mfma_f32_16x16x32_bf16 v[64:67], v[200:203], a[224:227], v[64:67]\n"
    "ds_read_b128 v[212:215], v234 offset:33728\n"
    "buffer_load_dwordx4 v232, s[36:39], s45 offen lds\n"
    "v_mfma_f32_16x16x32_bf16 v[68:71], v[200:203], a[228:231], v[68:71]\n"
    "v_add_f32 v135, v135, v229\n"
    "v_lshlrev_b32 v148, 16, v222\n"
    "v_mfma_f32_16x16x32_bf16 v[68:71], v[196:199], a[212:215], v[68:71]\n"
    "v_and_b32 v149, 0xffff0000, v222\n"
    "v_mfma_f32_16x16x32_bf16 v[68:71], v[192:195], a[196:199], v[68:71]\n"
    "s_waitcnt vmcnt(11)\n"
    "v_add_f32 v148, v148, v229\n"
    "v_mfma_f32_16x16x32_bf16 v[72:75], v[192:195], a[200:203], v[72:75]\n"
    "s_add_u32 m0, s56, 135168\n"
    "s_add_u32 s46, %6, 241664\n"
    "v_add_f32 v149, v149, v229\n"
    "v_mfma_f32_16x16x32_bf16 v[72:75], v[196:199], a[216:219], v[72:75]\n"
    "v_readfirstlane_b32 s51, v253\n"
    "buffer_load_dwordx4 v232, s[36:39], s46 offen lds\n"
    "v_mfma_f32_16x16x32_bf16 v[72:75], v[200:203], a[232:235], v[72:75]\n"
    "v_lshlrev_b32 v150, 16, v223\n"
    "v_and_b32 v151, 0xffff0000, v223\n"
    "v_mfma_f32_16x16x32_bf16 v[76:79], v[200:203], a[236:239], v[76:79]\n"
    "v_add_f32 v150, v150, v229\n"
    "v_mfma_f32_16x16x32_bf16 v[76:79], v[196:199], a[220:223], v[76:79]\n"
    "s_cmp_lt_u32 s51, 4\n"
    "s_cbranch_scc1 .Lps12_%=\n"
    ".Lpr12_%=:\n"
    "v_mfma_f32_16x16x32_bf16 v[76:79], v[192:195], a[204:207], v[76:79]\n"
    "s_waitcnt lgkmcnt(0)\n"
    "s_add_u32 m0, s56, 139392\n"
    "s_add_u32 s47, %6, 245760\n"
    "v_mfma_f32_16x16x32_bf16 v[80:83], v[204:207], a[192:195], v[80:83]\n"
    "ds_read_b128 v[192:195], v234 offset:35840\n"
    "buffer_load_dwordx4 v232, s[36:39], s47 offen lds\n"
    "v_mfma_f32_16x16x32_bf16 v[80:83], v[208:211], a[208:211], v[80:83]\n"
    "ds_read_b128 v[196:199], v234 offset:37952\n"
    "v_add_f32 v151, v151, v229\n"
    "v_mfma_f32_16x16x32_bf16 v[80:83], v[212:215], a[224:227], v[80:83]\n"
    "ds_read_b128 v[200:203], v234 offset:40064\n"
    "v_lshlrev_b32 v108, 16, v226\n"
    "v_mfma_f32_16x16x32_bf16 v[84:87], v[212:215], a[228:231], v[84:87]\n"
    "v_and_b32 v109, 0xffff0000, v226\n"
    "v_add_f32 v108, v108, v229\n"
    "v_mfma_f32_16x16x32_bf16 v[84:87], v[208:211], a[212:215], v[84:87]\n"
    "s_add_u32 m0, s56, 143616\n"
    "s_add_u32 s48, %6, 249856\n"
    "v_add_f32 v109, v109, v229\n"
    "v_mfma_f32_16x16x32_bf16 v[84:87], v[204:207], a[196:199], v[84:87]\n"
    "buffer_load_dwordx4 v232, s[36:39], s48 offen lds\n"
    "v_lshlrev_b32 v110, 16, v227\n"
    "v_mfma_f32_16x16x32_bf16 v[88:91], v[204:207], a[200:203], v[88:91]\n"
    "v_and_b32 v111, 0xffff0000, v227\n"
    "v_mfma_f32_16x16x32_bf16 v[88:91], v[208:211], a[216:219], v[88:91]\n"
    "v_add_f32 v110, v110, v229\n"
    "v_mfma_f32_16x16x32_bf16 v[88:91], v[212:215], a[232:235], v[88:91]\n"
    "v_add_f32 v111, v111, v229\n"
    "v_mfma_f32_16x16x32_bf16 v[92:95], v[212:215], a[236:239], v[92:95]\n"
    "s_add_u32 m0, s56, 147840\n"
    "s_add_u32 s49, %6, 253952\n"
    "v_mov_b32 v216, 0x3f80\n"
    "v_mfma_f32_16x16x32_bf16 v[92:95], v[208:211], a[220:223], v[92:95]\n"
    "buffer_load_dwordx4 v232, s[36:39], s49 offen lds\n"
    "v_mov_b32 v217, 0x3f800000\n"
    "v_mfma_f32_16x16x32_bf16 v[92:95], v[204:207], a[204:207], v[92:95]\n"
    "s_waitcnt lgkmcnt(0)\n"
    "v_mfma_f32_16x16x32_bf16 v[96:99], v[192:195], a[192:195], v[96:99]\n"
    "ds_read_b128 v[204:207], v234 offset:42176\n"
    "v_mfma_f32_16x16x32_bf16 v[96:99], v[196:199], a[208:211], v[96:99]\n"
    "ds_read_b128 v[208:211], v234 offset:44288\n"
    "v_mfma_f32_16x16x32_bf16 v[96:99], v[200:203], a[224:227], v[96:99]\n"
    "s_add_u32 m0, s56, 0\n"
    "s_add_u32 s44, %6, 258048\n"
    "ds_read_b128 v[212:215], v234 offset:46400\n"
    "v_mfma_f32_16x16x32_bf16 v[100:103], v[200:203], a[228:231], v[100:103]\n"
    "buffer_load_dwordx4 v232, s[36:39], s44 offen lds\n"
    "v_mfma_f32_16x16x32_bf16 v[100:103], v[196:199], a[212:215], v[100:103]\n"
    "v_mfma_f32_16x16x32_bf16 v[100:103], v[192:195], a[196:199], v[100:103]\n"
    "v_mfma_f32_16x16x32_bf16 v[112:115], v[192:195], a[200:203], v[112:115]\n"
    "v_mfma_f32_16x16x32_bf16 v[112:115], v[196:199], a[216:219], v[112:115]\n"
    "s_add_u32 m0, s56, 4224\n"
    "s_add_u32 s45, %6, 262144\n"
    "v_mfma_f32_16x16x32_bf16 v[112:115], v[200:203], a[232:235], v[112:115]\n"
    "buffer_load_dwordx4 v232, s[36:39], s45 offen lds\n"
    "v_mfma_f32_16x16x32_bf16 v[116:119], v[200:203], a[236:239], v[116:119]\n"
    "v_mfma_f32_16x16x32_bf16 v[116:119], v[196:199], a[220:223], v[116:119]\n"
    "v_mfma_f32_16x16x32_bf16 v[116:119], v[192:195], a[204:207], v[116:119]\n"
    "s_waitcnt vmcnt(6)\n"
    "s_barrier\n"
    "s_waitcnt lgkmcnt(0)\n"
    "v_mfma_f32_16x16x32_bf16 v[160:163], v[204:207], a[240:243], v[160:163]\n"
    "s_add_u32 m0, s56, 8448\n"
    "s_add_u32 s46, %6, 266240\n"
    "ds_read_b128 v[192:195], v234 offset:48512\n"
    "v_mfma_f32_16x16x32_bf16 v[160:163], v[208:211], v[0:3], v[160:163]\n"
    "ds_read_b128 v[196:199], v234 offset:50624\n"
    "buffer_load_dwordx4 v232, s[36:39], s46 offen lds\n"
    "v_mfma_f32_16x16x32_bf16 v[160:163], v[212:215], v[16:19], v[160:163]\n"
    "ds_read_b128 v[200:203], v234 offset:52736\n"
    "v_mfma_f32_16x16x32_bf16 v[164:167], v[212:215], v[20:23], v[164:167]\n"
    "v_mfma_f32_16x16x32_bf16 v[164:167], v[208:211], v[4:7], v[164:167]\n"
    "v_mfma_f32_16x16x32_bf16 v[164:167], v[204:207], a[244:247], v[164:167]\n"
    "s_add_u32 m0, s56, 12672\n"
    "s_add_u32 s47, %6, 270336\n"
    "v_mfma_f32_16x16x32_bf16 v[168:171], v[204:207], a[248:251], v[168:171]\n"
    "buffer_load_dwordx4 v232, s[36:39], s47 offen lds\n"
    "v_mfma_f32_16x16x32_bf16 v[168:171], v[208:211], v[8:11], v[168:171]\n"
    "v_mfma_f32_16x16x32_bf16 v[168:171], v[212:215], v[24:27], v[168:171]\n"
    "v_mfma_f32_16x16x32_bf16 v[172:175], v[212:215], v[28:31], v[172:175]\n"
    "v_mfma_f32_16x16x32_bf16 v[172:175], v[208:211], v[12:15], v[172:175]\n"
    "s_add_u32 m0, s56, 16896\n"
    "s_add_u32 s48, %6, 274432\n"
    "v_mfma_f32_16x16x32_bf16 v[172:175], v[204:207], a[252:255], v[172:175]\n"
    "s_waitcnt lgkmcnt(0)\n"
    "buffer_load_dwordx4 v232, s[36:39], s48 offen lds\n"
    "v_mfma_f32_16x16x32_bf16 v[176:179], v[192:195], a[240:243], v[176:179]\n"
    "ds_read_b128 v[204:207], v234 offset:54848\n"
    "v_mfma_f32_16x16x32_bf16 v[176:179], v[196:199], v[0:3], v[176:179]\n"
    "ds_read_b128 v[208:211], v234 offset:56960\n"
    "v_mfma_f32_16x16x32_bf16 v[176:179], v[200:203], v[16:19], v[176:179]\n"
    "ds_read_b128 v[212:215], v234 offset:59072\n"
    "v_mfma_f32_16x16x32_bf16 v[180:183], v[200:203], v[20:23], v[180:183]\n"
    "s_add_u32 m0, s56, 21120\n"
    "s_add_u32 s49, %6, 278528\n"
    "ds_write_b128 v246, v[168:171] offset:0\n"
    "v_mfma_f32_16x16x32_bf16 v[180:183], v[196:199], v[4:7], v[180:183]\n"
    "buffer_load_dwordx4 v232, s[36:39], s49 offen lds\n"
    "v_mfma_f32_16x16x32_bf16 v[180:183], v[192:195], a[244:247], v[180:183]\n"
    "v_mfma_f32_16x16x32_bf16 v[184:187], v[192:195], a[248:251], v[184:187]\n"
    "ds_write_b128 v246, v[172:175] offset:1024\n"
    "v_mfma_f32_16x16x32_bf16 v[184:187], v[196:199], v[8:11], v[184:187]\n"
    "v_mfma_f32_16x16x32_bf16 v[184:187], v[200:203], v[24:27], v[184:187]\n"
    "s_add_u32 m0, s56, 25344\n"
    "s_add_u32 s44, %6, 282624\n"
    "v_mfma_f32_16x16x32_bf16 v[188:191], v[200:203], v[28:31], v[188:191]\n"
    "buffer_load_dwordx4 v232, s[36:39], s44 offen lds\n"
    "v_mfma_f32_16x16x32_bf16 v[188:191], v[196:199], v[12:15], v[188:191]\n"
    "v_mfma_f32_16x16x32_bf16 v[188:191], v[192:195], a[252:255], v[188:191]\n"
    "s_waitcnt lgkmcnt(2)\n"
    "v_mfma_f32_16x16x32_bf16 v[32:35], v[204:207], a[240:243], v[32:35]\n"
    "ds_read_b128 v[192:195], v234 offset:61184\n"
    "v_mfma_f32_16x16x32_bf16 v[32:35], v[208:211], v[0:3], v[32:35]\n"
    "s_add_u32 m0, s56, 29568\n"
    "s_add_u32 s45, %6, 286720\n"
    "ds_read_b128 v[196:199], v234 offset:63296\n"
    "v_mfma_f32_16x16x32_bf16 v[32:35], v[212:215], v[16:19], v[32:35]\n"
    "ds_read_b128 v[200:203], v234 offset:65408\n"
    "buffer_load_dwordx4 v232, s[36:39], s45 offen lds\n"
    "v_mfma_f32_16x16x32_bf16 v[36:39], v[212:215], v[20:23], v[36:39]\n"
    "ds_write_b128 v246, v[184:187] offset:8192\n"
    "v_mfma_f32_16x16x32_bf16 v[36:39], v[208:211], v[4:7], v[36:39]\n"
    "v_mfma_f32_16x16x32_bf16 v[36:39], v[204:207], a[244:247], v[36:39]\n"
    "v_mfma_f32_16x16x32_bf16 v[40:43], v[204:207], a[248:251], v[40:43]\n"
    "s_add_u32 m0, s56, 33792\n"
    "s_add_u32 s46, %6, 290816\n"
    "ds_write_b128 v246, v[188:191] offset:9216\n"
    "v_mfma_f32_16x16x32_bf16 v[40:43], v[208:211], v[8:11], v[40:43]\n"
    "buffer_load_dwordx4 v232, s[36:39], s46 offen lds\n"
    "v_mfma_f32_16x16x32_bf16 v[40:43], v[212:215], v[24:27], v[40:43]\n"
    "v_mfma_f32_16x16x32_bf16 v[44:47], v[212:215], v[28:31], v[44:47]\n"
    "v_mfma_f32_16x16x32_bf16 v[44:47], v[208:211], v[12:15], v[44:47]\n"
    "v_mfma_f32_16x16x32_bf16 v[44:47], v[204:207], a[252:255], v[44:47]\n"
    "s_add_u32 m0, s56, 38016\n"
    "s_add_u32 s47, %6, 294912\n"
    "buffer_load_dwordx4 v232, s[36:39], s47 offen lds\n"
    "s_waitcnt vmcnt(8) lgkmcnt(0)\n"
    "s_barrier\n"
    "v_mfma_f32_16x16x32_bf16 v[48:51], v[192:195], a[240:243], v[48:51]\n"
    "s_add_u32 m0, s56, 42240\n"
    "s_add_u32 s48, %6, 299008\n"
    "ds_read_b128 v[204:207], v235 offset:1984\n"
    "v_mfma_f32_16x16x32_bf16 v[48:51], v[196:199], v[0:3], v[48:51]\n"
    "ds_read_b128 v[208:211], v235 offset:4096\n"
    "buffer_load_dwordx4 v232, s[36:39], s48 offen lds\n"
    "v_mfma_f32_16x16x32_bf16 v[48:51], v[200:203], v[16:19], v[48:51]\n"
    "ds_read_b128 v[212:215], v235 offset:6208\n"
    "v_mfma_f32_16x16x32_bf16 v[52:55], v[200:203], v[20:23], v[52:55]\n"
    "ds_write_b128 v246, v[40:43] offset:16384\n"
    "ds_read_b128 v[168:171], v247 offset:0\n"
    "v_mfma_f32_16x16x32_bf16 v[52:55], v[196:199], v[4:7], v[52:55]\n"
    "s_add_u32 m0, s56, 46464\n"
    "s_add_u32 s49, %6, 303104\n"
    "ds_read_b128 v[184:187], v247 offset:8192\n"
    "v_mfma_f32_16x16x32_bf16 v[52:55], v[192:195], a[244:247], v[52:55]\n"
    "buffer_load_dwordx4 v232, s[36:39], s49 offen lds\n"
    "v_mfma_f32_16x16x32_bf16 v[56:59], v[192:195], a[248:251], v[56:59]\n"
    "ds_write_b128 v246, v[44:47] offset:17408\n"
    "ds_read_b128 v[172:175], v247 offset:1024\n"
    "v_mfma_f32_16x16x32_bf16 v[56:59], v[196:199], v[8:11], v[56:59]\n"
    "ds_read_b128 v[188:191], v247 offset:9216\n"
    "v_mfma_f32_16x16x32_bf16 v[56:59], v[200:203], v[24:27], v[56:59]\n"
    "v_mfma_f32_16x16x32_bf16 v[60:63], v[200:203], v[28:31], v[60:63]\n"
    "s_add_u32 m0, s56, 50688\n"
    "s_add_u32 s44, %6, 307200\n"
    "v_mfma_f32_16x16x32_bf16 v[60:63], v[196:199], v[12:15], v[60:63]\n"
    "buffer_load_dwordx4 v232, s[36:39], s44 offen lds\n"
    "v_mfma_f32_16x16x32_bf16 v[60:63], v[192:195], a[252:255], v[60:63]\n"
    "s_waitcnt lgkmcnt(6)\n"
    "v_mfma_f32_16x16x32_bf16 v[64:67], v[204:207], a[240:243], v[64:67]\n"
    "ds_read_b128 v[192:195], v235 offset:8320\n"
    "v_mfma_f32_16x16x32_bf16 v[64:67], v[208:211], v[0:3], v[64:67]\n"
    "ds_read_b128 v[196:199], v235 offset:10432\n"
    "v_mfma_f32_16x16x32_bf16 v[64:67], v[212:215], v[16:19], v[64:67]\n"
    "s_add_u32 m0, s56, 54912\n"
    "s_add_u32 s45, %6, 311296\n"
    "ds_read_b128 v[200:203], v235 offset:12544\n"
    "v_mfma_f32_16x16x32_bf16 v[68:71], v[212:215], v[20:23], v[68:71]\n"
    "s_waitcnt lgkmcnt(7)\n"
    "buffer_load_dwordx4 v232, s[36:39], s45 offen lds\n"
    "v_add_f32 v160, v160, v168\n"
    "v_mfma_f32_16x16x32_bf16 v[68:71], v[208:211], v[4:7], v[68:71]\n"
    "ds_write_b128 v246, v[56:59] offset:24576\n"
    "v_add_f32 v161, v161, v169\n"
    "v_mfma_f32_16x16x32_bf16 v[68:71], v[204:207], a[244:247], v[68:71]\n"
    "v_add_f32 v162, v162, v170\n"
    "v_add_f32 v163, v163, v171\n"
    "v_mfma_f32_16x16x32_bf16 v[72:75], v[204:207], a[248:251], v[72:75]\n"
    "ds_write_b128 v246, v[60:63] offset:25600\n"
    "v_mfma_f32_16x16x32_bf16 v[72:75], v[208:211], v[8:11], v[72:75]\n"
    "s_add_u32 m0, s56, 59136\n"
    "s_add_u32 s46, %6, 315392\n"
    "s_waitcnt lgkmcnt(8)\n"
    "v_mfma_f32_16x16x32_bf16 v[72:75], v[212:215], v[24:27], v[72:75]\n"
    "buffer_load_dwordx4 v232, s[36:39], s46 offen lds\n"
    "v_add_f32 v176, v176, v184\n"
    "v_mfma_f32_16x16x32_bf16 v[76:79], v[212:215], v[28:31], v[76:79]\n"
    "v_add_f32 v177, v177, v185\n"
    "v_add_f32 v178, v178, v186\n"
    "v_mfma_f32_16x16x32_bf16 v[76:79], v[208:211], v[12:15], v[76:79]\n"
    "v_add_f32 v179, v179, v187\n"
    "v_mfma_f32_16x16x32_bf16 v[76:79], v[204:207], a[252:255], v[76:79]\n"
    "s_waitcnt lgkmcnt(2)\n"
    "v_cvt_pk_bf16_f32 v168, v160, v161\n"
    "v_mfma_f32_16x16x32_bf16 v[80:83], v[192:195], a[240:243], v[80:83]\n"
    "s_add_u32 m0, s56, 63360\n"
    "s_add_u32 s47, %6, 319488\n"
    "ds_read_b128 v[204:207], v235 offset:14656\n"
    "v_mfma_f32_16x16x32_bf16 v[80:83], v[196:199], v[0:3], v[80:83]\n"
    "ds_read_b128 v[208:211], v235 offset:16768\n"
    "buffer_load_dwordx4 v232, s[36:39], s47 offen lds\n"
    "v_mfma_f32_16x16x32_bf16 v[80:83], v[200:203], v[16:19], v[80:83]\n"
    "ds_read_b128 v[212:215], v235 offset:18880\n"
    "v_cvt_pk_bf16_f32 v169, v162, v163\n"
    "v_mfma_f32_16x16x32_bf16 v[84:87], v[200:203], v[20:23], v[84:87]\n"
    "v_cvt_pk_bf16_f32 v170, v176, v177\n"
    "v_cvt_pk_bf16_f32 v171, v178, v179\n"
    "v_mfma_f32_16x16x32_bf16 v[84:87], v[196:199], v[4:7], v[84:87]\n"
    "buffer_store_dwordx4 v[168:171], v232, s[36:39], %15 offen offset:0 sc1\n"
    "ds_write_b128 v246, v[72:75] offset:32768\n"
    "v_mfma_f32_16x16x32_bf16 v[84:87], v[192:195], a[244:247], v[84:87]\n"
    "s_add_u32 m0, s56, 67584\n"
    "s_add_u32 s48, %6, 323584\n"
    "v_mfma_f32_16x16x32_bf16 v[88:91], v[192:195], a[248:251], v[88:91]\n"
    "buffer_load_dwordx4 v232, s[36:39], s48 offen lds\n"
    "v_add_f32 v164, v164, v172\n"
    "v_mfma_f32_16x16x32_bf16 v[88:91], v[196:199], v[8:11], v[88:91]\n"
    "v_add_f32 v165, v165, v173\n"
    "v_add_f32 v166, v166, v174\n"
    "v_mfma_f32_16x16x32_bf16 v[88:91], v[200:203], v[24:27], v[88:91]\n"
    "ds_write_b128 v246, v[76:79] offset:33792\n"
    "v_add_f32 v167, v167, v175\n"
    "v_mfma_f32_16x16x32_bf16 v[92:95], v[200:203], v[28:31], v[92:95]\n"
    "v_add_f32 v180, v180, v188\n"
    "v_mfma_f32_16x16x32_bf16 v[92:95], v[196:199], v[12:15], v[92:95]\n"
    "s_add_u32 m0, s56, 71808\n"
    "s_add_u32 s49, %6, 327680\n"
    "v_add_f32 v181, v181, v189\n"
    "v_mfma_f32_16x16x32_bf16 v[92:95], v[192:195], a[252:255], v[92:95]\n"
    "s_waitcnt lgkmcnt(2)\n"
    "buffer_load_dwordx4 v232, s[36:39], s49 offen lds\n"
    "v_add_f32 v182, v182, v190\n"
    "v_mfma_f32_16x16x32_bf16 v[96:99], v[204:207], a[240:243], v[96:99]\n"
    "ds_read_b128 v[192:195], v233 offset:0\n"
    "v_add_f32 v183, v183, v191\n"
    "v_mfma_f32_16x16x32_bf16 v[96:99], v[208:211], v[0:3], v[96:99]\n"
    "ds_read_b128 v[196:199], v233 offset:2112\n"
    "v_cvt_pk_bf16_f32 v172, v164, v165\n"
    "v_mfma_f32_16x16x32_bf16 v[96:99], v[212:215], v[16:19], v[96:99]\n"
    "ds_read_b128 v[200:203], v233 offset:4224\n"
    "v_cvt_pk_bf16_f32 v173, v166, v167\n"
    "v_mfma_f32_16x16x32_bf16 v[100:103], v[212:215], v[20:23], v[100:103]\n"
    "v_cvt_pk_bf16_f32 v174, v180, v181\n"
    "v_cvt_pk_bf16_f32 v175, v182, v183\n"
    "v_mfma_f32_16x16x32_bf16 v[100:103], v[208:211], v[4:7], v[100:103]\n"
    "ds_write_b128 v246, v[88:91] offset:40960\n"
    "buffer_store_dwordx4 v[172:175], v232, s[36:39], %15 offen offset:1024 sc1\n"
    "v_mfma_f32_16x16x32_bf16 v[100:103], v[204:207], a[244:247], v[100:103]\n"
    "v_mfma_f32_16x16x32_bf16 v[112:115], v[204:207], a[248:251], v[112:115]\n"
    "ds_write_b128 v246, v[92:95] offset:41984\n"
    "v_mfma_f32_16x16x32_bf16 v[112:115], v[208:211], v[8:11], v[112:115]\n"
    "v_mfma_f32_16x16x32_bf16 v[112:115], v[212:215], v[24:27], v[112:115]\n"
    "v_mfma_f32_16x16x32_bf16 v[116:119], v[212:215], v[28:31], v[116:119]\n"
    "v_mfma_f32_16x16x32_bf16 v[116:119], v[208:211], v[12:15], v[116:119]\n"
    "v_mfma_f32_16x16x32_bf16 v[116:119], v[204:207], a[252:255], v[116:119]\n"
    "s_waitcnt vmcnt(17) lgkmcnt(0)\n"
    "s_barrier\n"
    "v_mfma_f32_16x16x32_bf16 v[128:131], v[192:195], a[0:3], v[128:131]\n"
    "ds_read_b128 v[204:207], v233 offset:6336\n"
    "v_mfma_f32_16x16x32_bf16 v[128:131], v[196:199], a[16:19], v[128:131]\n"
    "ds_read_b128 v[208:211], v233 offset:8448\n"
    "v_mfma_f32_16x16x32_bf16 v[128:131], v[200:203], a[32:35], v[128:131]\n"
    "ds_read_b128 v[212:215], v233 offset:10560\n"
    "v_mfma_f32_16x16x32_bf16 v[132:135], v[200:203], a[36:39], v[132:135]\n"
    "ds_write_b128 v246, v[112:115] offset:49152\n"
    "ds_read_b128 v[40:43], v247 offset:16384\n"
    "v_mfma_f32_16x16x32_bf16 v[132:135], v[196:199], a[20:23], v[132:135]\n"
    "ds_read_b128 v[56:59], v247 offset:24576\n"
    "v_mfma_f32_16x16x32_bf16 v[132:135], v[192:195], a[4:7], v[132:135]\n"
    "ds_read_b128 v[44:47], v247 offset:17408\n"
    "v_mfma_f32_16x16x32_bf16 v[136:139], v[192:195], a[8:11], 0\n"
    "ds_write_b128 v246, v[116:119] offset:50176\n"
    "ds_read_b128 v[60:63], v247 offset:25600\n"
    "v_mfma_f32_16x16x32_bf16 v[136:139], v[196:199], a[24:27], v[136:139]\n"
    "ds_read_b128 v[72:75], v247 offset:32768\n"
    "v_mfma_f32_16x16x32_bf16 v[136:139], v[200:203], a[40:43], v[136:139]\n"
    "ds_read_b128 v[88:91], v247 offset:40960\n"
    "v_mfma_f32_16x16x32_bf16 v[140:143], v[200:203], a[44:47], 0\n"
    "ds_read_b128 v[76:79], v247 offset:33792\n"
    "v_mfma_f32_16x16x32_bf16 v[140:143], v[196:199], a[28:31], v[140:143]\n"
    "ds_read_b128 v[92:95], v247 offset:41984\n"
    "v_mfma_f32_16x16x32_bf16 v[140:143], v[192:195], a[12:15], v[140:143]\n"
    "s_waitcnt vmcnt(9) lgkmcnt(5)\n"
    "s_barrier\n"
    "v_mfma_f32_16x16x32_bf16 v[144:147], v[204:207], a[0:3], v[144:147]\n"
    "ds_read_b128 v[192:195], v233 offset:12672\n"
    "v_mfma_f32_16x16x32_bf16 v[144:147], v[208:211], a[16:19], v[144:147]\n"
    "ds_read_b128 v[196:199], v233 offset:14784\n"
    "v_mfma_f32_16x16x32_bf16 v[144:147], v[212:215], a[32:35], v[144:147]\n"
    "ds_read_b128 v[200:203], v233 offset:16896\n"
    "v_mfma_f32_16x16x32_bf16 v[148:151], v[212:215], a[36:39], v[148:151]\n"
    "ds_read_b128 v[112:115], v247 offset:49152\n"
    "v_mfma_f32_16x16x32_bf16 v[148:151], v[208:211], a[20:23], v[148:151]\n"
    "ds_read_b128 v[116:119], v247 offset:50176\n"
    "v_mfma_f32_16x16x32_bf16 v[148:151], v[204:207], a[4:7], v[148:151]\n"
    "v_mfma_f32_16x16x32_bf16 v[152:155], v[204:207], a[8:11], 0\n"
    "v_add_f32 v32, v32, v40\n"
    "v_mfma_f32_16x16x32_bf16 v[152:155], v[208:211], a[24:27], v[152:155]\n"
    "v_add_f32 v33, v33, v41\n"
    "v_add_f32 v34, v34, v42\n"
    "v_mfma_f32_16x16x32_bf16 v[152:155], v[212:215], a[40:43], v[152:155]\n"
    "v_add_f32 v35, v35, v43\n"
    "v_mfma_f32_16x16x32_bf16 v[156:159], v[212:215], a[44:47], 0\n"
    "v_add_f32 v48, v48, v56\n"
    "v_add_f32 v49, v49, v57\n"
    "v_mfma_f32_16x16x32_bf16 v[156:159], v[208:211], a[28:31], v[156:159]\n"
    "v_add_f32 v50, v50, v58\n"
    "v_mfma_f32_16x16x32_bf16 v[156:159], v[204:207], a[12:15], v[156:159]\n"
    "s_waitcnt lgkmcnt(2)\n"
    "v_add_f32 v51, v51, v59\n"
    "v_cvt_pk_bf16_f32 v40, v32, v33\n"
    "v_mfma_f32_16x16x32_bf16 v[128:131], v[192:195], a[48:51], v[128:131]\n"
    "ds_read_b128 v[204:207], v233 offset:19008\n"
    "v_cvt_pk_bf16_f32 v41, v34, v35\n"
    "v_mfma_f32_16x16x32_bf16 v[128:131], v[196:199], a[64:67], v[128:131]\n"
    "ds_read_b128 v[208:211], v233 offset:21120\n"
    "v_cvt_pk_bf16_f32 v42, v48, v49\n"
    "v_mfma_f32_16x16x32_bf16 v[128:131], v[200:203], a[80:83], v[128:131]\n"
    "ds_read_b128 v[212:215], v233 offset:23232\n"
    "v_cvt_pk_bf16_f32 v43, v50, v51\n"
    "v_mfma_f32_16x16x32_bf16 v[132:135], v[200:203], a[84:87], v[132:135]\n"
    "buffer_store_dwordx4 v[40:43], v232, s[36:39], %16 offen offset:0 sc1\n"
    "v_mfma_f32_16x16x32_bf16 v[132:135], v[196:199], a[68:71], v[132:135]\n"
    "v_add_f32 v36, v36, v44\n"
    "v_add_f32 v37, v37, v45\n"
    "v_mfma_f32_16x16x32_bf16 v[132:135], v[192:195], a[52:55], v[132:135]\n"
    "v_add_f32 v38, v38, v46\n"
    "v_add_f32 v39, v39, v47\n"
    "v_mfma_f32_16x16x32_bf16 v[136:139], v[192:195], a[56:59], v[136:139]\n"
    "v_add_f32 v52, v52, v60\n"
    "v_add_f32 v53, v53, v61\n"
    "v_mfma_f32_16x16x32_bf16 v[136:139], v[196:199], a[72:75], v[136:139]\n"
    "v_add_f32 v54, v54, v62\n"
    "v_add_f32 v55, v55, v63\n"
    "v_mfma_f32_16x16x32_bf16 v[136:139], v[200:203], a[88:91], v[136:139]\n"
    "v_cvt_pk_bf16_f32 v44, v36, v37\n"
    "v_cvt_pk_bf16_f32 v45, v38, v39\n"
    "v_mfma_f32_16x16x32_bf16 v[140:143], v[200:203], a[92:95], v[140:143]\n"
    "v_cvt_pk_bf16_f32 v46, v52, v53\n"
    "v_cvt_pk_bf16_f32 v47, v54, v55\n"
    "v_mfma_f32_16x16x32_bf16 v[140:143], v[196:199], a[76:79], v[140:143]\n"
    "buffer_store_dwordx4 v[44:47], v232, s[36:39], %16 offen offset:1024 sc1\n"
    "v_mfma_f32_16x16x32_bf16 v[140:143], v[192:195], a[60:63], v[140:143]\n"
    "s_waitcnt lgkmcnt(0)\n"
    "v_add_f32 v64, v64, v72\n"
    "v_mfma_f32_16x16x32_bf16 v[144:147], v[204:207], a[48:51], v[144:147]\n"
    "ds_read_b128 v[192:195], v233 offset:25344\n"
    "v_add_f32 v65, v65, v73\n"
    "v_mfma_f32_16x16x32_bf16 v[144:147], v[208:211], a[64:67], v[144:147]\n"
    "ds_read_b128 v[196:199], v233 offset:27456\n"
    "v_add_f32 v66, v66, v74\n"
    "v_mfma_f32_16x16x32_bf16 v[144:147], v[212:215], a[80:83], v[144:147]\n"
    "ds_read_b128 v[200:203], v233 offset:29568\n"
    "v_add_f32 v67, v67, v75\n"
    "v_mfma_f32_16x16x32_bf16 v[148:151], v[212:215], a[84:87], v[148:151]\n"
    "v_add_f32 v80, v80, v88\n"
    "v_add_f32 v81, v81, v89\n"
    "v_mfma_f32_16x16x32_bf16 v[148:151], v[208:211], a[68:71], v[148:151]\n"
    "v_add_f32 v82, v82, v90\n"
    "v_add_f32 v83, v83, v91\n"
    "v_mfma_f32_16x16x32_bf16 v[148:151], v[204:207], a[52:55], v[148:151]\n"
    "v_cvt_pk_bf16_f32 v72, v64, v65\n"
    "v_cvt_pk_bf16_f32 v73, v66, v67\n"
    "v_mfma_f32_16x16x32_bf16 v[152:155], v[204:207], a[56:59], v[152:155]\n"
    "v_cvt_pk_bf16_f32 v74, v80, v81\n"
    "v_cvt_pk_bf16_f32 v75, v82, v83\n"
    "v_mfma_f32_16x16x32_bf16 v[152:155], v[208:211], a[72:75], v[152:155]\n"
    "buffer_store_dwordx4 v[72:75], v232, s[36:39], %17 offen offset:0 sc1\n"
    "v_mfma_f32_16x16x32_bf16 v[152:155], v[212:215], a[88:91], v[152:155]\n"
    "v_add_f32 v68, v68, v76\n"
    "v_add_f32 v69, v69, v77\n"
    "v_mfma_f32_16x16x32_bf16 v[156:159], v[212:215], a[92:95], v[156:159]\n"
    "v_add_f32 v70, v70, v78\n"
    "v_add_f32 v71, v71, v79\n"
    "v_mfma_f32_16x16x32_bf16 v[156:159], v[208:211], a[76:79], v[156:159]\n"
    "v_add_f32 v84, v84, v92\n"
    "v_mfma_f32_16x16x32_bf16 v[156:159], v[204:207], a[60:63], v[156:159]\n"
    "s_waitcnt lgkmcnt(0)\n"
    "v_add_f32 v85, v85, v93\n"
    "v_add_f32 v86, v86, v94\n"
    "v_mfma_f32_16x16x32_bf16 v[128:131], v[192:195], a[96:99], v[128:131]\n"
    "ds_read_b128 v[204:207], v233 offset:31680\n"
    "v_add_f32 v87, v87, v95\n"
    "v_mfma_f32_16x16x32_bf16 v[128:131], v[196:199], a[112:115], v[128:131]\n"
    "ds_read_b128 v[208:211], v233 offset:33792\n"
    "v_cvt_pk_bf16_f32 v76, v68, v69\n"
    "v_mfma_f32_16x16x32_bf16 v[128:131], v[200:203], a[128:131], v[128:131]\n"
    "ds_read_b128 v[212:215], v233 offset:35904\n"
    "v_cvt_pk_bf16_f32 v77, v70, v71\n"
    "v_mfma_f32_16x16x32_bf16 v[132:135], v[200:203], a[132:135], v[132:135]\n"
    "v_cvt_pk_bf16_f32 v78, v84, v85\n"
    "v_cvt_pk_bf16_f32 v79, v86, v87\n"
    "v_mfma_f32_16x16x32_bf16 v[132:135], v[196:199], a[116:119], v[132:135]\n"
    "buffer_store_dwordx4 v[76:79], v232, s[36:39], %17 offen offset:1024 sc1\n"
    "v_mfma_f32_16x16x32_bf16 v[132:135], v[192:195], a[100:103], v[132:135]\n"
    "v_add_f32 v96, v96, v112\n"
    "v_add_f32 v97, v97, v113\n"
    "v_mfma_f32_16x16x32_bf16 v[136:139], v[192:195], a[104:107], v[136:139]\n"
    "v_add_f32 v98, v98, v114\n"
    "v_add_f32 v99, v99, v115\n"
    "v_mfma_f32_16x16x32_bf16 v[136:139], v[196:199], a[120:123], v[136:139]\n"
    "v_add_f32 v100, v100, v116\n"
    "v_add_f32 v101, v101, v117\n"
    "v_mfma_f32_16x16x32_bf16 v[136:139], v[200:203], a[136:139], v[136:139]\n"
    "v_add_f32 v102, v102, v118\n"
    "v_add_f32 v103, v103, v119\n"
    "v_mfma_f32_16x16x32_bf16 v[140:143], v[200:203], a[140:143], v[140:143]\n"
    "v_cvt_pk_bf16_f32 v112, v96, v97\n"
    "v_cvt_pk_bf16_f32 v113, v98, v99\n"
    "v_mfma_f32_16x16x32_bf16 v[140:143], v[196:199], a[124:127], v[140:143]\n"
    "v_cvt_pk_bf16_f32 v114, v100, v101\n"
    "v_cvt_pk_bf16_f32 v115, v102, v103\n"
    "v_mfma_f32_16x16x32_bf16 v[140:143], v[192:195], a[108:111], v[140:143]\n"
    "s_waitcnt lgkmcnt(0)\n"
    "buffer_store_dwordx4 v[112:115], v232, s[36:39], %18 offen sc1\n"
    "v_mfma_f32_16x16x32_bf16 v[144:147], v[204:207], a[96:99], v[144:147]\n"
    "ds_read_b128 v[192:195], v233 offset:38016\n"
    "v_mfma_f32_16x16x32_bf16 v[144:147], v[208:211], a[112:115], v[144:147]\n"
    "ds_read_b128 v[196:199], v233 offset:40128\n"
    "v_mfma_f32_16x16x32_bf16 v[144:147], v[212:215], a[128:131], v[144:147]\n"
    "ds_read_b128 v[200:203], v233 offset:42240\n"
    "v_mfma_f32_16x16x32_bf16 v[148:151], v[212:215], a[132:135], v[148:151]\n"
    "v_mfma_f32_16x16x32_bf16 v[148:151], v[208:211], a[116:119], v[148:151]\n"
    "v_mfma_f32_16x16x32_bf16 v[148:151], v[204:207], a[100:103], v[148:151]\n"
    "v_mfma_f32_16x16x32_bf16 v[152:155], v[204:207], a[104:107], v[152:155]\n"
    "v_mfma_f32_16x16x32_bf16 v[152:155], v[208:211], a[120:123], v[152:155]\n"
    "v_mfma_f32_16x16x32_bf16 v[152:155], v[212:215], a[136:139], v[152:155]\n"
    "v_mfma_f32_16x16x32_bf16 v[156:159], v[212:215], a[140:143], v[156:159]\n"
    "v_mfma_f32_16x16x32_bf16 v[156:159], v[208:211], a[124:127], v[156:159]\n"
    "v_mfma_f32_16x16x32_bf16 v[156:159], v[204:207], a[108:111], v[156:159]\n"
    "s_waitcnt vmcnt(6)\n"
    "s_barrier\n"
    "s_waitcnt lgkmcnt(0)\n"
    "v_mfma_f32_16x16x32_bf16 v[128:131], v[192:195], a[144:147], v[128:131]\n"
    "ds_read_b128 v[204:207], v233 offset:44352\n"
    "v_mfma_f32_16x16x32_bf16 v[128:131], v[196:199], a[160:163], v[128:131]\n"
    "ds_read_b128 v[208:211], v233 offset:46464\n"
    "v_mfma_f32_16x16x32_bf16 v[128:131], v[200:203], a[176:179], v[128:131]\n"
    "ds_read_b128 v[212:215], v233 offset:48576\n"
    "v_mfma_f32_16x16x32_bf16 v[132:135], v[200:203], a[180:183], v[132:135]\n"
    "v_mfma_f32_16x16x32_bf16 v[132:135], v[196:199], a[164:167], v[132:135]\n"
    "v_mfma_f32_16x16x32_bf16 v[132:135], v[192:195], a[148:151], v[132:135]\n"
    "v_mfma_f32_16x16x32_bf16 v[136:139], v[192:195], a[152:155], v[136:139]\n"
    "v_mfma_f32_16x16x32_bf16 v[136:139], v[196:199], a[168:171], v[136:139]\n"
    "v_mfma_f32_16x16x32_bf16 v[136:139], v[200:203], a[184:187], v[136:139]\n"
    "v_mfma_f32_16x16x32_bf16 v[140:143], v[200:203], a[188:191], v[140:143]\n"
    "v_mfma_f32_16x16x32_bf16 v[140:143], v[196:199], a[172:175], v[140:143]\n"
    "v_mfma_f32_16x16x32_bf16 v[140:143], v[192:195], a[156:159], v[140:143]\n"
    "s_waitcnt lgkmcnt(0)\n"
    "v_mfma_f32_16x16x32_bf16 v[144:147], v[204:207], a[144:147], v[144:147]\n"
    "ds_read_b128 v[192:195], v233 offset:50688\n"
    "v_mfma_f32_16x16x32_bf16 v[144:147], v[208:211], a[160:163], v[144:147]\n"
    "ds_read_b128 v[196:199], v233 offset:52800\n"
    "v_mfma_f32_16x16x32_bf16 v[144:147], v[212:215], a[176:179], v[144:147]\n"
    "ds_read_b128 v[200:203], v233 offset:54912\n"
    "v_mfma_f32_16x16x32_bf16 v[148:151], v[212:215], a[180:183], v[148:151]\n"
    "v_mfma_f32_16x16x32_bf16 v[148:151], v[208:211], a[164:167], v[148:151]\n"
    "v_mfma_f32_16x16x32_bf16 v[148:151], v[204:207], a[148:151], v[148:151]\n"
    "v_mfma_f32_16x16x32_bf16 v[152:155], v[204:207], a[152:155], v[152:155]\n"
    "v_mfma_f32_16x16x32_bf16 v[152:155], v[208:211], a[168:171], v[152:155]\n"
    "v_mfma_f32_16x16x32_bf16 v[152:155], v[212:215], a[184:187], v[152:155]\n"
    "v_mfma_f32_16x16x32_bf16 v[156:159], v[212:215], a[188:191], v[156:159]\n"
    "v_mfma_f32_16x16x32_bf16 v[156:159], v[208:211], a[172:175], v[156:159]\n"
    "v_mfma_f32_16x16x32_bf16 v[156:159], v[204:207], a[156:159], v[156:159]\n"
    "s_waitcnt lgkmcnt(0)\n"
    "v_mfma_f32_16x16x32_bf16 v[128:131], v[192:195], a[192:195], v[128:131]\n"
    "ds_read_b128 v[204:207], v233 offset:57024\n"
    "v_mfma_f32_16x16x32_bf16 v[128:131], v[196:199], a[208:211], v[128:131]\n"
    "ds_read_b128 v[208:211], v233 offset:59136\n"
    "v_mfma_f32_16x16x32_bf16 v[128:131], v[200:203], a[224:227], v[128:131]\n"
    "ds_read_b128 v[212:215], v233 offset:61248\n"
    "v_mfma_f32_16x16x32_bf16 v[132:135], v[200:203], a[228:231], v[132:135]\n"
    "v_mfma_f32_16x16x32_bf16 v[132:135], v[196:199], a[212:215], v[132:135]\n"
    "v_mfma_f32_16x16x32_bf16 v[132:135], v[192:195], a[196:199], v[132:135]\n"
    "v_mfma_f32_16x16x32_bf16 v[136:139], v[192:195], a[200:203], v[136:139]\n"
    "v_mfma_f32_16x16x32_bf16 v[136:139], v[196:199], a[216:219], v[136:139]\n"
    "v_mfma_f32_16x16x32_bf16 v[136:139], v[200:203], a[232:235], v[136:139]\n"
    "v_mfma_f32_16x16x32_bf16 v[140:143], v[200:203], a[236:239], v[140:143]\n"
    "v_mfma_f32_16x16x32_bf16 v[140:143], v[196:199], a[220:223], v[140:143]\n"
    "v_mfma_f32_16x16x32_bf16 v[140:143], v[192:195], a[204:207], v[140:143]\n"
    "s_waitcnt lgkmcnt(0)\n"
    "v_mfma_f32_16x16x32_bf16 v[144:147], v[204:207], a[192:195], v[144:147]\n"
    "ds_read_b128 v[192:195], v233 offset:63360\n"
    "v_mfma_f32_16x16x32_bf16 v[144:147], v[208:211], a[208:211], v[144:147]\n"
    "ds_read_b128 v[196:199], v233 offset:65472\n"
    "v_mfma_f32_16x16x32_bf16 v[144:147], v[212:215], a[224:227], v[144:147]\n"
    "ds_read_b128 v[200:203], v234 offset:2048\n"
    "v_mfma_f32_16x16x32_bf16 v[148:151], v[212:215], a[228:231], v[148:151]\n"
    "v_mfma_f32_16x16x32_bf16 v[148:151], v[208:211], a[212:215], v[148:151]\n"
    "v_mfma_f32_16x16x32_bf16 v[148:151], v[204:207], a[196:199], v[148:151]\n"
    "v_mfma_f32_16x16x32_bf16 v[152:155], v[204:207], a[200:203], v[152:155]\n"
    "v_mfma_f32_16x16x32_bf16 v[152:155], v[208:211], a[216:219], v[152:155]\n"
    "v_mfma_f32_16x16x32_bf16 v[152:155], v[212:215], a[232:235], v[152:155]\n"
    "v_mfma_f32_16x16x32_bf16 v[156:159], v[212:215], a[236:239], v[156:159]\n"
    "v_mfma_f32_16x16x32_bf16 v[156:159], v[208:211], a[220:223], v[156:159]\n"
    "v_mfma_f32_16x16x32_bf16 v[156:159], v[204:207], a[204:207], v[156:159]\n"
    "s_waitcnt lgkmcnt(0)\n"
    "v_mfma_f32_16x16x32_bf16 v[128:131], v[192:195], a[240:243], v[128:131]\n"
    "ds_read_b128 v[204:207], v234 offset:4160\n"
    "v_mfma_f32_16x16x32_bf16 v[128:131], v[196:199], v[0:3], v[128:131]\n"
    "ds_read_b128 v[208:211], v234 offset:6272\n"
    "v_mfma_f32_16x16x32_bf16 v[128:131], v[200:203], v[16:19], v[128:131]\n"
    "ds_read_b128 v[212:215], v234 offset:8384\n"
    "v_mfma_f32_16x16x32_bf16 v[132:135], v[200:203], v[20:23], v[132:135]\n"
    "v_mfma_f32_16x16x32_bf16 v[132:135], v[196:199], v[4:7], v[132:135]\n"
    "v_mfma_f32_16x16x32_bf16 v[132:135], v[192:195], a[244:247], v[132:135]\n"
    "v_mfma_f32_16x16x32_bf16 v[136:139], v[192:195], a[248:251], v[136:139]\n"
    "v_mfma_f32_16x16x32_bf16 v[136:139], v[196:199], v[8:11], v[136:139]\n"
    "v_mfma_f32_16x16x32_bf16 v[136:139], v[200:203], v[24:27], v[136:139]\n"
    "v_mfma_f32_16x16x32_bf16 v[140:143], v[200:203], v[28:31], v[140:143]\n"
    "v_mfma_f32_16x16x32_bf16 v[140:143], v[196:199], v[12:15], v[140:143]\n"
    "v_mfma_f32_16x16x32_bf16 v[140:143], v[192:195], a[252:255], v[140:143]\n"
    "s_waitcnt lgkmcnt(0)\n"
    "v_mfma_f32_16x16x32_bf16 v[144:147], v[204:207], a[240:243], v[144:147]\n"
    "v_mfma_f32_16x16x32_bf16 v[144:147], v[208:211], v[0:3], v[144:147]\n"
    "v_mfma_f32_16x16x32_bf16 v[144:147], v[212:215], v[16:19], v[144:147]\n"
    "v_mfma_f32_16x16x32_bf16 v[148:151], v[212:215], v[20:23], v[148:151]\n"
    "v_mfma_f32_16x16x32_bf16 v[148:151], v[208:211], v[4:7], v[148:151]\n"
    "v_mfma_f32_16x16x32_bf16 v[148:151], v[204:207], a[244:247], v[148:151]\n"
    "v_mfma_f32_16x16x32_bf16 v[152:155], v[204:207], a[248:251], v[152:155]\n"
    "v_mfma_f32_16x16x32_bf16 v[152:155], v[208:211], v[8:11], v[152:155]\n"
    "v_mfma_f32_16x16x32_bf16 v[152:155], v[212:215], v[24:27], v[152:155]\n"
    "v_mfma_f32_16x16x32_bf16 v[156:159], v[212:215], v[28:31], v[156:159]\n"
    "v_mfma_f32_16x16x32_bf16 v[156:159], v[208:211], v[12:15], v[156:159]\n"
    "v_mfma_f32_16x16x32_bf16 v[156:159], v[204:207], a[252:255], v[156:159]\n"
    "s_add_u32 s57, %19, 0\n"
    "buffer_load_dwordx4 v[160:163], v232, s[36:39], s57 offen offset:0 sc1\n"
    "buffer_load_dwordx4 v[164:167], v232, s[36:39], s57 offen offset:1024 sc1\n"
    "s_add_u32 s58, %19, 8192\n"
    "buffer_load_dwordx4 v[176:179], v232, s[36:39], s58 offen offset:0 sc1\n"
    "buffer_load_dwordx4 v[180:183], v232, s[36:39], s58 offen offset:1024 sc1\n"
    "s_add_u32 s59, %19, 16384\n"
    "buffer_load_dwordx4 v[32:35], v232, s[36:39], s59 offen offset:0 sc1\n"
    "buffer_load_dwordx4 v[36:39], v232, s[36:39], s59 offen offset:1024 sc1\n"
    "buffer_load_dwordx4 v[48:51], v232, s[36:39], %20 offen offset:0 sc1\n"
    "buffer_load_dwordx4 v[52:55], v232, s[36:39], %20 offen offset:1024 sc1\n"
    "buffer_load_dwordx4 v[56:59], v232, s[36:39], %20 offen offset:2048 sc1\n"
    "buffer_load_dwordx4 v[60:63], v232, s[36:39], %20 offen offset:3072 sc1\n"
    "ds_write_b128 v246, v[136:139] offset:0\n"
    "ds_write_b128 v246, v[140:143] offset:1024\n"
    "ds_write_b128 v246, v[152:155] offset:8192\n"
    "ds_write_b128 v246, v[156:159] offset:9216\n"
    "s_waitcnt lgkmcnt(0)\n"
    "s_barrier\n"
    "ds_read_b128 v[136:139], v247 offset:0\n"
    "ds_read_b128 v[140:143], v247 offset:1024\n"
    "ds_read_b128 v[152:155], v247 offset:8192\n"
    "ds_read_b128 v[156:159], v247 offset:9216\n"
    "s_movk_i32 s52, 0x2000\n"
    "s_cmp_lg_u32 %26, 0\n"
    "s_cselect_b32 s51, 0, -1\n"
    ".Lchk_%=:\n"
    "s_waitcnt vmcnt(0)\n"
    "v_max_u32 v249, v48, v49\n"
    "v_max_u32 v248, v160, v161\n"
    "v_max3_u32 v249, v249, v50, v51\n"
    "v_max3_u32 v248, v248, v162, v163\n"
    "v_max3_u32 v249, v249, v52, v53\n"
    "v_max3_u32 v248, v248, v176, v177\n"
    "v_max3_u32 v249, v249, v54, v55\n"
    "v_max3_u32 v248, v248, v178, v179\n"
    "v_max3_u32 v249, v249, v56, v57\n"
    "v_max3_u32 v248, v248, v32, v33\n"
    "v_max3_u32 v249, v249, v58, v59\n"
    "v_max3_u32 v248, v248, v34, v35\n"
    "v_max3_u32 v249, v249, v60, v61\n"
    "v_max3_u32 v248, v248, v164, v165\n"
    "v_max3_u32 v249, v249, v62, v63\n"
    "v_max3_u32 v248, v248, v166, v167\n"
    "v_max3_u32 v248, v248, v180, v181\n"
    "v_max3_u32 v248, v248, v182, v183\n"
    "v_max3_u32 v248, v248, v36, v37\n"
    "v_max3_u32 v248, v248, v38, v39\n"
    "v_and_b32 v249, s51, v249\n"
    "v_max_u32 v248, v248, v249\n"
    "v_cmp_eq_u32 vcc, -1, v248\n"
    "s_sub_u32 s52, s52, 1\n"
    "s_cbranch_scc1 .Lok_%=\n"
    "s_nop 3\n"
    "s_cbranch_vccz .Lok_%=\n"
    "s_sleep 4\n"
    "s_add_u32 s57, %19, 0\n"
    "buffer_load_dwordx4 v[160:163], v232, s[36:39], s57 offen offset:0 sc1\n"
    "buffer_load_dwordx4 v[164:167], v232, s[36:39], s57 offen offset:1024 sc1\n"
    "s_add_u32 s58, %19, 8192\n"
    "buffer_load_dwordx4 v[176:179], v232, s[36:39], s58 offen offset:0 sc1\n"
    "buffer_load_dwordx4 v[180:183], v232, s[36:39], s58 offen offset:1024 sc1\n"
    "s_add_u32 s59, %19, 16384\n"
    "buffer_load_dwordx4 v[32:35], v232, s[36:39], s59 offen offset:0 sc1\n"
    "buffer_load_dwordx4 v[36:39], v232, s[36:39], s59 offen offset:1024 sc1\n"
    "buffer_load_dwordx4 v[48:51], v232, s[36:39], %20 offen offset:0 sc1\n"
    "buffer_load_dwordx4 v[52:55], v232, s[36:39], %20 offen offset:1024 sc1\n"
    "buffer_load_dwordx4 v[56:59], v232, s[36:39], %20 offen offset:2048 sc1\n"
    "buffer_load_dwordx4 v[60:63], v232, s[36:39], %20 offen offset:3072 sc1\n"
    "s_branch .Lchk_%=\n"
    ".Lok_%=:\n"
    "s_waitcnt lgkmcnt(0)\n"
    "v_pk_add_f32 v[128:129], v[128:129], v[136:137]\n"
    "v_pk_add_f32 v[130:131], v[130:131], v[138:139]\n"
    "v_pk_add_f32 v[144:145], v[144:145], v[152:153]\n"
    "v_pk_add_f32 v[146:147], v[146:147], v[154:155]\n"
    "v_pk_add_f32 v[132:133], v[132:133], v[140:141]\n"
    "v_pk_add_f32 v[134:135], v[134:135], v[142:143]\n"
    "v_pk_add_f32 v[148:149], v[148:149], v[156:157]\n"
    "v_pk_add_f32 v[150:151], v[150:151], v[158:159]\n"
    "v_dot2_f32_bf16 v128, v160, v216, v128\n"
    "v_dot2_f32_bf16 v129, v160, v217, v129\n"
    "v_dot2_f32_bf16 v130, v161, v216, v130\n"
    "v_dot2_f32_bf16 v131, v161, v217, v131\n"
    "v_dot2_f32_bf16 v144, v162, v216, v144\n"
    "v_dot2_f32_bf16 v145, v162, v217, v145\n"
    "v_dot2_f32_bf16 v146, v163, v216, v146\n"
    "v_dot2_f32_bf16 v147, v163, v217, v147\n"
    "v_dot2_f32_bf16 v128, v176, v216, v128\n"
    "v_dot2_f32_bf16 v129, v176, v217, v129\n"
    "v_dot2_f32_bf16 v130, v177, v216, v130\n"
    "v_dot2_f32_bf16 v131, v177, v217, v131\n"
    "v_dot2_f32_bf16 v144, v178, v216, v144\n"
    "v_dot2_f32_bf16 v145, v178, v217, v145\n"
    "v_dot2_f32_bf16 v146, v179, v216, v146\n"
    "v_dot2_f32_bf16 v147, v179, v217, v147\n"
    "v_dot2_f32_bf16 v128, v32, v216, v128\n"
    "v_dot2_f32_bf16 v129, v32, v217, v129\n"
    "v_dot2_f32_bf16 v130, v33, v216, v130\n"
    "v_dot2_f32_bf16 v131, v33, v217, v131\n"
    "v_dot2_f32_bf16 v144, v34, v216, v144\n"
    "v_dot2_f32_bf16 v145, v34, v217, v145\n"
    "v_dot2_f32_bf16 v146, v35, v216, v146\n"
    "v_dot2_f32_bf16 v147, v35, v217, v147\n"
    "s_add_u32 s44, %24, 0\n"
    "v_cvt_pk_bf16_f32 v136, v128, v129\n"
    "v_cvt_pk_bf16_f32 v137, v130, v131\n"
    "v_cvt_pk_bf16_f32 v138, v144, v145\n"
    "s_nop 0\n"
    "v_cvt_pk_bf16_f32 v139, v146, v147\n"
    "buffer_store_dwordx4 v[136:139], v230, s[40:43], s44 offen\n"
    "v_dot2_f32_bf16 v132, v164, v216, v132\n"
    "v_dot2_f32_bf16 v133, v164, v217, v133\n"
    "v_dot2_f32_bf16 v134, v165, v216, v134\n"
    "v_dot2_f32_bf16 v135, v165, v217, v135\n"
    "v_dot2_f32_bf16 v148, v166, v216, v148\n"
    "v_dot2_f32_bf16 v149, v166, v217, v149\n"
    "v_dot2_f32_bf16 v150, v167, v216, v150\n"
    "v_dot2_f32_bf16 v151, v167, v217, v151\n"
    "v_dot2_f32_bf16 v132, v180, v216, v132\n"
    "v_dot2_f32_bf16 v133, v180, v217, v133\n"
    "v_dot2_f32_bf16 v134, v181, v216, v134\n"
    "v_dot2_f32_bf16 v135, v181, v217, v135\n"
    "v_dot2_f32_bf16 v148, v182, v216, v148\n"
    "v_dot2_f32_bf16 v149, v182, v217, v149\n"
    "v_dot2_f32_bf16 v150, v183, v216, v150\n"
    "v_dot2_f32_bf16 v151, v183, v217, v151\n"
    "v_dot2_f32_bf16 v132, v36, v216, v132\n"
    "v_dot2_f32_bf16 v133, v36, v217, v133\n"
    "v_dot2_f32_bf16 v134, v37, v216, v134\n"
    "v_dot2_f32_bf16 v135, v37, v217, v135\n"
    "v_dot2_f32_bf16 v148, v38, v216, v148\n"
    "v_dot2_f32_bf16 v149, v38, v217, v149\n"
    "v_dot2_f32_bf16 v150, v39, v216, v150\n"
    "v_dot2_f32_bf16 v151, v39, v217, v151\n"
    "s_add_u32 s45, %24, 18432\n"
    "v_cvt_pk_bf16_f32 v140, v132, v133\n"
    "v_cvt_pk_bf16_f32 v141, v134, v135\n"
    "v_cvt_pk_bf16_f32 v142, v148, v149\n"
    "s_nop 0\n"
    "v_cvt_pk_bf16_f32 v143, v150, v151\n"
    "buffer_store_dwordx4 v[140:143], v230, s[40:43], s45 offen\n"
    "s_cmp_lg_u32 %26, 0\n"
    "s_cbranch_scc1 .Lend_%=\n"
    "v_dot2_f32_bf16 v104, v48, v216, v104\n"
    "v_dot2_f32_bf16 v105, v48, v217, v105\n"
    "v_dot2_f32_bf16 v106, v49, v216, v106\n"
    "v_dot2_f32_bf16 v107, v49, v217, v107\n"
    "v_dot2_f32_bf16 v108, v50, v216, v108\n"
    "v_dot2_f32_bf16 v109, v50, v217, v109\n"
    "v_dot2_f32_bf16 v110, v51, v216, v110\n"
    "v_dot2_f32_bf16 v111, v51, v217, v111\n"
    "v_dot2_f32_bf16 v104, v52, v216, v104\n"
    "v_dot2_f32_bf16 v105, v52, v217, v105\n"
    "v_dot2_f32_bf16 v106, v53, v216, v106\n"
    "v_dot2_f32_bf16 v107, v53, v217, v107\n"
    "v_dot2_f32_bf16 v108, v54, v216, v108\n"
    "v_dot2_f32_bf16 v109, v54, v217, v109\n"
    "v_dot2_f32_bf16 v110, v55, v216, v110\n"
    "v_dot2_f32_bf16 v111, v55, v217, v111\n"
    "v_dot2_f32_bf16 v104, v56, v216, v104\n"
    "v_dot2_f32_bf16 v105, v56, v217, v105\n"
    "v_dot2_f32_bf16 v106, v57, v216, v106\n"
    "v_dot2_f32_bf16 v107, v57, v217, v107\n"
    "v_dot2_f32_bf16 v108, v58, v216, v108\n"
    "v_dot2_f32_bf16 v109, v58, v217, v109\n"
    "v_dot2_f32_bf16 v110, v59, v216, v110\n"
    "v_dot2_f32_bf16 v111, v59, v217, v111\n"
    "v_dot2_f32_bf16 v104, v60, v216, v104\n"
    "v_dot2_f32_bf16 v105, v60, v217, v105\n"
    "v_dot2_f32_bf16 v106, v61, v216, v106\n"
    "v_dot2_f32_bf16 v107, v61, v217, v107\n"
    "v_dot2_f32_bf16 v108, v62, v216, v108\n"
    "v_dot2_f32_bf16 v109, v62, v217, v109\n"
    "v_dot2_f32_bf16 v110, v63, v216, v110\n"
    "v_dot2_f32_bf16 v111, v63, v217, v111\n"
    "s_nop 2\n"
    "v_cvt_pk_bf16_f32 v152, v104, v105\n"
    "v_cvt_pk_bf16_f32 v153, v106, v107\n"
    "s_add_u32 s46, %25, 0\n"
    "buffer_store_dwordx2 v[152:153], v231, s[40:43], s46 offen\n"
    "v_cvt_pk_bf16_f32 v156, v108, v109\n"
    "v_cvt_pk_bf16_f32 v157, v110, v111\n"
    "s_add_u32 s47, %25, 18432\n"
    "buffer_store_dwordx2 v[156:157], v231, s[40:43], s47 offen\n"
    ".Lend_%=:\n"
    "s_waitcnt vmcnt(0) lgkmcnt(0)\n"
    "s_branch .Lout_%=\n"
    ".Lps2_%=:\n"
    "s_movk_i32 s52, 0x4000\n"
    ".Lpt2_%=:\n"
    "s_sleep 1\n"
    "buffer_load_dword v252, off, s[36:39], %13 offset:0 sc1\n"
    "s_waitcnt vmcnt(0)\n"
    "v_readfirstlane_b32 s51, v252\n"
    "s_sub_u32 s52, s52, 1\n"
    "s_cbranch_scc1 .Lpr2_%=\n"
    "s_nop 1\n"
    "s_cmp_lt_u32 s51, 8\n"
    "s_cbranch_scc1 .Lpt2_%=\n"
    "s_branch .Lpr2_%=\n"
    ".Lps5_%=:\n"
    "s_movk_i32 s52, 0x4000\n"
    ".Lpt5_%=:\n"
    "s_sleep 1\n"
    "buffer_load_dword v253, off, s[36:39], %13 offset:128 sc1\n"
    "s_waitcnt vmcnt(0)\n"
    "v_readfirstlane_b32 s51, v253\n"
    "s_sub_u32 s52, s52, 1\n"
    "s_cbranch_scc1 .Lpr5_%=\n"
    "s_nop 1\n"
    "s_cmp_lt_u32 s51, 8\n"
    "s_cbranch_scc1 .Lpt5_%=\n"
    "s_branch .Lpr5_%=\n"
    ".Lps8_%=:\n"
    "s_movk_i32 s52, 0x4000\n"
    ".Lpt8_%=:\n"
    "s_sleep 1\n"
    "buffer_load_dword v254, off, s[36:39], %13 offset:256 sc1\n"
    "s_waitcnt vmcnt(0)\n"
    "v_readfirstlane_b32 s51, v254\n"
    "s_sub_u32 s52, s52, 1\n"
    "s_cbranch_scc1 .Lpr8_%=\n"
    "s_nop 1\n"
    "s_cmp_lt_u32 s51, 8\n"
    "s_cbranch_scc1 .Lpt8_%=\n"
    "s_branch .Lpr8_%=\n"
    ".Lps10_%=:\n"
    "s_movk_i32 s52, 0x4000\n"
    ".Lpt10_%=:\n"
    "s_sleep 1\n"
    "buffer_load_dword v255, off, s[36:39], %13 offset:384 sc1\n"
    "s_waitcnt vmcnt(0)\n"
    "v_readfirstlane_b32 s51, v255\n"
    "s_sub_u32 s52, s52, 1\n"
    "s_cbranch_scc1 .Lpr10_%=\n"
    "s_nop 1\n"
    "s_cmp_lt_u32 s51, 8\n"
    "s_cbranch_scc1 .Lpt10_%=\n"
    "s_branch .Lpr10_%=\n"
    ".Lps11_%=:\n"
    "s_movk_i32 s52, 0x4000\n"
    ".Lpt11_%=:\n"
    "s_sleep 1\n"
    "buffer_load_dword v252, off, s[36:39], %13 offset:512 sc1\n"
    "s_waitcnt vmcnt(0)\n"
    "v_readfirstlane_b32 s51, v252\n"
    "s_sub_u32 s52, s52, 1\n"
    "s_cbranch_scc1 .Lpr11_%=\n"
    "s_nop 1\n"
    "s_cmp_lt_u32 s51, 8\n"
    "s_cbranch_scc1 .Lpt11_%=\n"
    "s_branch .Lpr11_%=\n"
    ".Lps12_%=:\n"
    "s_movk_i32 s52, 0x4000\n"
    ".Lpt12_%=:\n"
    "s_sleep 1\n"
    "buffer_load_dword v253, off, s[36:39], %14 offset:0 sc1\n"
    "s_waitcnt vmcnt(0)\n"
    "v_readfirstlane_b32 s51, v253\n"
    "s_sub_u32 s52, s52, 1\n"
    "s_cbranch_scc1 .Lpr12_%=\n"
    "s_nop 1\n"
    "s_cmp_lt_u32 s51, 4\n"
    "s_cbranch_scc1 .Lpt12_%=\n"
    "s_branch .Lpr12_%=\n"
    ".Lout_%=:\n"
    "ffn_code_end:\n"
    :: "s"(ws), "s"(out), "s"(wg), "s"(wu), "s"((1152 * j + 128 * c) * 2 + w * 9216), "s"((u32)OFF_W1T + ct * 36864 + w * 1024),
       "s"((u32)OFF_W2T + (nu * 4 + j) * 331776 + w * 1024), "s"(w), "s"((u32)OFF_H32 + rb0 * 18432), "s"((u32)OFF_SSQ + rb0 * 256),
       "s"((u32)OFF_HH + (rb0 * 144 + j * 36 + (c >> 1) * 9) * 1024 + (c & 1) * 512),
       "s"((u32)OFF_HH + (rbk * 144 + j * 36 + 18 * kh) * 1024), "s"((u32)OFF_HH + (rbs * 144 + j * 36 + 18 * kh) * 1024),
       "s"((u32)OFF_FLAG + (rt * 4 + j) * 1024), "s"((u32)OFF_FLAG + 32 * 1024 + grp * 32),
       "s"(po[0]), "s"(po[1]), "s"(po[2]),
       "s"((u32)OFF_PART8 + (grp + mg) * 4096 + j * 1024), "s"((u32)OFF_PART + (grp + j) * 3 * 8192 + w * 2048), "s"((u32)OFF_PART8 + (grp + j) * 4096),
       "s"((u32)OFF_H32 + (rbk * 18 + 4 * nu + j) * 1024), "s"((u32)OFF_H32 + (rbk * 18 + 16 + (nu >> 1)) * 1024 + (nu & 1) * 512),
       "s"((u32)OFF_MEAN2 + row0 * 4), "s"((row0 * 576 + 144 * nu + 32 * j) * 2), "s"((row0 * 576 + 144 * nu + 128) * 2),
       "s"(mg ^ j), "s"(0u)
    : "v0", "v1", "v2", "v3", "v4", "v5", "v6", "v7", "v8", "v9", "v10", "v11", "v12", "v13", "v14", "v15", "v16", "v17", "v18", "v19", "v20", "v21", "v22", "v23", "v24", "v25", "v26", "v27", "v28", "v29", "v30", "v31", "v32", "v33", "v34", "v35", "v36", "v37", "v38", "v39", "v40", "v41", "v42", "v43", "v44", "v45", "v46", "v47", "v48", "v49", "v50", "v51", "v52", "v53", "v54", "v55", "v56", "v57", "v58", "v59", "v60", "v61", "v62", "v63", "v64", "v65", "v66", "v67", "v68", "v69", "v70", "v71", "v72", "v73", "v74", "v75", "v76", "v77", "v78", "v79", "v80", "v81", "v82", "v83", "v84", "v85", "v86", "v87", "v88", "v89", "v90", "v91", "v92", "v93", "v94", "v95", "v96", "v97", "v98", "v99", "v100", "v101", "v102", "v103", "v104", "v105", "v106", "v107", "v108", "v109", "v110", "v111", "v112", "v113", "v114", "v115", "v116", "v117", "v118", "v119", "v120", "v121", "v122", "v123", "v124", "v125", "v126", "v127", "v128", "v129", "v130", "v131", "v132", "v133", "v134", "v135", "v136", "v137", "v138", "v139", "v140", "v141", "v142", "v143", "v144", "v145", "v146", "v147", "v148", "v149", "v150", "v151", "v152", "v153", "v154", "v155", "v156", "v157", "v158", "v159", "v160", "v161", "v162", "v163", "v164", "v165", "v166", "v167", "v168", "v169", "v170", "v171", "v172", "v173", "v174", "v175", "v176", "v177", "v178", "v179", "v180", "v181", "v182", "v183", "v184", "v185", "v186", "v187", "v188", "v189", "v190", "v191", "v192", "v193", "v194", "v195", "v196", "v197", "v198", "v199", "v200", "v201", "v202", "v203", "v204", "v205", "v206", "v207", "v208", "v209", "v210", "v211", "v212", "v213", "v214", "v215", "v216", "v217", "v218", "v219", "v220", "v221", "v222", "v223", "v224", "v225", "v226", "v227", "v228", "v229", "v230", "v231", "v232", "v233", "v234", "v235", "v236", "v237", "v238", "v239", "v240", "v241", "v242", "v243", "v244", "v245", "v246", "v247", "v248", "v249", "v250", "v251", "v252", "v253", "v254", "v255", "a0", "a1", "a2", "a3", "a4", "a5", "a6", "a7", "a8", "a9", "a10", "a11", "a12", "a13", "a14", "a15", "a16", "a17", "a18", "a19", "a20", "a21", "a22", "a23", "a24", "a25", "a26", "a27", "a28", "a29", "a30", "a31", "a32", "a33", "a34", "a35", "a36", "a37", "a38", "a39", "a40", "a41", "a42", "a43", "a44", "a45", "a46", "a47", "a48", "a49", "a50", "a51", "a52", "a53", "a54", "a55", "a56", "a57", "a58", "a59", "a60", "a61", "a62", "a63", "a64", "a65", "a66", "a67", "a68", "a69", "a70", "a71", "a72", "a73", "a74", "a75", "a76", "a77", "a78", "a79", "a80", "a81", "a82", "a83", "a84", "a85", "a86", "a87", "a88", "a89", "a90", "a91", "a92", "a93", "a94", "a95", "a96", "a97", "a98", "a99", "a100", "a101", "a102", "a103", "a104", "a105", "a106", "a107", "a108", "a109", "a110", "a111", "a112", "a113", "a114", "a115", "a116", "a117", "a118", "a119", "a120", "a121", "a122", "a123", "a124", "a125", "a126", "a127", "a128", "a129", "a130", "a131", "a132", "a133", "a134", "a135", "a136", "a137", "a138", "a139", "a140", "a141", "a142", "a143", "a144", "a145", "a146", "a147", "a148", "a149", "a150", "a151", "a152", "a153", "a154", "a155", "a156", "a157", "a158", "a159", "a160", "a161", "a162", "a163", "a164", "a165", "a166", "a167", "a168", "a169", "a170", "a171", "a172", "a173", "a174", "a175", "a176", "a177", "a178", "a179", "a180", "a181", "a182", "a183", "a184", "a185", "a186", "a187", "a188", "a189", "a190", "a191", "a192", "a193", "a194", "a195", "a196", "a197", "a198", "a199", "a200", "a201", "a202", "a203", "a204", "a205", "a206", "a207", "a208", "a209", "a210", "a211", "a212", "a213", "a214", "a215", "a216", "a217", "a218", "a219", "a220", "a221", "a222", "a223", "a224", "a225", "a226", "a227", "a228", "a229", "a230", "a231", "a232", "a233", "a234", "a235", "a236", "a237", "a238", "a239", "a240", "a241", "a242", "a243", "a244", "a245", "a246", "a247", "a248", "a249", "a250", "a251", "a252", "a253", "a254", "a255", "s35", "s36", "s37", "s38", "s39", "s40", "s41", "s42", "s43", "s44", "s45", "s46", "s47", "s48", "s49", "s50", "s51", "s52", "s53", "s54", "s55", "s56", "s57", "s58", "s59", "s60", "s61", "s62", "s63", "s64", "s65", "s66", "s67", "s68", "s69", "s70", "s71", "s72", "s73", "s74", "s75", "s76", "s77", "s78", "s79", "s80", "s81", "s82", "s83", "s84", "s85", "s86", "s87", "s88", "s89", "s90", "s91", "s92", "s93", "s94", "s95", "s96", "s97", "s98", "s99", "vcc", "scc", "memory");
}


extern "C" void kernel_launch(void* const* d_in, const int* in_sizes, int n_in, void* d_out, int out_size, void* d_ws, size_t ws_size, hipStream_t stream) {
    const u16* x = (const u16*)d_in[0];
    const u16* wqkv = (const u16*)d_in[1];
    const u16* wo = (const u16*)d_in[2];
    const u16* wg = (const u16*)d_in[4];
    const u16* wu = (const u16*)d_in[5];
    const u16* wd = (const u16*)d_in[6];
    char* ws = (char*)d_ws;
    (void)hipFuncSetAttribute((const void*)k1_kernel, hipFuncAttributeMaxDynamicSharedMemorySize, 150 * 1024);
    k1_kernel<<<256, 256, 150 * 1024, stream>>>(x, wqkv, wo, wg, wu, wd, ws);
    (void)hipFuncSetAttribute((const void*)k2_kernel, hipFuncAttributeMaxDynamicSharedMemorySize, 160 * 1024);
    k2_kernel<<<256, 768, 124 * 1024, stream>>>(x, wg, wu, ws);
    (void)hipFuncSetAttribute((const void*)ffn_kernel, hipFuncAttributeMaxDynamicSharedMemorySize, 160 * 1024);
    ffn_kernel<<<256, 256, 160 * 1024, stream>>>(wg, wu, ws, (u16*)d_out);
}
```
